```python
import torch

def _softmax(x, axis=-1):
    return torch.softmax(x, dim=axis)
def _gelu(x):
    return torch.nn.functional.gelu(x, approximate="tanh")
def _silu(x):
    return torch.nn.functional.silu(x)
def _sigmoid(x):
    return torch.sigmoid(x)
def _layernorm(x, axis=-1, eps=1e-5):
    m = x.mean(dim=axis, keepdim=True)
    v = x.var(dim=axis, keepdim=True, unbiased=False)
    return (x - m) / torch.sqrt(v + eps)
def _scatter_add(tgt, idx, src, axis=0):
    return tgt.clone().index_add_(axis, idx, src)
def _segment_sum(src, seg, n):
    out = torch.zeros((n,) + src.shape[1:], dtype=src.dtype)
    return out.index_add_(0, seg, src)
def _pad(x, pw):
    flat = tuple(v for pair in reversed(pw) for v in pair)
    return torch.nn.functional.pad(x, flat)

kernel_name = 'standalone__mha_block_cached__near_ridge'

def setup_inputs():

    _w = (lambda a: (lambda t: t.to(torch.bfloat16) if t.is_floating_point() else t)(torch.as_tensor(a)))
    B, Sq, D = 16, 96, 640
    H, Sk, Dh = 32, 1024, 192
    return {
        "x": _w((lambda *s: torch.randn(*s, dtype=torch.bfloat16) * 0.8)(B, Sq, D)),
        "k_cache": _w((lambda *s: torch.randn(*s, dtype=torch.bfloat16) * 0.8)(B, H, Sk, Dh)),
        "v_cache": _w((lambda *s: torch.randn(*s, dtype=torch.bfloat16) * 0.05)(B, H, Sk, Dh)),
        "wq": _w((lambda *s: torch.randn(*s, dtype=torch.bfloat16) * 0.8)(D, H * Dh) * (D ** -0.5)),
        "wo": _w((lambda *s: torch.randn(*s, dtype=torch.bfloat16) * 0.05)(H * Dh, D) * ((H * Dh) ** -0.5)),
    }

def reference(x, k_cache, v_cache, wq, wo):
    B, Sq, D = x.shape
    H, Dh = 32, 192
    q = (lambda x, *d: x.permute(*d))((x @ wq).reshape(B, Sq, H, Dh), 0, 2, 1, 3)
    scores = torch.einsum("bhqd,bhkd->bhqk", q, k_cache) * (Dh ** -0.5)
    p = _softmax(scores, axis=-1)
    o = torch.einsum("bhqk,bhkd->bhqd", p, v_cache)
    return (lambda x, *d: x.permute(*d))(o, 0, 2, 1, 3).reshape(B, Sq, H * Dh) @ wo
```

```cpp
#include <hip/hip_runtime.h>
#include <hip/hip_bf16.h>

typedef unsigned short u16;
__device__ __forceinline__ float bf2f(u16 v){ return __uint_as_float(((unsigned)v)<<16); }
__device__ __forceinline__ u16 f2bf(float f){ unsigned u=__float_as_uint(f); u += 0x7fffu + ((u>>16)&1u); return (u16)(u>>16); }

constexpr int B=16,SQ=96,D=640,H=32,SK=1024,DH=192;

__global__ void gemm_naive(const u16* A,const u16* Bm,u16* C,int M,int N,int K){
  __shared__ float As[16][17], Bs[16][17];
  int tx=threadIdx.x&15, ty=threadIdx.x>>4;
  int row=blockIdx.y*16+ty, col=blockIdx.x*16+tx;
  float acc=0;
  for(int k0=0;k0<K;k0+=16){
    As[ty][tx]=bf2f(A[(size_t)row*K+k0+tx]);
    Bs[ty][tx]=bf2f(Bm[(size_t)(k0+ty)*N+col]);
    __syncthreads();
    for(int k=0;k<16;++k) acc+=As[ty][k]*Bs[k][tx];
    __syncthreads();
  }
  C[(size_t)row*N+col]=f2bf(acc);
}

__global__ void attn_naive(const u16* Q,const u16* Kc,const u16* Vc,u16* O){
  __shared__ float q[DH]; __shared__ float s[SK]; __shared__ float red[256];
  int qi=blockIdx.x, h=blockIdx.y, b=blockIdx.z, t=threadIdx.x;
  if(t<DH) q[t]=bf2f(Q[((size_t)(b*SQ+qi))*(H*DH)+h*DH+t]);
  __syncthreads();
  const u16* Kp=Kc+((size_t)(b*H+h))*SK*DH; const u16* Vp=Vc+((size_t)(b*H+h))*SK*DH;
  float mx=-1e30f;
  for(int k=t;k<SK;k+=256){ float a=0; for(int d=0;d<DH;++d) a+=q[d]*bf2f(Kp[(size_t)k*DH+d]); a*=0.07216878364870322f; s[k]=a; mx=fmaxf(mx,a);}
  red[t]=mx; __syncthreads();
  for(int o=128;o>0;o>>=1){ if(t<o) red[t]=fmaxf(red[t],red[t+o]); __syncthreads(); }
  mx=red[0]; __syncthreads();
  float sum=0; for(int k=t;k<SK;k+=256){ float e=__expf(s[k]-mx); s[k]=e; sum+=e; }
  red[t]=sum; __syncthreads();
  for(int o=128;o>0;o>>=1){ if(t<o) red[t]+=red[t+o]; __syncthreads(); }
  sum=red[0];
  if(t<DH){ float a=0; for(int k=0;k<SK;++k) a+=s[k]*bf2f(Vp[(size_t)k*DH+t]); O[((size_t)(b*SQ+qi))*(H*DH)+h*DH+t]=f2bf(a/sum);}
}

extern "C" void kernel_launch(void* const* d_in, const int* in_sizes, int n_in,
                              void* d_out, int out_size, void* d_ws, size_t ws_size,
                              hipStream_t stream) {
  const u16* x=(const u16*)d_in[0]; const u16* kc=(const u16*)d_in[1]; const u16* vc=(const u16*)d_in[2];
  const u16* wq=(const u16*)d_in[3]; const u16* wo=(const u16*)d_in[4];
  u16* q=(u16*)d_ws; u16* o=q+(size_t)B*SQ*H*DH;
  gemm_naive<<<dim3(H*DH/16,B*SQ/16),256,0,stream>>>(x,wq,q,B*SQ,H*DH,D);
  attn_naive<<<dim3(SQ,H,B),256,0,stream>>>(q,kc,vc,o);
  gemm_naive<<<dim3(D/16,B*SQ/16),256,0,stream>>>(o,wo,(u16*)d_out,B*SQ,D,H*DH);
}
```

```cpp
#include <hip/hip_runtime.h>
#include <hip/hip_bf16.h>
#include <stdint.h>

typedef unsigned short u16;
typedef short bf16x8 __attribute__((ext_vector_type(8)));
typedef short s16x4 __attribute__((ext_vector_type(4)));
typedef float f32x16 __attribute__((ext_vector_type(16)));
typedef float f32x4 __attribute__((ext_vector_type(4)));
typedef float f32x2 __attribute__((ext_vector_type(2)));
typedef __bf16 bf16x2 __attribute__((ext_vector_type(2)));
typedef unsigned u32x4 __attribute__((ext_vector_type(4)));
typedef unsigned u32x2 __attribute__((ext_vector_type(2)));
#define LAS __attribute__((address_space(3)))
#define DI __device__ __forceinline__

constexpr int NB = 16, SQ = 96, DM = 640, NH = 32, SK = 1024, DH = 192;
constexpr int HD = NH * DH;

#define MFMA32(a, b, c) __builtin_amdgcn_mfma_f32_32x32x16_bf16((a), (b), (c), 0, 0, 0)
#define MFMA16(a, b, c) __builtin_amdgcn_mfma_f32_16x16x32_bf16((a), (b), (c), 0, 0, 0)
#define BAR() asm volatile("s_waitcnt lgkmcnt(0)\n\ts_barrier" ::: "memory")
#define WAITV(n) asm volatile("s_waitcnt vmcnt(" #n ")" ::: "memory")

DI unsigned cvtpk(float lo, float hi) {
  f32x2 v = {lo, hi};
  bf16x2 b = __builtin_convertvector(v, bf16x2);
  return __builtin_bit_cast(unsigned, b);
}
template <bool NT>
DI void dma16(unsigned lds_addr, const void* sbase, unsigned voff) {
  if constexpr (NT)
    asm volatile("s_mov_b32 m0, %0\n\ts_nop 0\n\tglobal_load_lds_dwordx4 %1, %2 nt" ::"s"(lds_addr), "v"(voff), "s"(sbase) : "memory");
  else
    asm volatile("s_mov_b32 m0, %0\n\ts_nop 0\n\tglobal_load_lds_dwordx4 %1, %2" ::"s"(lds_addr), "v"(voff), "s"(sbase) : "memory");
}
DI bf16x8 lds128(unsigned a) { return *(const LAS bf16x8*)(size_t)a; }
DI s16x4 ldstr(unsigned a) { return __builtin_amdgcn_ds_read_tr16_b64_v4i16((LAS s16x4*)(size_t)a); }
DI bf16x8 cat(s16x4 a, s16x4 b) { return __builtin_shufflevector(a, b, 0, 1, 2, 3, 4, 5, 6, 7); }

constexpr int NS = 4;
constexpr unsigned PHB = 24576;
constexpr unsigned STG = NS * PHB;
constexpr unsigned STGS = 18432;
constexpr unsigned MISC = STG + 3 * STGS;
constexpr unsigned LDS_A = MISC + 4096;
constexpr float CSC = 0.07216878364870322f * 1.4426950408889634f;
constexpr float THR_RAW = 8.0f / CSC;

__global__ __launch_bounds__(512, 2) void attn_kernel(const u16* __restrict__ x, const u16* __restrict__ kc,
                                                      const u16* __restrict__ vc, const u16* __restrict__ wq,
                                                      u16* __restrict__ O, unsigned long long* __restrict__ stamps) {
  extern __shared__ __attribute__((aligned(16))) char lds[];
  const unsigned L0 = (unsigned)(size_t)(LAS char*)lds;
  const int tid = threadIdx.x, lane = tid & 63;
  const int wave = __builtin_amdgcn_readfirstlane(tid >> 6);
  const int xj = blockIdx.x & 7, xi = blockIdx.x >> 3;
  const int h = 4 * xj + (xi & 3), b0 = xi >> 2;

  if (wave == 6) {
    unsigned offK[6], offV[3];
#pragma unroll
    for (int j = 0; j < 6; ++j) {
      int n = 64 * j + lane, row = n / 24, pc = n % 24;
      offK[j] = row * 384 + ((pc ^ ((row >> 1) & 7)) * 16);
    }
#pragma unroll
    for (int j = 0; j < 3; ++j) {
      int n = 64 * j + lane, row = n / 24, pc = n % 24;
      offV[j] = row * 384 + ((pc ^ (((row >> 1) & 1) << 2)) * 16);
    }
    auto issue = [&](int gp) {
      const int u = gp >> 5, p = gp & 31, t = p >> 1;
      const size_t ub = ((size_t)((b0 + 8 * u) * NH + h) * SK + 64 * t) * 384;
      const unsigned dst = L0 + (gp & (NS - 1)) * PHB;
      if (p & 1) {
        const char* src = (const char*)vc + ub;
#pragma unroll
        for (int j = 0; j < 24; ++j) dma16<true>(dst + j * 1024, src + (j / 3) * 3072, offV[j % 3]);
      } else {
        const char* src = (const char*)kc + ub;
#pragma unroll
        for (int j = 0; j < 24; ++j) dma16<true>(dst + j * 1024, src + (j / 6) * 6144, offK[j % 6]);
      }
    };
    issue(0);
    issue(1);
    for (int u = 0; u < 2; ++u) {
      for (int s = 0; s < 20; ++s) {
        BAR();
        if (u == 0 && s == 8) { WAITV(24); issue(2); }
        if (u == 0 && s == 12) { WAITV(24); issue(3); }
      }
      BAR();
      BAR();
      for (int p = 0; p < 32; ++p) {
        const int gp = 32 * u + p;
        if (gp < 62) WAITV(48);
        else if (gp == 62) WAITV(24);
        else WAITV(0);
        BAR();
        if (p >= 1 && gp + NS - 1 < 64) issue(gp + NS - 1);
      }
      BAR();
      if (u == 0) issue(31 + NS);
      BAR();
      BAR();
      BAR();
    }
  } else if (wave == 7) {
    unsigned offW[3];
#pragma unroll
    for (int j = 0; j < 3; ++j) {
      int n = 64 * j + lane, row = n / 24, pc = n % 24;
      offW[j] = row * (HD * 2) + ((pc ^ (((row >> 1) & 1) << 2)) * 16);
    }
    const unsigned offX = (lane >> 2) * (DM * 2) + (((lane & 3) ^ ((lane >> 4) & 3)) * 16);
    for (int u = 0; u < 2; ++u) {
      const char* xb = (const char*)x + (size_t)(b0 + 8 * u) * SQ * DM * 2;
      const char* wb = (const char*)wq + h * DH * 2;
      auto issue = [&](int s) {
        const unsigned dst = L0 + STG + (s % 3) * STGS;
        const char* ws = wb + (size_t)s * 32 * HD * 2;
#pragma unroll
        for (int j = 0; j < 12; ++j) dma16<false>(dst + j * 1024, ws + (size_t)(j / 3) * 8 * HD * 2, offW[j % 3]);
        const char* xs = xb + s * 64;
#pragma unroll
        for (int j = 0; j < 6; ++j) dma16<false>(dst + 12288 + j * 1024, xs + j * 16 * DM * 2, offX);
      };
      issue(0);
      issue(1);
      for (int s = 0; s < 20; ++s) {
        if (s < 19) WAITV(18);
        else WAITV(0);
        BAR();
        if (s + 2 < 20) issue(s + 2);
      }
      BAR();
      BAR();
      for (int p = 0; p < 32; ++p) BAR();
      BAR();
      BAR();
      BAR();
      BAR();
    }
  } else {
    const int r = wave % 3, kh = wave / 3;
    const int c32 = lane & 31, hi = lane >> 5;
    const int blk = (lane >> 4) & 1, q4 = (lane & 15) >> 2, p4 = lane & 3, xq = (q4 >> 1) & 1;
    unsigned kof[4];
#pragma unroll
    for (int i = 0; i < 4; ++i) kof[i] = kh * 12288 + c32 * 384 + (((2 * i + hi) ^ ((c32 >> 1) & 7)) * 16);
    const unsigned vE = kh * 12288 + (4 * hi + q4) * 384 + 64 * xq + 32 * blk + 8 * p4;
    const unsigned vO = kh * 12288 + (4 * hi + q4) * 384 - 64 * xq + 32 * blk + 8 * p4;
    const unsigned wA = (8 * hi + q4) * 384 + 64 * (wave ^ xq) + 32 * blk + 8 * p4;
    const unsigned g2 = (c32 >> 2) & 3;
    const unsigned xo0 = 12288 + c32 * 64 + ((hi ^ g2) * 16);
    const unsigned xo1 = 12288 + c32 * 64 + (((2 + hi) ^ g2) * 16);

#define STAMP(i)
    STAMP(0);
    for (int u = 0; u < 2; ++u) {
      const int b = b0 + 8 * u;
      f32x16 qa[3] = {};
      for (int s = 0; s < 20; ++s) {
        BAR();
        const unsigned sb = L0 + STG + (s % 3) * STGS;
#pragma unroll
        for (int s2 = 0; s2 < 2; ++s2) {
          bf16x8 af = cat(ldstr(sb + wA + (16 * s2) * 384), ldstr(sb + wA + (16 * s2 + 4) * 384));
#pragma unroll
          for (int rb = 0; rb < 3; ++rb) {
            bf16x8 bfr = lds128(sb + (s2 ? xo1 : xo0) + 2048 * rb);
            qa[rb] = MFMA32(af, bfr, qa[rb]);
          }
        }
      }
      BAR();
#pragma unroll
      for (int rb = 0; rb < 3; ++rb)
#pragma unroll
        for (int g = 0; g < 4; ++g) {
          u32x2 w = {cvtpk(qa[rb][4 * g], qa[rb][4 * g + 1]), cvtpk(qa[rb][4 * g + 2], qa[rb][4 * g + 3])};
          const int ks = 2 * wave + (g >> 1);
          *(LAS u32x2*)(size_t)(L0 + STG + ((rb * 12 + ks) * 64 + c32 + 32 * (g & 1)) * 16 + 8 * hi) = w;
        }
      BAR();
      STAMP(1 + 4 * u);
      bf16x8 qf[12];
#pragma unroll
      for (int ks = 0; ks < 12; ++ks) qf[ks] = lds128(L0 + STG + ((r * 12 + ks) * 64 + lane) * 16);

      float m = -1e30f, l = 0.f;
      f32x16 o[6] = {};
      for (int t = 0; t < 16; ++t) {
        BAR();
        const unsigned kb = L0 + ((2 * t) & (NS - 1)) * PHB;
        f32x16 sc = {};
#pragma unroll
        for (int ks = 0; ks < 12; ++ks) {
          bf16x8 kf = lds128(kb + kof[ks & 3] + 128 * (ks >> 2));
          sc = MFMA32(kf, qf[ks], sc);
        }
        float pmax = sc[0];
#pragma unroll
        for (int i = 1; i < 16; ++i) pmax = fmaxf(pmax, sc[i]);
        {
          auto rr = __builtin_amdgcn_permlane32_swap(__float_as_uint(pmax), __float_as_uint(pmax), false, false);
          pmax = fmaxf(__uint_as_float(rr[0]), __uint_as_float(rr[1]));
        }
        if (!__all(pmax - m <= THR_RAW)) {
          const float mn = fmaxf(m, pmax);
          const float alpha = __builtin_amdgcn_exp2f((m - mn) * CSC);
          m = mn;
          l *= alpha;
#pragma unroll
          for (int d = 0; d < 6; ++d)
#pragma unroll
            for (int i = 0; i < 16; ++i) o[d][i] *= alpha;
        }
        const float mc = -m * CSC;
#pragma unroll
        for (int i = 0; i < 16; ++i) {
          sc[i] = __builtin_amdgcn_exp2f(fmaf(sc[i], CSC, mc));
          l += sc[i];
        }
        bf16x8 pa[2];
#pragma unroll
        for (int s = 0; s < 2; ++s) {
          u32x4 w = {cvtpk(sc[8 * s], sc[8 * s + 1]), cvtpk(sc[8 * s + 2], sc[8 * s + 3]), cvtpk(sc[8 * s + 4], sc[8 * s + 5]),
                     cvtpk(sc[8 * s + 6], sc[8 * s + 7])};
          pa[s] = __builtin_bit_cast(bf16x8, w);
        }
        BAR();
        const unsigned vb = L0 + ((2 * t + 1) & (NS - 1)) * PHB;
#pragma unroll
        for (int d = 0; d < 6; ++d)
#pragma unroll
          for (int s = 0; s < 2; ++s) {
            const unsigned a = vb + ((d & 1) ? vO : vE) + (16 * s) * 384 + 64 * d;
            bf16x8 vf = cat(ldstr(a), ldstr(a + 8 * 384));
            o[d] = MFMA32(vf, pa[s], o[d]);
          }
      }
      STAMP(2 + 4 * u);
      {
        auto rr = __builtin_amdgcn_permlane32_swap(__float_as_uint(l), __float_as_uint(l), false, false);
        l = __uint_as_float(rr[0]) + __uint_as_float(rr[1]);
      }
      const int pw = wave < 3 ? wave + 3 : wave - 3;
      if (hi == 0) *(LAS f32x2*)(size_t)(L0 + MISC + (wave * 32 + c32) * 8) = f32x2{m, l};
      const unsigned xw = L0 + STG + wave * 6144 + lane * 16, xr = L0 + STG + pw * 6144 + lane * 16;
      float ws_, wo_;
      f32x4 rv[12];
#pragma unroll
      for (int rd = 0; rd < 2; ++rd) {
#pragma unroll
        for (int i = 0; i < 6; ++i) {
          const int e = rd * 6 + i;
          f32x4 v;
          if (kh == 0) v = f32x4{o[3 + (e >> 2)][4 * (e & 3)], o[3 + (e >> 2)][4 * (e & 3) + 1], o[3 + (e >> 2)][4 * (e & 3) + 2], o[3 + (e >> 2)][4 * (e & 3) + 3]};
          else v = f32x4{o[(e >> 2)][4 * (e & 3)], o[(e >> 2)][4 * (e & 3) + 1], o[(e >> 2)][4 * (e & 3) + 2], o[(e >> 2)][4 * (e & 3) + 3]};
          *(LAS f32x4*)(size_t)(xw + i * 1024) = v;
        }
        BAR();
#pragma unroll
        for (int i = 0; i < 6; ++i) rv[rd * 6 + i] = *(const LAS f32x4*)(size_t)(xr + i * 1024);
        if (rd == 0) {
          f32x2 st = *(const LAS f32x2*)(size_t)(L0 + MISC + (pw * 32 + c32) * 8);
          const float M = fmaxf(m, st[0]);
          const float as = __builtin_amdgcn_exp2f((m - M) * CSC), ao = __builtin_amdgcn_exp2f((st[0] - M) * CSC);
          const float inv = 1.0f / (as * l + ao * st[1]);
          ws_ = as * inv;
          wo_ = ao * inv;
        }
        BAR();
      }
      u16* orow = O + (size_t)(b * SQ + 32 * r + c32) * HD + h * DH + 96 * kh + 8 * hi;
#pragma unroll
      for (int i = 0; i < 3; ++i) {
        unsigned pk[8];
#pragma unroll
        for (int g = 0; g < 4; ++g) {
          float v[4];
#pragma unroll
          for (int e = 0; e < 4; ++e) {
            const float own = kh == 0 ? o[i][4 * g + e] : o[3 + i][4 * g + e];
            v[e] = own * ws_ + rv[i * 4 + g][e] * wo_;
          }
          pk[2 * g] = cvtpk(v[0], v[1]);
          pk[2 * g + 1] = cvtpk(v[2], v[3]);
        }
#pragma unroll
        for (int g = 0; g < 4; g += 2) {
          auto r0 = __builtin_amdgcn_permlane32_swap(pk[2 * g], pk[2 * g + 2], false, false);
          auto r1 = __builtin_amdgcn_permlane32_swap(pk[2 * g + 1], pk[2 * g + 3], false, false);
          u32x4 w = {r0[0], r1[0], r0[1], r1[1]};
          *(u32x4*)(orow + 32 * i + 8 * g) = w;
        }
      }
      STAMP(3 + 4 * u);
    }
  }
}

constexpr unsigned OSLOT = 32768;
constexpr int ONS = 4;
constexpr unsigned LDS_B = ONS * OSLOT;
__global__ __launch_bounds__(512, 2) void oproj_kernel(const u16* __restrict__ O, const u16* __restrict__ wo, float* __restrict__ part) {
  extern __shared__ __attribute__((aligned(16))) char lds[];
  const unsigned L0 = (unsigned)(size_t)(LAS char*)lds;
  const int tid = threadIdx.x, lane = tid & 63;
  const int wave = __builtin_amdgcn_readfirstlane(tid >> 6);
  const int bid = blockIdx.x;
  const int ksp = bid & 3, cq = (bid >> 2) & 3, b = bid >> 4;
  unsigned voff[4];
#pragma unroll
  for (int i = 0; i < 4; ++i) {
    const int pc_ = wave + 8 * i;
    if (pc_ < 12) {
      int n = 64 * pc_ + lane, row = n >> 3, pc = n & 7;
      voff[i] = row * (HD * 2) + ((pc ^ ((row >> 1) & 7)) * 16);
    } else {
      int n = 64 * (pc_ - 12) + lane, row = n / 20, pc = n % 20;
      voff[i] = row * (DM * 2) + ((pc ^ (((row >> 3) & 1) << 1)) * 16);
    }
  }
  const char* abase = (const char*)O + ((size_t)b * SQ * HD + ksp * 1536) * 2;
  const char* bbase = (const char*)wo + ((size_t)ksp * 1536 * DM + cq * 160) * 2;
  auto issue = [&](int s) {
    const unsigned dst = L0 + (s % ONS) * OSLOT;
#pragma unroll
    for (int i = 0; i < 4; ++i) {
      const int pc_ = wave + 8 * i;
      const char* src = pc_ < 12 ? abase + s * 128 : bbase + (size_t)s * 64 * DM * 2;
      dma16<false>(dst + pc_ * 1024, src, voff[i]);
    }
  };
  const int kh2 = wave >> 2, rh = (wave >> 1) & 1, ch = wave & 1;
  const int l15 = lane & 15, g = lane >> 4, q4 = l15 >> 2, p4 = lane & 3;
  unsigned aof[3];
#pragma unroll
  for (int rb = 0; rb < 3; ++rb) {
    const int row = 48 * rh + 16 * rb + l15;
    aof[rb] = row * 128 + (((4 * kh2 + g) ^ ((row >> 1) & 7)) * 16);
  }
  const unsigned bE = 12288 + (32 * kh2 + 8 * g + q4) * 320 + 32 * (g & 1) + 8 * p4;
  const unsigned bO = 12288 + (32 * kh2 + 8 * g + q4) * 320 - 32 * (g & 1) + 8 * p4;
  f32x4 acc[3][5] = {};
  issue(0);
  issue(1);
  issue(2);
  for (int s = 0; s < 24; ++s) {
    if (s < 22) WAITV(8);
    else if (s == 22) WAITV(4);
    else WAITV(0);
    BAR();
    if (s + 3 < 24) issue(s + 3);
    const unsigned sb = L0 + (s % ONS) * OSLOT;
    bf16x8 af[3];
#pragma unroll
    for (int rb = 0; rb < 3; ++rb) af[rb] = lds128(sb + aof[rb]);
#pragma unroll
    for (int cb = 0; cb < 5; ++cb) {
      bf16x8 bfr;
      if (ch == 0) {
        const unsigned a = sb + ((cb & 1) ? bO : bE) + 32 * cb;
        bfr = cat(ldstr(a), ldstr(a + 4 * 320));
      } else {
        const unsigned a = sb + (((5 + cb) & 1) ? bO : bE) + 32 * (5 + cb);
        bfr = cat(ldstr(a), ldstr(a + 4 * 320));
      }
#pragma unroll
      for (int rb = 0; rb < 3; ++rb) acc[rb][cb] = MFMA16(af[rb], bfr, acc[rb][cb]);
    }
  }
  BAR();
  const unsigned xb = L0 + (wave & 3) * 15360 + lane * 16;
  if (kh2 == 1) {
#pragma unroll
    for (int rb = 0; rb < 3; ++rb)
#pragma unroll
      for (int cb = 0; cb < 5; ++cb) *(LAS f32x4*)(size_t)(xb + (rb * 5 + cb) * 1024) = acc[rb][cb];
  }
  BAR();
  if (kh2 == 0) {
    float* pp = part + (size_t)ksp * (NB * SQ * DM) + (size_t)(b * SQ + 48 * rh + 4 * g) * DM + cq * 160 + 80 * ch + l15;
#pragma unroll
    for (int rb = 0; rb < 3; ++rb)
#pragma unroll
      for (int cb = 0; cb < 5; ++cb) {
        f32x4 v = *(const LAS f32x4*)(size_t)(xb + (rb * 5 + cb) * 1024);
#pragma unroll
        for (int e = 0; e < 4; ++e) pp[(size_t)(16 * rb + e) * DM + 16 * cb] = acc[rb][cb][e] + v[e];
      }
  }
}

__global__ __launch_bounds__(256) void reduce_kernel(const float* __restrict__ part, u16* __restrict__ out) {
  const int i = (blockIdx.x * 256 + threadIdx.x) * 4;
  constexpr int N = NB * SQ * DM;
  f32x4 a = *(const f32x4*)(part + i);
#pragma unroll
  for (int s = 1; s < 4; ++s) {
    f32x4 v = *(const f32x4*)(part + (size_t)s * N + i);
    a += v;
  }
  u32x2 w = {cvtpk(a[0], a[1]), cvtpk(a[2], a[3])};
  *(u32x2*)(out + i) = w;
}

extern "C" void kernel_launch(void* const* d_in, const int* in_sizes, int n_in, void* d_out, int out_size, void* d_ws,
                              size_t ws_size, hipStream_t stream) {
  const u16* x = (const u16*)d_in[0];
  const u16* kc = (const u16*)d_in[1];
  const u16* vc = (const u16*)d_in[2];
  const u16* wq = (const u16*)d_in[3];
  const u16* wo = (const u16*)d_in[4];
  u16* O = (u16*)d_ws;
  float* part = (float*)((char*)d_ws + (size_t)NB * SQ * HD * 2);
  (void)hipFuncSetAttribute((const void*)attn_kernel, hipFuncAttributeMaxDynamicSharedMemorySize, LDS_A);
  (void)hipFuncSetAttribute((const void*)oproj_kernel, hipFuncAttributeMaxDynamicSharedMemorySize, LDS_B);
  attn_kernel<<<256, 512, LDS_A, stream>>>(x, kc, vc, wq, O, (unsigned long long*)((char*)d_ws + (64u << 20)));
  oproj_kernel<<<256, 512, LDS_B, stream>>>(O, wo, part);
  reduce_kernel<<<NB * SQ * DM / 1024, 256, 0, stream>>>(part, (u16*)d_out);
}
```

```cpp
#include <hip/hip_runtime.h>
#include <hip/hip_bf16.h>
#include <stdint.h>
#include <type_traits>

typedef unsigned short u16;
typedef short bf16x8 __attribute__((ext_vector_type(8)));
typedef short s16x4 __attribute__((ext_vector_type(4)));
typedef float f32x16 __attribute__((ext_vector_type(16)));
typedef float f32x4 __attribute__((ext_vector_type(4)));
typedef float f32x2 __attribute__((ext_vector_type(2)));
typedef __bf16 bf16x2 __attribute__((ext_vector_type(2)));
typedef unsigned u32x4 __attribute__((ext_vector_type(4)));
typedef unsigned u32x2 __attribute__((ext_vector_type(2)));
#define LAS __attribute__((address_space(3)))
#define DI __device__ __forceinline__

constexpr int NB = 16, SQ = 96, DM = 640, NH = 32, SK = 1024, DH = 192;
constexpr int HD = NH * DH;

#define MFMA32(a, b, c) __builtin_amdgcn_mfma_f32_32x32x16_bf16((a), (b), (c), 0, 0, 0)
#define MFMA16(a, b, c) __builtin_amdgcn_mfma_f32_16x16x32_bf16((a), (b), (c), 0, 0, 0)
#define MFMA16A(acc, a, b) asm volatile("v_mfma_f32_16x16x32_bf16 %0, %1, %2, %0" : "+v"(acc) : "v"(a), "v"(b))
#define BAR() do { asm volatile("" ::: "memory"); __builtin_amdgcn_s_waitcnt(0xC07F); __builtin_amdgcn_s_barrier(); asm volatile("" ::: "memory"); } while (0)
#define WAITV(n) asm volatile("s_waitcnt vmcnt(" #n ")" ::: "memory")
#define SBAR() __builtin_amdgcn_sched_barrier(0)

DI unsigned cvtpk(float lo, float hi) {
  f32x2 v = {lo, hi};
  bf16x2 b = __builtin_convertvector(v, bf16x2);
  return __builtin_bit_cast(unsigned, b);
}
template <bool NT>
DI void dma16(unsigned lds_addr, const void* sbase, unsigned voff) {
  if constexpr (NT)
    asm volatile("s_mov_b32 m0, %0\n\ts_nop 0\n\tglobal_load_lds_dwordx4 %1, %2 nt" ::"s"(lds_addr), "v"(voff), "s"(sbase) : "memory");
  else
    asm volatile("s_mov_b32 m0, %0\n\ts_nop 0\n\tglobal_load_lds_dwordx4 %1, %2" ::"s"(lds_addr), "v"(voff), "s"(sbase) : "memory");
}
DI bf16x8 lds128(unsigned a) { return *(const LAS bf16x8*)(size_t)a; }
DI s16x4 ldstr(unsigned a) { return __builtin_amdgcn_ds_read_tr16_b64_v4i16((LAS s16x4*)(size_t)a); }
DI bf16x8 cat(s16x4 a, s16x4 b) { return __builtin_shufflevector(a, b, 0, 1, 2, 3, 4, 5, 6, 7); }

constexpr unsigned ITB = 12288;
constexpr unsigned GRB = 4 * ITB;
constexpr unsigned SIDE = 2 * GRB;
constexpr unsigned XCH = SIDE + 36864;
constexpr unsigned MISC = 159744;
constexpr unsigned CTL = MISC + 2048;
constexpr unsigned C_L = CTL;
constexpr unsigned C_R = CTL + 32;
constexpr unsigned C_S = CTL + 64;
constexpr unsigned C_QRDY = CTL + 72;
constexpr unsigned C_XF = CTL + 96;
constexpr unsigned LDS_A = 163840;
constexpr float CSC = 0.07216878364870322f * 1.4426950408889634f;
constexpr float THR_RAW = 8.0f / CSC;
constexpr int SPIN_MAX = 1 << 20;
#define KVSTART 12

#define STAMP(i)

DI constexpr unsigned headW(int c) { return 98304u + (unsigned)(c & 3) * ITB; }
DI constexpr unsigned headX(int j) { return (j % 3) == 0 ? 147456u : ((j % 3) == 1 ? 36864u : 86016u); }

DI void lds_post(unsigned a, unsigned v) {
  asm volatile("s_waitcnt lgkmcnt(0)\n\tds_write_b32 %0, %1" ::"v"(a), "v"(v) : "memory");
}
DI void lds_wait_ge(unsigned a, unsigned n) {
#pragma nounroll
  for (int it = 0; it < SPIN_MAX; ++it) {
    unsigned v;
    asm volatile("ds_read_b32 %0, %1\n\ts_waitcnt lgkmcnt(0)" : "=v"(v) : "v"(a) : "memory");
    if (__builtin_amdgcn_readfirstlane(v) >= n) break;
    __builtin_amdgcn_s_sleep(1);
  }
}
template <int NW>
DI void lds_wait_min(unsigned a, unsigned n) {
#pragma nounroll
  for (int it = 0; it < SPIN_MAX; ++it) {
    u32x4 d;
    asm volatile("ds_read_b128 %0, %1\n\ts_waitcnt lgkmcnt(0)" : "=v"(d) : "v"(a) : "memory");
    unsigned mn = min(min(d[0], d[1]), d[2]);
    if (NW == 4) mn = min(mn, d[3]);
    if (__builtin_amdgcn_readfirstlane(mn) >= n) break;
    __builtin_amdgcn_s_sleep(1);
  }
}

typedef __attribute__((address_space(1))) unsigned long long gu64;
extern "C" __device__ unsigned long long amd_dispatch_id() __asm("llvm.amdgcn.dispatch.id");
constexpr size_t WS_O = 0, WS_P = 32u << 20, WS_W = 72u << 20, WS_F = 96u << 20;
constexpr unsigned C_O0 = CTL + 160;
constexpr unsigned C_W = CTL + 88;
constexpr unsigned C_S2 = CTL + 144;
constexpr unsigned C_GO = CTL + 152;

DI bool poll_flags(gu64* f, int nl, int skip, unsigned long long T, int lane) {
#pragma nounroll
  for (int it = 0; it < SPIN_MAX; ++it) {
    unsigned long long v = T;
    if (lane < nl && lane != skip) v = __hip_atomic_load(f + lane, __ATOMIC_RELAXED, __HIP_MEMORY_SCOPE_AGENT);
    if (__all(v == T)) {
      if (lane < nl && lane != skip) __hip_atomic_store(f + lane, 0ull, __ATOMIC_RELAXED, __HIP_MEMORY_SCOPE_AGENT);
      return true;
    }
    __builtin_amdgcn_s_sleep(2);
  }
  return false;
}

__global__ __launch_bounds__(512, 2) void attn_kernel(const u16* __restrict__ x, const u16* __restrict__ kc,
                                                      const u16* __restrict__ vc, const u16* __restrict__ wq,
                                                      const u16* __restrict__ wo, u16* __restrict__ out,
                                                      char* __restrict__ ws, unsigned long long* __restrict__ stamps) {
  extern __shared__ __attribute__((aligned(16))) char lds[];
  const unsigned L0 = (unsigned)(size_t)(LAS char*)lds;
  const int tid = threadIdx.x, lane = tid & 63;
  const int wave = __builtin_amdgcn_readfirstlane(tid >> 6);
  const int xj = blockIdx.x & 7, xi = blockIdx.x >> 3;
  const int hq = xi & 3, h = 4 * xj + hq, b0 = xi >> 2;
  const int rot = ((b0 * 5) / 4 + (hq * 5) / 2) % 10;
  const int c32 = lane & 31, hi = lane >> 5;
  const int blk = (lane >> 4) & 1, q4 = (lane & 15) >> 2, p4 = lane & 3, xq = (q4 >> 1) & 1;
  const char* wb = (const char*)wq + h * DH * 2;
  const size_t UB0 = (size_t)(b0 * NH + h) * SK * 384, UB1 = (size_t)((b0 + 8) * NH + h) * SK * 384;
  unsigned long long TAG;
  {
    const unsigned long long did = amd_dispatch_id(), dp = (unsigned long long)__builtin_amdgcn_dispatch_ptr();
    const unsigned t32 = (unsigned)(did * 0x9E3779B1ull) ^ (unsigned)(dp >> 6) * 0x85EBCA77u ^ (unsigned)(did >> 32);
    TAG = ((unsigned long long)(t32 ^ 0x5A17C3E9u) << 32) | t32;
  }
  const int qbase = xj + 32 * b0;
  gu64* FL = (gu64*)(ws + WS_F);

  if (wave >= 6) {
    const int hw = wave - 6;
    unsigned lh;
    asm volatile("v_mbcnt_lo_u32_b32 %0, -1, 0\n\tv_mbcnt_hi_u32_b32 %0, -1, %0" : "=v"(lh));
    if (hw == 0 && lh < 48) *(LAS unsigned*)(size_t)(L0 + CTL + lh * 4) = 0u;
#pragma unroll 1
    for (int c = -1; c < KVSTART; ++c) BAR();
    {
      unsigned offK[6], offV[3];
#pragma unroll
      for (int j = 0; j < 6; ++j) {
        int n = 64 * j + lh, row = n / 24, pc = n % 24;
        offK[j] = row * 384 + ((pc ^ ((row >> 1) & 7)) * 16);
      }
#pragma unroll
      for (int j = 0; j < 3; ++j) {
        int n = 64 * j + lh, row = n / 24, pc = n % 24;
        offV[j] = row * 384 + ((pc ^ (((row >> 1) & 1) << 2)) * 16);
      }
#pragma unroll
      for (int q = 0; q < 6; ++q) {
        const int n = q >> 1, j0 = (q & 1) * 6;
        const size_t ub = UB0 + (size_t)(64 * (n >> 1) + 32 * hw) * 384;
        const unsigned dst = L0 + hw * GRB + n * ITB;
        if (n & 1) {
          const char* src = (const char*)vc + ub;
#pragma unroll
          for (int j = j0; j < j0 + 6; ++j) dma16<true>(dst + j * 1024, src + (j / 3) * 3072, offV[j % 3]);
        } else {
          const char* src = (const char*)kc + ub;
#pragma unroll
          for (int j = j0; j < j0 + 6; ++j) dma16<true>(dst + j * 1024, src + (j / 6) * 6144, offK[j % 6]);
        }
        BAR();
      }
    }
#pragma unroll 1
    for (int c = KVSTART + 6; c < 20; ++c) BAR();
    BAR();
    BAR();
    BAR();
    WAITV(24); lds_post(L0 + C_L + 16 * hw + 12, 1);
    WAITV(12); lds_post(L0 + C_L + 16 * hw + 12, 2);
    WAITV(0);  lds_post(L0 + C_L + 16 * hw + 12, 0xFFFFu);
    {
      unsigned ln;
      asm volatile("v_mbcnt_lo_u32_b32 %0, -1, 0\n\tv_mbcnt_hi_u32_b32 %0, -1, %0" : "=v"(ln));
      const __amdgpu_buffer_rsrc_t rsW = __builtin_amdgcn_make_buffer_rsrc(ws + WS_W, (short)0, 32 * 245760, 0x00020000);
      const unsigned lb = L0 + SIDE + hw * 15360;
#pragma unroll
      for (int q = 0; q < 3; ++q) {
        const char* src = (const char*)wo + ((size_t)(4 * xj * DH + 32 * (3 * b0 + q)) * DM + 160 * hq + 80 * hw) * 2;
#pragma unroll
        for (int j = 0; j < 5; ++j) {
          const int n = 64 * j + ln, row = n / 10, pc = n % 10;
          dma16<false>(lb + q * 5120 + j * 1024, src, row * (DM * 2) + pc * 16);
        }
      }
      WAITV(0);
      const unsigned g = ln >> 4, q4 = (ln & 15) >> 2, p4 = ln & 3;
      const unsigned ra = lb + (8 * g + q4) * 160 + 8 * p4;
#pragma unroll
      for (int q = 0; q < 3; ++q)
#pragma unroll
        for (int cb = 0; cb < 5; ++cb) {
          const bf16x8 f = cat(ldstr(ra + q * 5120 + 32 * cb), ldstr(ra + q * 5120 + 32 * cb + 4 * 160));
          __builtin_amdgcn_raw_buffer_store_b128(__builtin_bit_cast(u32x4, f), rsW,
                                                 (xj * 4 + hq) * 245760 + ((3 * b0 + q) * 10 + 5 * hw + cb) * 1024 + ln * 16, 0, 16);
        }
      WAITV(0);
      lds_post(L0 + C_W + 4 * hw, 1);
      lds_wait_ge(L0 + C_W + 4 * (1 - hw), 1);
      if (hw == 0 && ln < 8) __hip_atomic_store(FL + (size_t)(xj + 8 * (4 * ln + hq)) * 32 + 16 + b0, TAG, __ATOMIC_RELAXED, __HIP_MEMORY_SCOPE_AGENT);
    }
    {
      unsigned ln;
      asm volatile("v_mbcnt_lo_u32_b32 %0, -1, 0\n\tv_mbcnt_hi_u32_b32 %0, -1, %0" : "=v"(ln));
      const unsigned c32 = ln & 31, hi = ln >> 5, blk = (ln >> 4) & 1, q4 = (ln & 15) >> 2, p4 = ln & 3, xq = (q4 >> 1) & 1;
      unsigned offW[6], offX[6], xo[2];
#pragma unroll
      for (int i = 0; i < 6; ++i) {
        int n = 64 * (6 * hw + i) + ln, row = n / 24, pc = n % 24;
        offW[i] = row * (HD * 2) + ((pc ^ (((row >> 1) & 1) << 2)) * 16);
        int rx = 8 * (6 * hw + i) + (ln >> 3);
        offX[i] = rx * (DM * 2) + (((ln & 7) ^ ((rx >> 1) & 7)) * 16);
      }
#pragma unroll
      for (int i = 0; i < 2; ++i) xo[i] = c32 * 128 + (((2 * i + hi) ^ ((c32 >> 1) & 7)) * 16);
      const char* xb1 = (const char*)x + (size_t)(b0 + 8) * SQ * DM * 2;
      const unsigned wA = L0 + SIDE + (8 * hi + q4) * 384 + 32 * blk + 8 * p4 + 192 * hw;
      const unsigned wP = wA + 64 * xq, wM = wA - 64 * xq;
      const unsigned w0 = hw ? wM : wP, w1 = hw ? wP : wM;
      const unsigned aME = L0 + C_S + 4 * hw, aPR = L0 + C_S + 4 * (1 - hw);
      const unsigned dS = L0 + SIDE + hw * 6144;
      auto issW = [&](int c) {
        int kk = c + 2 * rot; kk = kk >= 20 ? kk - 20 : kk;
        const char* src = wb + (size_t)(unsigned)kk * (32 * HD * 2);
#pragma unroll
        for (int i = 0; i < 6; ++i) dma16<false>(dS + (c % 3) * ITB + i * 1024, src, offW[i]);
      };
      auto issX = [&](int j) {
        int kk = j + rot; kk = kk >= 10 ? kk - 10 : kk;
        const char* src = xb1 + (unsigned)kk * 128;
#pragma unroll
        for (int i = 0; i < 6; ++i) dma16<false>(dS + (3 + (j & 1)) * ITB + i * 1024, src, offX[i]);
      };
      f32x16 qa[3][3] = {};
      issX(0); issW(0); issW(1);
#pragma unroll
      for (int c = 0; c < 20; ++c) {
        if (c == 19) WAITV(0);
        else if (c & 1) WAITV(12);
        else WAITV(6);
        lds_post(aME, c + 1);
        lds_wait_ge(aPR, c + 1);
        if (!(c & 1) && (c >> 1) + 1 < 10) issX((c >> 1) + 1);
        if (c + 2 < 20) issW(c + 2);
        bf16x8 bfr[2][3], af[2][3];
#pragma unroll
        for (int s2 = 0; s2 < 2; ++s2) {
          const unsigned xa = L0 + SIDE + (3 + ((c >> 1) & 1)) * ITB + (xo[s2] ^ ((c & 1) * 64));
#pragma unroll
          for (int rb = 0; rb < 3; ++rb) bfr[s2][rb] = lds128(xa + 4096 * rb);
#pragma unroll
          for (int db = 0; db < 3; ++db) {
            const unsigned a = ((db & 1) ? w1 : w0) + (c % 3) * ITB + 64 * db + (16 * s2) * 384;
            af[s2][db] = cat(ldstr(a), ldstr(a + 4 * 384));
          }
        }
        SBAR();
#pragma unroll
        for (int s2 = 0; s2 < 2; ++s2)
#pragma unroll
          for (int db = 0; db < 3; ++db)
#pragma unroll
            for (int rb = 0; rb < 3; ++rb) qa[db][rb] = MFMA32(af[s2][db], bfr[s2][rb], qa[db][rb]);
      }
      lds_post(aME, 21);
      lds_wait_ge(aPR, 21);
      {
        unsigned lz;
        asm volatile("v_mbcnt_lo_u32_b32 %0, -1, 0\n\tv_mbcnt_hi_u32_b32 %0, -1, %0" : "=v"(lz));
        const unsigned qb = L0 + SIDE + (lz & 31) * 16 + (lz >> 5) * 8 + hw * 6144;
#pragma unroll
        for (int db = 0; db < 3; ++db)
#pragma unroll
          for (int rb = 0; rb < 3; ++rb)
#pragma unroll
            for (int g = 0; g < 4; ++g) {
              u32x2 w = {cvtpk(qa[db][rb][4 * g], qa[db][rb][4 * g + 1]), cvtpk(qa[db][rb][4 * g + 2], qa[db][rb][4 * g + 3])};
              const int ks = 2 * db + (g >> 1);
              *(LAS u32x2*)(size_t)(qb + ((rb * 12 + ks) * 64 + 32 * (g & 1)) * 16) = w;
            }
      }
      lds_post(L0 + C_QRDY + 4 * hw, 1);
    }
    {
      unsigned lw;
      asm volatile("v_mbcnt_lo_u32_b32 %0, -1, 0\n\tv_mbcnt_hi_u32_b32 %0, -1, %0" : "=v"(lw));
      const char* p = ws + WS_W + (size_t)(xj * 4 + hq) * 245760 + (size_t)(3 * b0) * 10240 + hw * 15360 + lw * 16;
      u32x4 a = {0, 0, 0, 0};
#pragma unroll
      for (int i = 0; i < 15; ++i) a ^= *(const u32x4*)(p + i * 1024);
      asm volatile("" ::"v"(a));
    }
    {
      unsigned ln;
      asm volatile("v_mbcnt_lo_u32_b32 %0, -1, 0\n\tv_mbcnt_hi_u32_b32 %0, -1, %0" : "=v"(ln));
      lds_wait_min<3>(L0 + C_O0, 1);
      lds_wait_min<3>(L0 + C_O0 + 16, 1);
      if (hw == 0) {
        if (ln < 4 && (int)ln != hq) __hip_atomic_store(FL + (size_t)(qbase + 8 * ln) * 32 + hq, TAG, __ATOMIC_RELAXED, __HIP_MEMORY_SCOPE_AGENT);
        poll_flags(FL + (size_t)blockIdx.x * 32, 4, hq, TAG, ln);
        poll_flags(FL + (size_t)blockIdx.x * 32 + 16, 8, -1, TAG, ln);
        __builtin_amdgcn_fence(__ATOMIC_ACQUIRE, "agent");
        WAITV(0);
        lds_post(L0 + C_GO, 1);
      } else {
        lds_wait_ge(L0 + C_GO, 1);
      }
      const char* og = ws + WS_O;
      const char* wtb = ws + WS_W + (size_t)(xj * 4 + hq) * 245760 + (size_t)(5 * hw) * 1024;
      const unsigned voff = ln * 16;
      const unsigned l15 = ln & 15, g = ln >> 4;
      const unsigned aof0 = l15 * 128 + ((g ^ ((l15 >> 1) & 7)) * 16);
      const unsigned aME = L0 + C_S2 + 4 * hw, aPR = L0 + C_S2 + 4 * (1 - hw);
      auto issA = [&](int c) {
        const int hs = (hq + c / 3) & 3, c3 = c % 3;
        const char* src = og + (size_t)(qbase + 8 * hs) * 36864 + c3 * 12288 + hw * 6144;
#pragma unroll
        for (int i = 0; i < 6; ++i) dma16<false>(L0 + SIDE + (c % 3) * ITB + hw * 6144 + i * 1024, src + i * 1024, voff);
      };
      u32x4 bq[4][5];
      auto issB = [&](int kk, u32x4 (&d)[5]) {
        const int hs = (hq + kk / 6) & 3, k6 = kk % 6;
        const char* src = wtb + (size_t)((hs * 6 + k6) * 10) * 1024;
#pragma unroll
        for (int cb = 0; cb < 5; ++cb)
          asm volatile("global_load_dwordx4 %0, %1, %2 offset:%3" : "=v"(d[cb]) : "v"(voff), "s"(src + 2048), "i"(cb * 1024 - 2048) : "memory");
      };
      f32x4 acc[6][5] = {};
      issA(0); issB(0, bq[0]); issB(1, bq[1]); issA(1); issB(2, bq[2]);
      auto ostep = [&](int kk, auto J, bool last) {
        constexpr int j4 = decltype(J)::value;
        if (!last) WAITV(16);
        else if (j4 <= 0) WAITV(16);
        else if (j4 == 1) WAITV(10);
        else if (j4 == 2) WAITV(5);
        else WAITV(0);
        asm volatile("" : "+v"(bq[j4][0]), "+v"(bq[j4][1]), "+v"(bq[j4][2]), "+v"(bq[j4][3]), "+v"(bq[j4][4]));
        const int c = kk >> 1;
        if (!(j4 & 1)) {
          lds_post(aME, c + 1);
          lds_wait_ge(aPR, c + 1);
          if (c + 2 < 12) issA(c + 2);
        }
        if (!last || j4 == 0) issB(kk + 3, bq[(j4 + 3) & 3]);
        const unsigned ab = (aof0 ^ ((j4 & 1) * 64)) + (L0 + SIDE + (c % 3) * ITB);
#pragma unroll
        for (int hf = 0; hf < 2; ++hf) {
          bf16x8 af[3];
#pragma unroll
          for (int rb = 0; rb < 3; ++rb) af[rb] = lds128(ab + (3 * hf + rb) * 2048);
          SBAR();
#pragma unroll
          for (int cb = 0; cb < 5; ++cb)
#pragma unroll
            for (int rb = 0; rb < 3; ++rb) MFMA16A(acc[3 * hf + rb][cb], bq[j4][cb], af[rb]);
          SBAR();
        }
      };
#pragma unroll 1
      for (int k4 = 0; k4 < 6; ++k4) {
        const bool last = k4 == 5;
        ostep(4 * k4 + 0, std::integral_constant<int, 0>{}, last);
        ostep(4 * k4 + 1, std::integral_constant<int, 1>{}, last);
        ostep(4 * k4 + 2, std::integral_constant<int, 2>{}, last);
        ostep(4 * k4 + 3, std::integral_constant<int, 3>{}, last);
      }
      asm volatile("s_nop 15\n\ts_nop 15" ::: "memory");
      lds_post(aME, 13);
      lds_wait_ge(aPR, 13);
      unsigned l2;
      asm volatile("v_mbcnt_lo_u32_b32 %0, -1, 0\n\tv_mbcnt_hi_u32_b32 %0, -1, %0" : "=v"(l2));
      {
        const unsigned yw = L0 + SIDE + (l2 & 15) * 320 + (80 * hw + 4 * (l2 >> 4)) * 2;
#pragma unroll
        for (int rb = 0; rb < 6; ++rb)
#pragma unroll
          for (int cb = 0; cb < 5; ++cb) {
            u32x2 w = {cvtpk(acc[rb][cb][0], acc[rb][cb][1]), cvtpk(acc[rb][cb][2], acc[rb][cb][3])};
            *(LAS u32x2*)(size_t)(yw + rb * 16 * 320 + cb * 32) = w;
          }
      }
      lds_post(aME, 14);
      lds_wait_ge(aPR, 14);
      {
        const __amdgpu_buffer_rsrc_t rsP = __builtin_amdgcn_make_buffer_rsrc(ws + WS_P, (short)0, 2 * 8 * 4 * 8 * 30720, 0x00020000);
#pragma unroll
        for (int i = 0; i < 15; ++i) {
          const int j = 15 * hw + i;
          const u32x4 v = *(const LAS u32x4*)(size_t)(L0 + SIDE + j * 1024 + l2 * 16);
          __builtin_amdgcn_raw_buffer_store_b128(v, rsP, ((b0 * 4 + hq) * 8 + xj) * 30720 + j * 1024 + l2 * 16, 0, 16);
        }
        WAITV(0);
      }
      lds_post(aME, 15);
      if (hw == 0) {
        lds_wait_ge(aPR, 15);
        if (l2 < 8) __hip_atomic_store(FL + (size_t)((int)l2 + 8 * xi) * 32 + 24 + xj, TAG, __ATOMIC_RELAXED, __HIP_MEMORY_SCOPE_AGENT);
      }
    }
  } else {
    const int r = wave % 3, kh = wave / 3;
    STAMP(0);
    bf16x8 qf[12];
    {
      unsigned offW[2], offX[2];
#pragma unroll
      for (int i = 0; i < 2; ++i) {
        int n = 64 * (2 * wave + i) + lane, row = n / 24, pc = n % 24;
        offW[i] = row * (HD * 2) + ((pc ^ (((row >> 1) & 1) << 2)) * 16);
        int rx = 8 * (2 * wave + i) + (lane >> 3);
        offX[i] = rx * (DM * 2) + (((lane & 7) ^ ((rx >> 1) & 7)) * 16);
      }
      const char* xb0 = (const char*)x + (size_t)b0 * SQ * DM * 2;
      auto srcWf = [&](int c) { int kk = c + 2 * rot; kk = kk >= 20 ? kk - 20 : kk; return wb + (size_t)(unsigned)kk * (32 * HD * 2); };
      auto srcXf = [&](int j) { int kk = j + rot; kk = kk >= 10 ? kk - 10 : kk; return xb0 + (unsigned)kk * 128; };
      const unsigned dW = L0 + wave * 2048;
      auto issW = [&](int c) { const char* src = srcWf(c); dma16<false>(dW + headW(c), src, offW[0]); dma16<false>(dW + headW(c) + 1024, src, offW[1]); };
      auto issX = [&](int j) { const char* src = srcXf(j); dma16<false>(dW + headX(j), src, offX[0]); dma16<false>(dW + headX(j) + 1024, src, offX[1]); };
      issX(0); issW(0); issX(1); issW(1); issW(2);
      const unsigned wAb = L0 + 98304 + (8 * hi + q4) * 384 + 64 * (wave ^ xq) + 32 * blk + 8 * p4;
      unsigned xa_lo[4], xa_hi[4];
#pragma unroll
      for (int i = 0; i < 4; ++i) {
        const unsigned xo = c32 * 128 + (((2 * (i & 1) + hi) ^ ((c32 >> 1) & 7)) * 16);
        xa_lo[i] = L0 + 36864 + (xo ^ ((i >> 1) * 64));
        xa_hi[i] = L0 + 147456 + (xo ^ ((i >> 1) * 64));
      }
      f32x16 qa[3] = {};
      bf16x8 af[2][2], bfr[2][2][3];
#pragma unroll
      for (int c = -1; c < 20; ++c) {
        if (c <= 15) WAITV(6);
        else if (c == 16) WAITV(4);
        else if (c == 17) WAITV(2);
        else WAITV(0);
        BAR();
        const int cur = c & 1, nxt = cur ^ 1;
        if (c < 19) {
          const int c1 = c + 1, j1 = c1 >> 1, xs = j1 % 3;
#pragma unroll
          for (int s2 = 0; s2 < 2; ++s2) {
            af[nxt][s2] = cat(ldstr(wAb + (c1 & 3) * ITB + (16 * s2) * 384), ldstr(wAb + (c1 & 3) * ITB + (16 * s2 + 4) * 384));
            const unsigned xa = (xs == 0 ? xa_hi[s2 + 2 * (c1 & 1)] : xa_lo[s2 + 2 * (c1 & 1)]) + (xs == 2 ? 49152 : 0);
#pragma unroll
            for (int rb = 0; rb < 3; ++rb) bfr[nxt][s2][rb] = lds128(xa + 4096 * rb);
          }
        }
        SBAR();
        const bool dx = (c & 1) && c <= 13, dw = c <= 15, mm = c >= 0;
        const int jx = ((c - 1) >> 1) + 3, cw = c + 4;
        if (mm) qa[0] = MFMA32(af[cur][0], bfr[cur][0][0], qa[0]);
        SBAR();
        if (dx) dma16<false>(dW + headX(jx), srcXf(jx), offX[0]);
        SBAR();
        if (mm) qa[1] = MFMA32(af[cur][0], bfr[cur][0][1], qa[1]);
        SBAR();
        if (dx) dma16<false>(dW + headX(jx) + 1024, srcXf(jx), offX[1]);
        SBAR();
        if (mm) qa[2] = MFMA32(af[cur][0], bfr[cur][0][2], qa[2]);
        SBAR();
        if (dw) dma16<false>(dW + headW(cw), srcWf(cw), offW[0]);
        SBAR();
        if (mm) qa[0] = MFMA32(af[cur][1], bfr[cur][1][0], qa[0]);
        SBAR();
        if (dw) dma16<false>(dW + headW(cw) + 1024, srcWf(cw), offW[1]);
        SBAR();
        if (mm) qa[1] = MFMA32(af[cur][1], bfr[cur][1][1], qa[1]);
        if (mm) qa[2] = MFMA32(af[cur][1], bfr[cur][1][2], qa[2]);
        SBAR();
      }
      BAR();
#pragma unroll
      for (int rb = 0; rb < 3; ++rb)
#pragma unroll
        for (int g = 0; g < 4; ++g) {
          u32x2 w = {cvtpk(qa[rb][4 * g], qa[rb][4 * g + 1]), cvtpk(qa[rb][4 * g + 2], qa[rb][4 * g + 3])};
          const int ks = 2 * wave + (g >> 1);
          *(LAS u32x2*)(size_t)(L0 + SIDE + ((rb * 12 + ks) * 64 + c32 + 32 * (g & 1)) * 16 + 8 * hi) = w;
        }
      BAR();
#pragma unroll
      for (int ks = 0; ks < 12; ++ks) qf[ks] = lds128(L0 + SIDE + ((r * 12 + ks) * 64 + lane) * 16);
      BAR();
    }
    STAMP(1);
    unsigned ln;
    asm volatile("v_mbcnt_lo_u32_b32 %0, -1, 0\n\tv_mbcnt_hi_u32_b32 %0, -1, %0" : "=v"(ln));
    unsigned vK[4], vV[4];
#pragma unroll
    for (int i = 0; i < 4; ++i) {
      int n = 64 * (4 * r + i) + ln, row = n / 24, pc = n % 24;
      vK[i] = row * 384 + ((pc ^ ((row >> 1) & 7)) * 16);
      vV[i] = row * 384 + ((pc ^ (((row >> 1) & 1) << 2)) * 16);
    }
    unsigned kof[4], vE, vO;
    {
      const unsigned a32 = ln & 31, ahi = ln >> 5, ablk = (ln >> 4) & 1, aq4 = (ln & 15) >> 2, ap4 = ln & 3, axq = (aq4 >> 1) & 1;
#pragma unroll
      for (int i = 0; i < 4; ++i) kof[i] = L0 + kh * GRB + a32 * 384 + (((2 * i + ahi) ^ ((a32 >> 1) & 7)) * 16);
      vE = L0 + kh * GRB + (4 * ahi + aq4) * 384 + 64 * axq + 32 * ablk + 8 * ap4;
      vO = L0 + kh * GRB + (4 * ahi + aq4) * 384 - 64 * axq + 32 * ablk + 8 * ap4;
    }
    const unsigned aL = L0 + C_L + 16 * kh, aR = L0 + C_R + 16 * kh;
    const unsigned dKV = L0 + kh * GRB + r * 4096;
    const char* kbase = (const char*)kc + 32 * kh * 384;
    const char* vbase = (const char*)vc + 32 * kh * 384;

    for (int u = 0; u < 2; ++u) {
      float m = -1e30f, l = 0.f;
      f32x16 o[6] = {};
#pragma unroll 1
      for (int t = 0; t < 16; ++t) {
        const int n = 32 * u + 2 * t;
        const unsigned sK = (t & 1) * 2 * ITB, sV = sK + ITB;
        const int t2 = t + 2;
        const bool more = (u == 0) || (t2 < 16);
        const size_t nb = (t2 < 16 ? (u ? UB1 : UB0) + (size_t)t2 * 24576 : UB1 + (size_t)(t2 - 16) * 24576);
        const int t1 = t + 1;
        const size_t nb1 = (t1 < 16 ? (u ? UB1 : UB0) + (size_t)t1 * 24576 : UB1);
        if (n < 62) WAITV(8); else WAITV(4);
        lds_post(aL + 4 * r, n + 1);
        lds_wait_min<4>(aL, n + 1);
        if (n + 3 < 64) {
          const char* src = vbase + nb1;
#pragma unroll
          for (int i = 0; i < 4; ++i) dma16<true>(dKV + (4 * ITB - sV) + i * 1024, src, vV[i]);
        }
        f32x16 sc = {};
        {
          bf16x8 kf[12];
          const unsigned k0 = kof[0] + sK, k1 = kof[1] + sK, k2 = kof[2] + sK, k3 = kof[3] + sK;
#pragma unroll
          for (int ks = 0; ks < 12; ++ks) kf[ks] = lds128(((ks & 3) == 0 ? k0 : (ks & 3) == 1 ? k1 : (ks & 3) == 2 ? k2 : k3) + 128 * (ks >> 2));
          SBAR();
#pragma unroll
          for (int ks = 0; ks < 12; ++ks) sc = MFMA32(kf[ks], qf[ks], sc);
          SBAR();
        }
        if (u == 0 && t == 15) {
          lds_wait_ge(L0 + C_QRDY, 1);
          lds_wait_ge(L0 + C_QRDY + 4, 1);
#pragma unroll
          for (int ks = 0; ks < 12; ++ks) qf[ks] = lds128(L0 + SIDE + ((r * 12 + ks) * 64 + ln) * 16);
        }
        float pmax = sc[0];
#pragma unroll
        for (int i = 1; i < 16; ++i) pmax = fmaxf(pmax, sc[i]);
        {
          auto rr = __builtin_amdgcn_permlane32_swap(__float_as_uint(pmax), __float_as_uint(pmax), false, false);
          pmax = fmaxf(__uint_as_float(rr[0]), __uint_as_float(rr[1]));
        }
        if (!__all(pmax - m <= THR_RAW)) {
          const float mn = fmaxf(m, pmax);
          const float alpha = __builtin_amdgcn_exp2f((m - mn) * CSC);
          m = mn;
          l *= alpha;
#pragma unroll
          for (int d = 0; d < 6; ++d)
#pragma unroll
            for (int i = 0; i < 16; ++i) o[d][i] *= alpha;
        }
        const float mc = -m * CSC;
#pragma unroll
        for (int i = 0; i < 16; ++i) {
          sc[i] = __builtin_amdgcn_exp2f(fmaf(sc[i], CSC, mc));
          l += sc[i];
        }
        bf16x8 pa[2];
#pragma unroll
        for (int s = 0; s < 2; ++s) {
          u32x4 w = {cvtpk(sc[8 * s], sc[8 * s + 1]), cvtpk(sc[8 * s + 2], sc[8 * s + 3]), cvtpk(sc[8 * s + 4], sc[8 * s + 5]),
                     cvtpk(sc[8 * s + 6], sc[8 * s + 7])};
          pa[s] = __builtin_bit_cast(bf16x8, w);
        }
        if (n + 1 < 62) WAITV(8); else WAITV(0);
        if (n + 1 == 37) lds_post(L0 + C_O0 + 16 * kh + 4 * r, 1);
        lds_post(aL + 4 * r, n + 2);
        lds_wait_min<4>(aL, n + 2);
        if (more) {
          const char* src = kbase + nb;
#pragma unroll
          for (int i = 0; i < 4; ++i) dma16<true>(dKV + sK + i * 1024, src, vK[i]);
        }
        {
          bf16x8 vf[6][2];
          const unsigned vbE = vE + sV, vbO = vO + sV;
#pragma unroll
          for (int s = 0; s < 2; ++s)
#pragma unroll
            for (int d = 0; d < 6; ++d) {
              const unsigned a = ((d & 1) ? vbO : vbE) + (16 * s) * 384 + 64 * d;
              vf[d][s] = cat(ldstr(a), ldstr(a + 8 * 384));
            }
          SBAR();
#pragma unroll
          for (int s = 0; s < 2; ++s)
#pragma unroll
            for (int d = 0; d < 6; ++d) o[d] = MFMA32(vf[d][s], pa[s], o[d]);
          SBAR();
        }
      }
      STAMP(2 + 4 * u);
      {
        auto rr = __builtin_amdgcn_permlane32_swap(__float_as_uint(l), __float_as_uint(l), false, false);
        l = __uint_as_float(rr[0]) + __uint_as_float(rr[1]);
      }
      unsigned lz;
      asm volatile("v_mbcnt_lo_u32_b32 %0, -1, 0\n\tv_mbcnt_hi_u32_b32 %0, -1, %0" : "=v"(lz));
      const int zc = lz & 31, zh = lz >> 5;
      const int pw = wave < 3 ? wave + 3 : wave - 3;
      if (zh == 0) *(LAS f32x2*)(size_t)(L0 + MISC + (wave * 32 + zc) * 8) = f32x2{m, l};
      const unsigned fme = L0 + C_XF + 8 * wave, fpa = L0 + C_XF + 8 * pw;
      float ws_ = 0.f, wo_ = 0.f;
      const __amdgpu_buffer_rsrc_t rsO = __builtin_amdgcn_make_buffer_rsrc(ws + WS_O, (short)0, 2 * 256 * 36864, 0x00020000);
      const int orw = 32 * r + zc;
      const int obase = (u * 256 + blockIdx.x) * 36864 + orw * 128;
      const int ofs = (orw >> 1) & 7;
      auto combine_store = [&](int rd, const f32x4 (&rv)[4]) {
#pragma unroll
        for (int i = 0; i < 4; i += 2) {
          unsigned pk[4];
#pragma unroll
          for (int j = 0; j < 2; ++j) {
            float v[4];
#pragma unroll
            for (int k = 0; k < 4; ++k) {
              const float own = kh == 0 ? o[rd][4 * (i + j) + k] : o[3 + rd][4 * (i + j) + k];
              v[k] = own * ws_ + rv[i + j][k] * wo_;
            }
            pk[2 * j] = cvtpk(v[0], v[1]);
            pk[2 * j + 1] = cvtpk(v[2], v[3]);
          }
          auto r0 = __builtin_amdgcn_permlane32_swap(pk[0], pk[2], false, false);
          auto r1 = __builtin_amdgcn_permlane32_swap(pk[1], pk[3], false, false);
          u32x4 w = {r0[0], r1[0], r0[1], r1[1]};
          const int cidx = 4 * (3 * kh + rd) + i + zh;
          __builtin_amdgcn_raw_buffer_store_b128(w, rsO, obase + (cidx >> 3) * 12288 + (((cidx & 7) ^ ofs) * 16), 0, 16);
        }
      };
      auto weights = [&]() {
        f32x2 st = *(const LAS f32x2*)(size_t)(L0 + MISC + (pw * 32 + zc) * 8);
        const float M = fmaxf(m, st[0]);
        const float as = __builtin_amdgcn_exp2f((m - M) * CSC), ao = __builtin_amdgcn_exp2f((st[0] - M) * CSC);
        const float inv = 1.0f / (as * l + ao * st[1]);
        ws_ = as * inv;
        wo_ = ao * inv;
      };
      if (u == 0) {
        const unsigned xw = L0 + XCH + wave * 4096 + lz * 16, xr = L0 + XCH + pw * 4096 + lz * 16;
#pragma unroll
        for (int rd = 0; rd < 3; ++rd) {
          const unsigned seq = rd + 1;
          if (rd >= 1) lds_wait_ge(fpa + 4, seq - 1);
#pragma unroll
          for (int i = 0; i < 4; ++i) {
            f32x4 v;
            if (kh == 0) v = f32x4{o[3 + rd][4 * i], o[3 + rd][4 * i + 1], o[3 + rd][4 * i + 2], o[3 + rd][4 * i + 3]};
            else v = f32x4{o[rd][4 * i], o[rd][4 * i + 1], o[rd][4 * i + 2], o[rd][4 * i + 3]};
            *(LAS f32x4*)(size_t)(xw + i * 1024) = v;
          }
          lds_post(fme, seq);
          lds_wait_ge(fpa, seq);
          f32x4 rv[4];
#pragma unroll
          for (int i = 0; i < 4; ++i) rv[i] = *(const LAS f32x4*)(size_t)(xr + i * 1024);
          if (rd == 0) weights();
          lds_post(fme + 4, seq);
          combine_store(rd, rv);
        }
      } else {
        const unsigned xw = L0 + kh * GRB + r * ITB + lz * 16, xr = L0 + (1 - kh) * GRB + r * ITB + lz * 16;
#pragma unroll
        for (int rd = 0; rd < 3; ++rd)
#pragma unroll
          for (int i = 0; i < 4; ++i) {
            f32x4 v;
            if (kh == 0) v = f32x4{o[3 + rd][4 * i], o[3 + rd][4 * i + 1], o[3 + rd][4 * i + 2], o[3 + rd][4 * i + 3]};
            else v = f32x4{o[rd][4 * i], o[rd][4 * i + 1], o[rd][4 * i + 2], o[rd][4 * i + 3]};
            *(LAS f32x4*)(size_t)(xw + (rd * 4 + i) * 1024) = v;
          }
        lds_post(fme, 4);
        lds_wait_ge(fpa, 4);
        weights();
#pragma unroll
        for (int rd = 0; rd < 3; ++rd) {
          f32x4 rv[4];
#pragma unroll
          for (int i = 0; i < 4; ++i) rv[i] = *(const LAS f32x4*)(size_t)(xr + (rd * 4 + i) * 1024);
          combine_store(rd, rv);
        }
      }
      STAMP(3 + 4 * u);
    }
  }
  {
    WAITV(0);
    BAR();
    unsigned ln;
    asm volatile("v_mbcnt_lo_u32_b32 %0, -1, 0\n\tv_mbcnt_hi_u32_b32 %0, -1, %0" : "=v"(ln));
    if (wave == 0 && ln < 4 && (int)ln != hq) __hip_atomic_store(FL + (size_t)(qbase + 8 * ln) * 32 + 4 + hq, TAG, __ATOMIC_RELAXED, __HIP_MEMORY_SCOPE_AGENT);
    constexpr unsigned OSLOT = 32768;
    const char* og = ws + WS_O;
    const char* wtb = ws + WS_W + (size_t)(xj * 4 + hq) * 245760;
    const unsigned voff = ln * 16;
    auto issue = [&](int s) {
      const int hs = (hq + s / 3) & 3, c3 = s % 3;
      const char* asrc = og + (size_t)(256 + qbase + 8 * hs) * 36864 + c3 * 12288;
      const char* bsrc = wtb + (size_t)(hs * 3 + c3) * 20480 - 12288;
      const unsigned dst = L0 + (s & 3) * OSLOT;
#pragma unroll
      for (int i = 0; i < 4; ++i) {
        const int pc_ = wave + 8 * i;
        dma16<false>(dst + pc_ * 1024, (pc_ < 12 ? asrc : bsrc) + pc_ * 1024, voff);
      }
    };
    const int kp = wave >> 2, rh = (wave >> 1) & 1, ch = wave & 1;
    const int l15 = ln & 15, g = ln >> 4;
    unsigned aof[3];
#pragma unroll
    for (int rb = 0; rb < 3; ++rb) {
      const int row = 48 * rh + 16 * rb + l15;
      aof[rb] = L0 + row * 128 + (((4 * kp + g) ^ ((row >> 1) & 7)) * 16);
    }
    const unsigned bof = L0 + 12288 + (kp * 10 + 5 * ch) * 1024 + ln * 16;
    f32x4 acc[3][5] = {};
    bf16x8 af[2][3], bfr[2][5];
    issue(0);
    issue(1);
    issue(2);
    if (wave == 0) {
      poll_flags(FL + (size_t)blockIdx.x * 32 + 4, 4, hq, TAG, ln);
      __builtin_amdgcn_fence(__ATOMIC_ACQUIRE, "agent");
    }
#pragma unroll
    for (int s = -1; s < 12; ++s) {
      if (s == -1) { if (wave == 0) WAITV(0); else WAITV(8); }
      else if (s <= 8) WAITV(8);
      else if (s == 9) WAITV(4);
      else WAITV(0);
      BAR();
      const int cur = s & 1, nxt = cur ^ 1;
      {
        const unsigned sb = ((s + 1) & 3) * OSLOT;
        const int s4 = s + 4;
        const bool dd = s4 < 12;
        const int hs4 = (hq + s4 / 3) & 3, c34 = s4 % 3;
        const char* asrc = og + (size_t)(256 + qbase + 8 * hs4) * 36864 + c34 * 12288;
        const char* bsrc = wtb + (size_t)(hs4 * 3 + c34) * 20480 - 12288;
        const unsigned dst = L0 + (s4 & 3) * OSLOT;
#pragma unroll
        for (int i = 0; i < 15; ++i) {
          if (s >= 0) acc[i % 3][i / 3] = MFMA16(bfr[cur][i / 3], af[cur][i % 3], acc[i % 3][i / 3]);
          SBAR();
          if (s < 11) {
            if (i < 3) af[nxt][i] = lds128(aof[i] + sb);
            else if (i < 8) bfr[nxt][i - 3] = lds128(bof + sb + (i - 3) * 1024);
          }
          if (dd && i >= 8 && i < 12) {
            const int pc_ = wave + 8 * (i - 8);
            dma16<false>(dst + pc_ * 1024, (pc_ < 12 ? asrc : bsrc) + pc_ * 1024, voff);
          }
          SBAR();
        }
      }
    }
    STAMP(8);
    BAR();
    const unsigned xb = L0 + (wave & 3) * 15360 + ln * 16;
    if (kp == 1) {
#pragma unroll
      for (int rb = 0; rb < 3; ++rb)
#pragma unroll
        for (int cb = 0; cb < 5; ++cb) *(LAS f32x4*)(size_t)(xb + (rb * 5 + cb) * 1024) = acc[rb][cb];
    }
    BAR();
    if (kp == 0) {
      const unsigned yw = L0 + 61440 + (48 * rh + l15) * 320 + (80 * ch + 4 * g) * 2;
#pragma unroll
      for (int rb = 0; rb < 3; ++rb)
#pragma unroll
        for (int cb = 0; cb < 5; ++cb) {
          f32x4 v = *(const LAS f32x4*)(size_t)(xb + (rb * 5 + cb) * 1024);
          v += acc[rb][cb];
          u32x2 w = {cvtpk(v[0], v[1]), cvtpk(v[2], v[3])};
          *(LAS u32x2*)(size_t)(yw + rb * 16 * 320 + cb * 32) = w;
        }
    }
    BAR();
    {
      const __amdgpu_buffer_rsrc_t rsP = __builtin_amdgcn_make_buffer_rsrc(ws + WS_P, (short)0, 2 * 8 * 4 * 8 * 30720, 0x00020000);
#pragma unroll
      for (int i = 0; i < 4; ++i) {
        const int j = wave + 8 * i;
        if (j < 30) {
          const u32x4 v = *(const LAS u32x4*)(size_t)(L0 + 61440 + j * 1024 + ln * 16);
          __builtin_amdgcn_raw_buffer_store_b128(v, rsP, (((8 + b0) * 4 + hq) * 8 + xj) * 30720 + j * 1024 + ln * 16, 0, 16);
        }
      }
      WAITV(0);
    }
    BAR();
    if (wave == 0) {
      if (ln < 8) __hip_atomic_store(FL + (size_t)((int)ln + 8 * xi) * 32 + 8 + xj, TAG, __ATOMIC_RELAXED, __HIP_MEMORY_SCOPE_AGENT);
      poll_flags(FL + (size_t)blockIdx.x * 32 + 24, 8, -1, TAG, ln);
      poll_flags(FL + (size_t)blockIdx.x * 32 + 8, 8, -1, TAG, ln);
      __builtin_amdgcn_fence(__ATOMIC_ACQUIRE, "agent");
      WAITV(0);
    }
    BAR();
    STAMP(9);
    const int tz = wave * 64 + (int)ln;
    if (tz < 480) {
      const int u = tz / 240, e = tz % 240;
      const char* pp = ws + WS_P + (size_t)(((u * 8 + b0) * 4 + hq) * 8) * 30720 + 12 * xj * 320 + e * 16;
      u32x4 v[8];
#pragma unroll
      for (int j = 0; j < 8; ++j) v[j] = *(const u32x4*)(pp + (size_t)j * 30720);
      float a[8];
#pragma unroll
      for (int k = 0; k < 4; ++k) {
        float lo = 0.f, hi2 = 0.f;
#pragma unroll
        for (int j = 0; j < 8; ++j) { lo += __uint_as_float(v[j][k] << 16); hi2 += __uint_as_float(v[j][k] & 0xffff0000u); }
        a[2 * k] = lo; a[2 * k + 1] = hi2;
      }
      u32x4 w = {cvtpk(a[0], a[1]), cvtpk(a[2], a[3]), cvtpk(a[4], a[5]), cvtpk(a[6], a[7])};
      const int row = e / 20, c8 = e % 20;
      *(u32x4*)(out + (size_t)((b0 + 8 * u) * SQ + 12 * xj + row) * DM + hq * 160 + c8 * 8) = w;
    }
    STAMP(10);
  }
}

extern "C" void kernel_launch(void* const* d_in, const int* in_sizes, int n_in, void* d_out, int out_size, void* d_ws,
                              size_t ws_size, hipStream_t stream) {
  const u16* x = (const u16*)d_in[0];
  const u16* kc = (const u16*)d_in[1];
  const u16* vc = (const u16*)d_in[2];
  const u16* wq = (const u16*)d_in[3];
  const u16* wo = (const u16*)d_in[4];
  (void)hipFuncSetAttribute((const void*)attn_kernel, hipFuncAttributeMaxDynamicSharedMemorySize, LDS_A);
  attn_kernel<<<256, 512, LDS_A, stream>>>(x, kc, vc, wq, wo, (u16*)d_out, (char*)d_ws, (unsigned long long*)((char*)d_ws + (128u << 20)));
}
```

```cpp
#include <hip/hip_runtime.h>
#include <hip/hip_bf16.h>
#include <stdint.h>
#include <type_traits>

typedef unsigned short u16;
typedef short bf16x8 __attribute__((ext_vector_type(8)));
typedef short s16x4 __attribute__((ext_vector_type(4)));
typedef float f32x16 __attribute__((ext_vector_type(16)));
typedef float f32x4 __attribute__((ext_vector_type(4)));
typedef float f32x2 __attribute__((ext_vector_type(2)));
typedef __bf16 bf16x2 __attribute__((ext_vector_type(2)));
typedef unsigned u32x4 __attribute__((ext_vector_type(4)));
typedef unsigned u32x2 __attribute__((ext_vector_type(2)));
#define LAS __attribute__((address_space(3)))
#define DI __device__ __forceinline__

constexpr int NB = 16, SQ = 96, DM = 640, NH = 32, SK = 1024, DH = 192;
constexpr int HD = NH * DH;

#define MFMA32(a, b, c) __builtin_amdgcn_mfma_f32_32x32x16_bf16((a), (b), (c), 0, 0, 0)
#define MFMA16(a, b, c) __builtin_amdgcn_mfma_f32_16x16x32_bf16((a), (b), (c), 0, 0, 0)
#define BAR() do { asm volatile("" ::: "memory"); __builtin_amdgcn_s_waitcnt(0xC07F); __builtin_amdgcn_s_barrier(); asm volatile("" ::: "memory"); } while (0)
#define WAITV(n) asm volatile("s_waitcnt vmcnt(%0)" ::"i"(n) : "memory")
#define SBAR() __builtin_amdgcn_sched_barrier(0)
#define MAX3(a, b, c) fmaxf(fmaxf((a), (b)), (c))
#define LANEID(v) asm volatile("v_mbcnt_lo_u32_b32 %0, -1, 0\n\tv_mbcnt_hi_u32_b32 %0, -1, %0" : "=v"(v))

DI unsigned cvtpk(float lo, float hi) {
  f32x2 v = {lo, hi};
  bf16x2 b = __builtin_convertvector(v, bf16x2);
  return __builtin_bit_cast(unsigned, b);
}
DI void dma16nt(unsigned lds_addr, const void* sbase, unsigned voff) {
  asm volatile("s_mov_b32 m0, %0\n\ts_nop 0\n\tglobal_load_lds_dwordx4 %1, %2 nt" ::"s"(lds_addr), "v"(voff), "s"(sbase) : "memory");
}
DI void bdma(unsigned lds_addr, __amdgpu_buffer_rsrc_t rs, unsigned voff, unsigned soff) {
  asm volatile("s_mov_b32 m0, %0\n\ts_nop 0\n\tbuffer_load_dwordx4 %1, %2, %3 offen lds" ::"s"(lds_addr), "v"(voff), "s"(rs), "s"(soff) : "memory");
}
DI void dma16(unsigned lds_addr, const void* sbase, unsigned voff) {
  asm volatile("s_mov_b32 m0, %0\n\ts_nop 0\n\tglobal_load_lds_dwordx4 %1, %2" ::"s"(lds_addr), "v"(voff), "s"(sbase) : "memory");
}
template <int I, int N, class F>
DI void sfor(F&& f) {
  if constexpr (I < N) { f(std::integral_constant<int, I>{}); sfor<I + 1, N>(f); }
}
DI bf16x8 lds128(unsigned a) { return *(const LAS bf16x8*)(size_t)a; }
DI s16x4 ldstr(unsigned a) { return __builtin_amdgcn_ds_read_tr16_b64_v4i16((LAS s16x4*)(size_t)a); }
DI bf16x8 cat(s16x4 a, s16x4 b) { return __builtin_shufflevector(a, b, 0, 1, 2, 3, 4, 5, 6, 7); }

constexpr unsigned ITB = 12288;
constexpr unsigned NSL = 5;
constexpr unsigned GRB = NSL * ITB;
constexpr unsigned SIDE = 2 * GRB;
constexpr unsigned MISC = SIDE + 36864;
constexpr unsigned CTL = MISC + 2048;
constexpr unsigned HB = MISC - 49152;
constexpr unsigned C_L = CTL;
constexpr unsigned C_R = CTL + 16;
constexpr unsigned C_QRDY = CTL + 48;
constexpr unsigned C_XF = CTL + 64;
constexpr unsigned LDS_A = 163840;
constexpr float CSC = 0.07216878364870322f * 1.4426950408889634f;
constexpr float THR_RAW = 8.0f / CSC;
constexpr int SPIN_MAX = 1 << 20;
#define HD_D 4
#define TL_D 4
#define KV0 2

#define STAMP(i)
#define FST(i)

DI void lds_post(unsigned a, unsigned v) {
  asm volatile("s_waitcnt lgkmcnt(0)\n\tds_write_b32 %0, %1" ::"v"(a), "v"(v) : "memory");
}
DI void lds_post_nw(unsigned a, unsigned v) {
  asm volatile("ds_write_b32 %0, %1" ::"v"(a), "v"(v) : "memory");
}
DI void lds_wait_ge(unsigned a, unsigned n) {
#pragma nounroll
  for (int it = 0; it < SPIN_MAX; ++it) {
    unsigned v;
    asm volatile("ds_read_b32 %0, %1\n\ts_waitcnt lgkmcnt(0)" : "=v"(v) : "v"(a) : "memory");
    if (__builtin_amdgcn_readfirstlane(v) >= n) break;
    __builtin_amdgcn_s_sleep(1);
  }
}
DI void lds_wait_min4(unsigned a, unsigned n) {
#pragma nounroll
  for (int it = 0; it < SPIN_MAX; ++it) {
    u32x4 d;
    asm volatile("ds_read_b128 %0, %1\n\ts_waitcnt lgkmcnt(0)" : "=v"(d) : "v"(a) : "memory");
    const unsigned mn = min(min(d[0], d[1]), min(d[2], d[3]));
    if (__builtin_amdgcn_readfirstlane(mn) >= n) break;
    __builtin_amdgcn_s_sleep(1);
  }
}
DI unsigned lds_peek(unsigned a) { return *(volatile LAS unsigned*)(size_t)a; }

typedef __attribute__((address_space(1))) unsigned long long gu64;
extern "C" __device__ unsigned long long amd_dispatch_id() __asm("llvm.amdgcn.dispatch.id");
constexpr size_t WS_O = 0, WS_P = 32u << 20, WS_F = 96u << 20;

DI bool poll_flags(gu64* f, int nl, int skip, unsigned long long T, int lane) {
#pragma nounroll
  for (int it = 0; it < SPIN_MAX; ++it) {
    unsigned long long v = T;
    if (lane < nl && lane != skip) v = __hip_atomic_load(f + lane, __ATOMIC_RELAXED, __HIP_MEMORY_SCOPE_AGENT);
    if (__all(v == T)) {
      if (lane < nl && lane != skip) __hip_atomic_store(f + lane, 0ull, __ATOMIC_RELAXED, __HIP_MEMORY_SCOPE_AGENT);
      return true;
    }
    __builtin_amdgcn_s_sleep(2);
  }
  return false;
}

constexpr int hNL(int j) { return j < 20 ? 3 : 0; }
constexpr int hKV(int p) { return (p >= KV0 && p < KV0 + 12) ? 1 : 0; }
constexpr int hPend(int j) {
  int cnt = 0, first = 0;
  if (j < HD_D) { cnt = 3 * (HD_D - 1 - j); first = 0; }
  else { cnt = hKV(j - HD_D); first = j - HD_D + 1; }
  for (int p = first; p < j; ++p) cnt += hNL(p + HD_D) + hKV(p);
  return cnt;
}
constexpr int tNL(int j) { return j < 24 ? 3 : 0; }
constexpr int tPend(int j) {
  int cnt = 0, first = 0;
  if (j < TL_D) { cnt = 3 * (TL_D - 1 - j); first = 0; }
  else { cnt = 0; first = j - TL_D + 1; }
  for (int p = first; p < j; ++p) cnt += tNL(p + TL_D);
  return cnt;
}

__global__ __launch_bounds__(512, 2) void attn_kernel(const u16* x, const u16* kc, const u16* vc, const u16* wq, const u16* wo,
                                                      u16* out, char* ws, unsigned long long* stamps) {
  extern __shared__ __attribute__((aligned(16))) char lds[];
  const unsigned L0 = (unsigned)(size_t)(LAS char*)lds;
  const int tid = threadIdx.x, lane = tid & 63;
  const int wave = __builtin_amdgcn_readfirstlane(tid >> 6);
  const int xj = blockIdx.x & 7, xi = blockIdx.x >> 3;
  const int hq = xi & 3, h = 4 * xj + hq, b0 = xi >> 2;
  const size_t UB0 = (size_t)(b0 * NH + h) * SK * 384, UB1 = (size_t)((b0 + 8) * NH + h) * SK * 384;
  unsigned long long TAG;
  {
    const unsigned long long did = amd_dispatch_id(), dp = (unsigned long long)__builtin_amdgcn_dispatch_ptr();
    const unsigned t32 = (unsigned)(did * 0x9E3779B1ull) ^ (unsigned)(dp >> 6) * 0x85EBCA77u ^ (unsigned)(did >> 32);
    TAG = ((unsigned long long)(t32 ^ 0x5A17C3E9u) << 32) | t32;
  }
  const int qbase = xj + 32 * b0;
  gu64* FL = (gu64*)(ws + WS_F);
  const int kh = (0x1A >> wave) & 1, r = (0x102210 >> (4 * wave)) & 3;
  STAMP(0);
  bf16x8 qf[12];

  unsigned offK[6], offV[3];
  {
    if (wave == 6 && lane < 48) *(LAS unsigned*)(size_t)(L0 + CTL + lane * 4) = 0u;
    const int rot = (b0 * 5 / 4 + hq * 5 / 2) % 10;
    const int dh = wave & 1, rq = wave >> 1;
    const unsigned g = lane >> 4, q4 = (lane & 15) >> 2, p4 = lane & 3, l15 = lane & 15;
    const __amdgpu_buffer_rsrc_t rsW = __builtin_amdgcn_make_buffer_rsrc((void*)((const char*)wq + h * DH * 2), (short)0, DM * HD * 2 - h * DH * 2, 0x00020000);
    const __amdgpu_buffer_rsrc_t rsX = __builtin_amdgcn_make_buffer_rsrc((void*)((const char*)x + b0 * SQ * DM * 2), (short)0, (NB - b0) * SQ * DM * 2, 0x00020000);
    unsigned gA, gB, gC;
    {
      auto wpiece = [&](int p) {
        const int n = 64 * p + lane, row = n / 24, pc = n % 24;
        const int sw = ((row >> 1) & 1) | (((row >> 3) & 1) << 1);
        return (unsigned)(row * (HD * 2) + ((pc ^ (sw << 1)) * 16));
      };
      auto xpiece = [&](int p) {
        const int row = 8 * p + (lane >> 3);
        return (unsigned)(row * (DM * 2) + (((lane & 7) ^ ((row >> 1) & 7)) * 16));
      };
      gA = wpiece(wave);
      gB = wave < 4 ? wpiece(8 + wave) : xpiece(wave - 4);
      gC = xpiece(4 + wave);
    }
#pragma unroll
    for (int j = 0; j < 6; ++j) {
      int n = 64 * j + lane, row = n / 24, pc = n % 24;
      offK[j] = row * 384 + ((pc ^ ((row >> 1) & 7)) * 16);
    }
#pragma unroll
    for (int j = 0; j < 3; ++j) {
      int n = 64 * j + lane, row = n / 24, pc = n % 24;
      offV[j] = row * 384 + ((pc ^ (((row >> 1) & 1) << 2)) * 16);
    }
    constexpr unsigned WB = 3 * ITB, G1X = GRB + 2 * ITB, U1 = 8 * SQ * DM * 2;
    constexpr unsigned WSTEP = 32 * HD * 2;
    const unsigned mW = L0 + WB + wave * 1024;
    const unsigned mX = L0 + (4 + wave) * 1024;
    const unsigned s2 = ((q4 >> 1) & 1) | ((g & 1) << 1);
    unsigned E[4];
#pragma unroll
    for (int j = 0; j < 4; ++j) E[j] = L0 + WB + (8 * g + q4) * 384 + 32 * ((unsigned)(6 * dh + j) ^ s2) + 8 * p4;
    const unsigned XA0 = L0 + (rq >> 1) * G1X + (48 * (rq & 1) + l15) * 128 + ((g ^ ((l15 >> 1) & 7)) * 16), XA1 = XA0 ^ 64;
    f32x4 acc[6][3] = {};
    bf16x8 af[2][6], bfr[2][3];
    unsigned xa;
    auto rdA = [&](int par, int c1, int db) {
      const unsigned a = E[db & 3] + (db >> 2) * 128 + (c1 & 3) * ITB;
      af[par][db] = cat(ldstr(a), ldstr(a + 4 * 384));
    };
    auto rdB = [&](int par, int rb) { bfr[par][rb] = lds128(xa + rb * 2048); };
    unsigned woff = (unsigned)(2 * rot) * WSTEP, xoff = (unsigned)rot * 128;
    auto advW = [&]() { woff += WSTEP; woff = woff == 20 * WSTEP ? 0u : woff; };
    auto advX = [&]() { xoff += 128; xoff = xoff == 1280 ? 0u : xoff; };
    auto request = [&](auto VV, int k, int wsl, unsigned xs, int un) {
      constexpr int V = decltype(VV)::value;
      if (k == 0) bdma(mW + wsl * ITB, rsW, gA, woff);
      if (k == 1) {
        if (V == 0) bdma(mW + 8192 + wsl * ITB, rsW, gB, woff);
        else bdma(mX - 8192 + un * G1X + xs, rsX, gB, xoff + un * U1);
      }
      if (k == 2) bdma(mX + un * G1X + xs, rsX, gC, xoff + un * U1);
    };
    unsigned xr = 2 * ITB;
    unsigned xw = 1 * ITB;
    auto body = [&](auto VV) {
      constexpr int V = decltype(VV)::value;
      if (V == 1) bdma(mX - 8192 + 2 * ITB, rsX, gB, xoff);
      bdma(mX + 2 * ITB, rsX, gC, xoff);
#pragma unroll
      for (int c = -4; c < 0; ++c) {
        if ((c & 1) != 0) advX();
        const unsigned xs = (unsigned)((((c + 1) >> 1) + 2 + 2) % 3) * ITB;
#pragma unroll
        for (int k = 0; k < 3; ++k) request(VV, k, c & 3, xs, (c + 1) & 1);
        advW();
      }
      WAITV(9);
      BAR();
      xa = XA0 + 2 * ITB;
#pragma unroll
      for (int rb = 0; rb < 3; ++rb) rdB(0, rb);
#pragma unroll
      for (int db = 0; db < 6; ++db) rdA(0, 0, db);
      WAITV(6);
      BAR();
      STAMP(4);
      auto phase = [&](int c, auto QQ, auto MODE) {
        constexpr int q = decltype(QQ)::value, mode = decltype(MODE)::value, cur = q & 1;
        if (cur == 1) {
          xr = xr == 2 * ITB ? 0 : xr + ITB;
          if (mode == 0) { xw = xw == 2 * ITB ? 0 : xw + ITB; advX(); }
        }
        xa = (cur == 1 ? XA0 : XA1) + xr;
        constexpr int R0 = V == 0 ? 0 : 5, D0 = V == 0 ? 10 : 0;
#pragma unroll
        for (int i = 0; i < 18; ++i) {
          acc[i / 3][i % 3] = MFMA16(af[cur][i / 3], bfr[cur][i % 3], acc[i / 3][i % 3]);
          SBAR();
          if (i >= R0 && i < R0 + 3) rdB(cur ^ 1, i - R0);
          if (i >= R0 + 3 && i < R0 + 9) rdA(cur ^ 1, q + 1, i - R0 - 3);
          if (mode == 0 && i >= D0 && i < D0 + 6 && !((i - D0) & 1)) request(VV, (i - D0) >> 1, q, xw, (q + 1) & 1);
          if (mode == 0 && i == D0 + 5) advW();
          if (V == 1 && mode == 2 && i >= 4 && i < 16 && wave >= 6) {
            const int pi = i - 4, li = c - 17, hw = wave - 6;
            const unsigned dst = L0 + hw * G1X + li * ITB + pi * 1024;
            if (li == 2) dma16nt(dst, (const char*)vc + UB0 + (size_t)(32 * hw) * 384 + (pi / 3) * 3072, offV[pi % 3]);
            else dma16nt(dst, (const char*)kc + UB0 + (size_t)(64 * li + 32 * hw) * 384 + (pi / 6) * 6144, offK[pi % 6]);
          }
          SBAR();
        }
        if (mode == 0) WAITV(6);
        if (mode == 1) WAITV(0);
        BAR();
      };
      using I0 = std::integral_constant<int, 0>; using I1 = std::integral_constant<int, 1>;
      using I2 = std::integral_constant<int, 2>; using I3 = std::integral_constant<int, 3>;
#pragma unroll 1
      for (int c = 0; c < 16; c += 4) {
        phase(c, I0{}, I0{});
        phase(c + 1, I1{}, I0{});
        phase(c + 2, I2{}, I0{});
        phase(c + 3, I3{}, I0{});
      }
      STAMP(5);
      phase(16, I0{}, I1{});
      phase(17, I1{}, I2{});
      phase(18, I2{}, I2{});
      phase(19, I3{}, I2{});
    };
    if (wave < 4) body(std::integral_constant<int, 0>{}); else body(std::integral_constant<int, 1>{});
    STAMP(11);
    auto qstore = [&]() {
      unsigned lz; LANEID(lz);
      const unsigned zg = lz >> 4, zl = lz & 15;
#pragma unroll
      for (int rb = 0; rb < 3; ++rb) {
        const int R16 = 3 * (rq & 1) + rb;
        const unsigned qb = L0 + SIDE + ((R16 >> 1) * 12 + 6 * dh) * 1024 + (16 * (R16 & 1) + zl + 32 * (zg >> 1)) * 16 + 8 * (zg & 1);
#pragma unroll
        for (int db = 0; db < 6; ++db) {
          u32x2 w = {cvtpk(acc[db][rb][0], acc[db][rb][1]), cvtpk(acc[db][rb][2], acc[db][rb][3])};
          *(LAS u32x2*)(size_t)(qb + db * 1024) = w;
        }
      }
    };
    if (wave < 4) qstore();
    BAR();
    if (wave < 6) {
      unsigned lz; LANEID(lz);
#pragma unroll
      for (int ks = 0; ks < 12; ++ks) qf[ks] = lds128(L0 + SIDE + ((r * 12 + ks) * 64 + lz) * 16);
    }
    BAR();
    if (wave >= 4) { qstore(); lds_post(L0 + C_QRDY + 4 * (wave - 4), 1); }
  }
  STAMP(1);

  if (wave >= 6) {
    const int hw = wave - 6;
    __builtin_amdgcn_s_setprio(3);
    unsigned ll; LANEID(ll);
    const unsigned aL = L0 + C_L + 4 * hw, aR = L0 + C_R + 16 * hw;
    unsigned li = 3, lpub = 0, slot = hw ? 0 : 3 * ITB;
#pragma nounroll
    for (int it = 0; it < (SPIN_MAX << 2); ++it) {
      bool idle = true;
      if (li < 64) {
        u32x4 d;
        asm volatile("ds_read_b128 %0, %1\n\ts_waitcnt lgkmcnt(0)" : "=v"(d) : "v"(aR) : "memory");
        const unsigned mn = __builtin_amdgcn_readfirstlane(min(min(d[0], d[1]), d[2]));
        if (mn + NSL > li) {
          const unsigned j = li & 31;
          const bool isK = (j == 0) || ((j & 1) && j < 31);
          const unsigned tile = isK ? (j + 1) >> 1 : (j - 1) >> 1;
          const size_t ub = ((li >> 5) ? UB1 : UB0) + (size_t)(64 * tile + 32 * hw) * 384;
          const unsigned dst = L0 + hw * GRB + slot;
          if (isK) {
            const char* src = (const char*)kc + ub;
#pragma unroll
            for (int q = 0; q < 12; ++q) dma16nt(dst + q * 1024, src + (q / 6) * 6144, offK[q % 6]);
          } else {
            const char* src = (const char*)vc + ub;
#pragma unroll
            for (int q = 0; q < 12; ++q) dma16nt(dst + q * 1024, src + (q / 3) * 3072, offV[q % 3]);
          }
          ++li;
          slot = slot + ITB == GRB ? 0 : slot + ITB;
          idle = false;
        }
      }
      if (lpub < li) {
        unsigned sts;
        asm volatile("s_getreg_b32 %0, hwreg(HW_REG_IB_STS)" : "=s"(sts));
        const unsigned vm = (sts & 15u) | ((sts >> 18) & 0x30u);
        const unsigned pend = ((vm + 11u) * 43691u) >> 19;
        const unsigned done = li - pend;
        if (done > lpub) {
          if (pend == 0) WAITV(0);
          else if (pend == 1) WAITV(12);
          else if (pend == 2) WAITV(24);
          else if (pend == 3) WAITV(36);
          else WAITV(48);
          lpub = done;
          lds_post_nw(aL, lpub);
          idle = false;
        }
      } else if (li == 64) {
        break;
      }
      if (idle) __builtin_amdgcn_s_sleep(1);
    }
    __builtin_amdgcn_s_setprio(0);
    {
      const unsigned n0 = ll;
      unsigned a = 0;
#pragma unroll
      for (int i = 0; i < 4; ++i) {
        const unsigned n = n0 + 64 * i, row = n / 5, sg = n % 5;
        if (n < 240) a ^= *(const unsigned*)((const char*)wo + (size_t)(4 * xj * DH + 96 * b0 + 48 * hw + row) * (DM * 2) + hq * 320 + sg * 64);
      }
      asm volatile("" ::"v"(a));
    }
  } else {
    unsigned ln; LANEID(ln);
    unsigned kof[4], vE, vO;
    {
      const unsigned a32 = ln & 31, ahi = ln >> 5, ablk = (ln >> 4) & 1, aq4 = (ln & 15) >> 2, ap4 = ln & 3, axq = (aq4 >> 1) & 1;
#pragma unroll
      for (int i = 0; i < 4; ++i) kof[i] = L0 + kh * GRB + a32 * 384 + (((2 * i + ahi) ^ ((a32 >> 1) & 7)) * 16);
      vE = L0 + kh * GRB + (4 * ahi + aq4) * 384 + 64 * axq + 32 * ablk + 8 * ap4;
      vO = L0 + kh * GRB + (4 * ahi + aq4) * 384 - 64 * axq + 32 * ablk + 8 * ap4;
    }
    const unsigned aF = L0 + C_L + 4 * kh, aR = L0 + C_R + 16 * kh + 4 * r;
    const int pw = (0x102354 >> (4 * wave)) & 7;
    const unsigned fme = L0 + C_XF + 8 * wave, fpa = L0 + C_XF + 8 * pw;
    unsigned cur = kh ? 2 * ITB : 0;
    unsigned cnt = 0;
#define ADV() do { cur = (cur + ITB == GRB) ? 0u : cur + ITB; ++cnt; } while (0)
#define KADDR(ks) (((ks) & 3) == 0 ? k0 : ((ks) & 3) == 1 ? k1 : ((ks) & 3) == 2 ? k2 : k3) + 128 * ((ks) >> 2)
#define VADDR(j) ((((j) % 6) & 1) ? vbO : vbE) + (16 * ((j) / 6)) * 384 + 64 * ((j) % 6)

#pragma unroll 1
    for (int u = 0; u < 2; ++u) {
      float m, l = 0.f;
      f32x16 o[6] = {};
      f32x16 sB;
      float pe[16];
      bf16x8 kf[12], vf[12];
      unsigned k0, k1, k2, k3, vbE, vbO;
      lds_wait_ge(aF, cnt + 1);
      k0 = kof[0] + cur; k1 = kof[1] + cur; k2 = kof[2] + cur; k3 = kof[3] + cur;
#pragma unroll
      for (int ks = 0; ks < 12; ++ks) kf[ks] = lds128(KADDR(ks));
      ADV();
      lds_post_nw(aR, cnt);
      SBAR();
      {
        f32x16 z = {};
        sB = MFMA32(kf[0], qf[0], z);
#pragma unroll
        for (int ks = 1; ks < 12; ++ks) sB = MFMA32(kf[ks], qf[ks], sB);
      }
      SBAR();
      lds_wait_ge(aF, cnt + 1);
      k0 = kof[0] + cur; k1 = kof[1] + cur; k2 = kof[2] + cur; k3 = kof[3] + cur;
#pragma unroll
      for (int ks = 0; ks < 4; ++ks) kf[ks] = lds128(KADDR(ks));
      {
        float pmax = sB[0];
#pragma unroll
        for (int i = 1; i < 16; ++i) pmax = fmaxf(pmax, sB[i]);
        auto rr = __builtin_amdgcn_permlane32_swap(__float_as_uint(pmax), __float_as_uint(pmax), false, false);
        m = fmaxf(__uint_as_float(rr[0]), __uint_as_float(rr[1]));
        const float mc = -m * CSC;
#pragma unroll
        for (int i = 0; i < 16; ++i) pe[i] = fmaf(sB[i], CSC, mc);
      }
      SBAR();
      bf16x8 pa[2];
      auto expstep = [&](int i) {
        pe[2 * i] = __builtin_amdgcn_exp2f(pe[2 * i]);
        pe[2 * i + 1] = __builtin_amdgcn_exp2f(pe[2 * i + 1]);
        l += pe[2 * i];
        l += pe[2 * i + 1];
        asm volatile("" : "+v"(pe[2 * i]), "+v"(pe[2 * i + 1]), "+v"(l));
      };
      auto cvtstep = [&](int s) {
        u32x4 w = {cvtpk(pe[8 * s], pe[8 * s + 1]), cvtpk(pe[8 * s + 2], pe[8 * s + 3]), cvtpk(pe[8 * s + 4], pe[8 * s + 5]),
                   cvtpk(pe[8 * s + 6], pe[8 * s + 7])};
        asm volatile("" : "+v"(w));
        pa[s] = __builtin_bit_cast(bf16x8, w);
      };
#pragma unroll 1
      for (int t = 0; t < 15; ++t) {
        unsigned fl = 0;
#pragma unroll
        for (int i = 0; i < 12; ++i) {
          if (i == 0) { f32x16 z = {}; sB = MFMA32(kf[0], qf[0], z); }
          else sB = MFMA32(kf[i], qf[i], sB);
          if (i < 8) kf[i + 4] = lds128(KADDR(i + 4));
          if (i == 2) fl = lds_peek(aF);
          if (i == 7) { ADV(); lds_post_nw(aR, cnt); }
          if (i < 8) expstep(i);
          if (i == 8) {
            if (__builtin_amdgcn_readfirstlane(fl) < cnt + 1) lds_wait_ge(aF, cnt + 1);
            vbE = vE + cur; vbO = vO + cur;
            cvtstep(0);
          }
          if (i == 9) cvtstep(1);
          if (i >= 8) vf[i - 8] = cat(ldstr(VADDR(i - 8)), ldstr(VADDR(i - 8) + 8 * 384));
          SBAR();
        }
        float pmax = 0.f, alpha = 1.f, mc = 0.f;
        bool resc = false;
#pragma unroll
        for (int j = 0; j < 12; ++j) {
          o[j % 6] = MFMA32(vf[j], pa[j / 6], o[j % 6]);
          if (j < 8) vf[j + 4] = cat(ldstr(VADDR(j + 4)), ldstr(VADDR(j + 4) + 8 * 384));
          if (j == 2) fl = lds_peek(aF);
          if (j == 7) { ADV(); lds_post_nw(aR, cnt); }
          if (j == 1) {
            asm volatile("" : "+v"(sB));
            pmax = MAX3(MAX3(sB[0], sB[1], sB[2]), MAX3(sB[3], sB[4], sB[5]), MAX3(sB[6], sB[7], sB[8]));
            asm volatile("" : "+v"(pmax));
          }
          if (j == 2) {
            pmax = MAX3(MAX3(sB[9], sB[10], sB[11]), MAX3(sB[12], sB[13], sB[14]), MAX3(sB[15], sB[15], pmax));
            asm volatile("" : "+v"(pmax));
          }
          if (j == 3) {
            auto rr = __builtin_amdgcn_permlane32_swap(__float_as_uint(pmax), __float_as_uint(pmax), false, false);
            pmax = fmaxf(__uint_as_float(rr[0]), __uint_as_float(rr[1]));
            resc = pmax - m > THR_RAW;
            const float mn = resc ? pmax : m;
            alpha = __builtin_amdgcn_exp2f((m - mn) * CSC);
            m = mn;
            mc = -mn * CSC;
            l *= alpha;
            asm volatile("" : "+v"(m), "+v"(mc), "+v"(l), "+v"(alpha));
          }
          if (j >= 4 && j < 8) {
#pragma unroll
            for (int i = 4 * (j - 4); i < 4 * (j - 4) + 4; ++i) pe[i] = fmaf(sB[i], CSC, mc);
            asm volatile("" : "+v"(pe[4 * (j - 4)]), "+v"(pe[4 * (j - 4) + 1]), "+v"(pe[4 * (j - 4) + 2]), "+v"(pe[4 * (j - 4) + 3]));
          }
          if (j == 8) {
            if (t < 14 && __builtin_amdgcn_readfirstlane(fl) < cnt + 1) lds_wait_ge(aF, cnt + 1);
            k0 = kof[0] + cur; k1 = kof[1] + cur; k2 = kof[2] + cur; k3 = kof[3] + cur;
          }
          if (j >= 8) kf[j - 8] = lds128(KADDR(j - 8));
          SBAR();
        }
        if (__any(resc)) {
#pragma unroll
          for (int d = 0; d < 6; ++d)
#pragma unroll
            for (int i = 0; i < 16; ++i) o[d][i] *= alpha;
        }
      }
      {
        lds_wait_ge(aF, cnt + 1);
        vbE = vE + cur; vbO = vO + cur;
#pragma unroll
        for (int j = 0; j < 6; ++j) vf[j] = cat(ldstr(VADDR(j)), ldstr(VADDR(j) + 8 * 384));
        SBAR();
#pragma unroll
        for (int i = 0; i < 8; ++i) expstep(i);
        cvtstep(0);
        cvtstep(1);
        SBAR();
#pragma unroll
        for (int j = 0; j < 12; ++j) {
          o[j % 6] = MFMA32(vf[j], pa[j / 6], o[j % 6]);
          if (j < 6) vf[j + 6] = cat(ldstr(VADDR(j + 6)), ldstr(VADDR(j + 6) + 8 * 384));
          if (j == 5) { ADV(); lds_post_nw(aR, cnt); }
          SBAR();
        }
        if (u == 0) {
          lds_wait_min4(L0 + C_QRDY, 1);
#pragma unroll
          for (int ks = 0; ks < 12; ++ks) qf[ks] = lds128(L0 + SIDE + ((r * 12 + ks) * 64 + ln) * 16);
        }
        lds_post(fme + 4, 4 * u + 1);
      }
      STAMP(2 + 4 * u);
      {
        auto rr = __builtin_amdgcn_permlane32_swap(__float_as_uint(l), __float_as_uint(l), false, false);
        l = __uint_as_float(rr[0]) + __uint_as_float(rr[1]);
      }
      unsigned lz; LANEID(lz);
      const int zc = lz & 31, zh = lz >> 5;
      if (zh == 0) *(LAS f32x2*)(size_t)(L0 + MISC + (wave * 32 + zc) * 8) = f32x2{m, l};
      float ws_ = 0.f, wo_ = 0.f;
      const __amdgpu_buffer_rsrc_t rsO = __builtin_amdgcn_make_buffer_rsrc(ws + WS_O, (short)0, 2 * 256 * 36864, 0x00020000);
      const int obase = (u * 256 + blockIdx.x) * 36864 + (32 * r + zc) * 64;
      const int osw = ((zc >> 2) & 1) << 1;
      const unsigned xw = L0 + SIDE + r * ITB + kh * 4096 + lz * 16, xr = L0 + SIDE + r * ITB + (1 - kh) * 4096 + lz * 16;
#pragma unroll
      for (int rd = 0; rd < 3; ++rd) {
        lds_wait_ge(fpa + 4, 4 * u + rd + 1);
#pragma unroll
        for (int i = 0; i < 4; ++i) {
          f32x4 v;
          if (kh == 0) v = f32x4{o[3 + rd][4 * i], o[3 + rd][4 * i + 1], o[3 + rd][4 * i + 2], o[3 + rd][4 * i + 3]};
          else v = f32x4{o[rd][4 * i], o[rd][4 * i + 1], o[rd][4 * i + 2], o[rd][4 * i + 3]};
          *(LAS f32x4*)(size_t)(xw + i * 1024) = v;
        }
        lds_post(fme, 4 * u + rd + 1);
        lds_wait_ge(fpa, 4 * u + rd + 1);
        f32x4 rv[4];
#pragma unroll
        for (int i = 0; i < 4; ++i) rv[i] = *(const LAS f32x4*)(size_t)(xr + i * 1024);
        if (rd == 0) {
          f32x2 stt = *(const LAS f32x2*)(size_t)(L0 + MISC + (pw * 32 + zc) * 8);
          const float M = fmaxf(m, stt[0]);
          const float as = __builtin_amdgcn_exp2f((m - M) * CSC), ao = __builtin_amdgcn_exp2f((stt[0] - M) * CSC);
          const float inv = 1.0f / (as * l + ao * stt[1]);
          ws_ = as * inv;
          wo_ = ao * inv;
        }
        lds_post(fme + 4, 4 * u + rd + 2);
#pragma unroll
        for (int i = 0; i < 4; i += 2) {
          unsigned pk[4];
#pragma unroll
          for (int j = 0; j < 2; ++j) {
            float v[4];
#pragma unroll
            for (int k = 0; k < 4; ++k) {
              const float own = kh == 0 ? o[rd][4 * (i + j) + k] : o[3 + rd][4 * (i + j) + k];
              v[k] = own * ws_ + rv[i + j][k] * wo_;
            }
            pk[2 * j] = cvtpk(v[0], v[1]);
            pk[2 * j + 1] = cvtpk(v[2], v[3]);
          }
          auto r0 = __builtin_amdgcn_permlane32_swap(pk[0], pk[2], false, false);
          auto r1 = __builtin_amdgcn_permlane32_swap(pk[1], pk[3], false, false);
          u32x4 w = {r0[0], r1[0], r0[1], r1[1]};
          __builtin_amdgcn_raw_buffer_store_b128(w, rsO, obase + (3 * kh + rd) * 6144 + (((i + zh) ^ osw) * 16), 0, 16);
        }
      }
      STAMP(3 + 4 * u);
    }
  }
  {
    WAITV(0);
    BAR();
    unsigned ln; LANEID(ln);
    if (wave == 0 && ln < 4 && (int)ln != hq) __hip_atomic_store(FL + (size_t)(qbase + 8 * ln) * 32 + hq, TAG, __ATOMIC_RELAXED, __HIP_MEMORY_SCOPE_AGENT);
    constexpr unsigned TST = 22528, TOO = 10240;
    const int ch = wave & 1, rq = wave >> 1;
    const unsigned g = ln >> 4, q4 = (ln & 15) >> 2, p4 = ln & 3, l15 = ln & 15;
    unsigned gA, gB;
    {
      auto wpiece = [&](int p) {
        const int n = 64 * p + ln, row = n / 20, pc = n % 20;
        return (unsigned)(row * (DM * 2) + ((pc ^ (((row >> 3) & 1) << 1)) * 16));
      };
      gA = wpiece(wave);
      gB = wave < 2 ? wpiece(8 + wave) : ln * 16 + (wave - 2) * 1024;
    }
    const int pc3 = wave < 6 ? wave : wave - 2;
    const unsigned gC = ln * 16 + pc3 * 1024 + 256 * 36864;
    const __amdgpu_buffer_rsrc_t rsWo = __builtin_amdgcn_make_buffer_rsrc((void*)((const char*)wo + 4 * xj * DH * DM * 2 + hq * 320), (short)0, HD * DM * 2 - (4 * xj * DH * DM * 2 + hq * 320), 0x00020000);
    const __amdgpu_buffer_rsrc_t rsOt = __builtin_amdgcn_make_buffer_rsrc(ws + WS_O + (size_t)qbase * 36864, (short)0, (512 - qbase) * 36864, 0x00020000);
    const __amdgpu_buffer_rsrc_t rsB = wave < 2 ? rsWo : rsOt;
    const unsigned mA = L0 + wave * 1024, mB = wave < 2 ? L0 + (8 + wave) * 1024 : L0 + TOO + (wave - 2) * 1024, mC = L0 + TOO + 6144 + pc3 * 1024;
    const unsigned stepB = wave < 2 ? 32 * DM * 2 : 6144;
    unsigned wsrc, osrc, bsrc;
    auto sethead = [&](int hs) { wsrc = hs * (DH * DM * 2); osrc = hs * (8 * 36864); bsrc = wave < 2 ? wsrc : osrc; };
    auto request = [&](int c6, int k) {
      if (k == 0) bdma(mA + c6 * TST, rsWo, gA, wsrc + c6 * (32 * DM * 2));
      if (k == 1) bdma(mB + c6 * TST, rsB, gB, bsrc + c6 * stepB);
      if (k == 2) bdma(mC + c6 * TST, rsOt, gC, osrc + c6 * 6144);
    };
    const unsigned WAb = L0 + (8 * g + q4) * 320 + 160 * ch + 8 * p4;
    unsigned WA0[2], WA1[2], OA[2];
    WA0[0] = WAb + ((ch & 1) ? -32 * (int)(g & 1) : 32 * (int)(g & 1));
    WA1[0] = WAb + ((ch & 1) ? 32 * (int)(g & 1) : -32 * (int)(g & 1));
    OA[0] = L0 + TOO + (48 * rq + l15) * 64 + ((g ^ (((l15 >> 2) & 1) << 1)) * 16);
    WA0[1] = WA0[0] + 3 * TST; WA1[1] = WA1[0] + 3 * TST; OA[1] = OA[0] + 3 * TST;
    f32x4 acc[3][5] = {};
    bf16x8 af[2][5], bfr[2][3];
    auto rdA = [&](int par, int sl, int cb) {
      const unsigned a = ((cb & 1) ? WA1[sl / 3] : WA0[sl / 3]) + (sl % 3) * TST + cb * 32;
      af[par][cb] = cat(ldstr(a), ldstr(a + 4 * 320));
    };
    auto rdB = [&](int par, int sl, int rb) { bfr[par][rb] = lds128(OA[sl / 3] + (sl % 3) * TST + rb * 1024); };
    auto tbody = [&](auto VV) {
      constexpr int V = decltype(VV)::value;
      sethead(hq);
#pragma unroll
      for (int j = 0; j < 6; ++j) { request(j, 0); request(j, 1); request(j, 2); }
      WAITV(15);
      BAR();
#pragma unroll
      for (int rb = 0; rb < 3; ++rb) rdB(0, 0, rb);
#pragma unroll
      for (int cb = 0; cb < 5; ++cb) rdA(0, 0, cb);
      WAITV(12);
      if (wave == 7) {
        poll_flags(FL + (size_t)blockIdx.x * 32, 4, hq, TAG, ln);
        __builtin_amdgcn_fence(__ATOMIC_ACQUIRE, "agent");
        WAITV(0);
      }
      BAR();
      STAMP(12);
      auto phase = [&](auto QQ, auto MODE) {
        constexpr int q = decltype(QQ)::value, mode = decltype(MODE)::value, cur = q & 1;
        constexpr int R0 = V == 0 ? 0 : 5, D0 = V == 0 ? 9 : 0;
#pragma unroll
        for (int i = 0; i < 15; ++i) {
          acc[i % 3][i / 3] = MFMA16(af[cur][i / 3], bfr[cur][i % 3], acc[i % 3][i / 3]);
          SBAR();
          if (i >= R0 && i < R0 + 3) rdB(cur ^ 1, (q + 1) % 6, i - R0);
          if (i >= R0 + 3 && i < R0 + 8) rdA(cur ^ 1, (q + 1) % 6, i - R0 - 3);
          if (mode == 0 && i >= D0 && i < D0 + 6 && !((i - D0) & 1)) request(q, (i - D0) >> 1);
          SBAR();
        }
        if (mode == 0) WAITV(12);
        else if (q == 0) WAITV(9);
        else if (q == 1) WAITV(6);
        else if (q == 2) WAITV(3);
        else WAITV(0);
        BAR();
      };
      auto head6 = [&](auto MODE) {
        phase(std::integral_constant<int, 0>{}, MODE);
        phase(std::integral_constant<int, 1>{}, MODE);
        phase(std::integral_constant<int, 2>{}, MODE);
        phase(std::integral_constant<int, 3>{}, MODE);
        phase(std::integral_constant<int, 4>{}, MODE);
        phase(std::integral_constant<int, 5>{}, MODE);
      };
#pragma unroll 1
      for (int i = 1; i < 4; ++i) {
        sethead((hq + i) & 3);
        head6(std::integral_constant<int, 0>{});
      }
      STAMP(13);
      head6(std::integral_constant<int, 1>{});
    };
    if (wave < 4) tbody(std::integral_constant<int, 0>{}); else tbody(std::integral_constant<int, 1>{});
    STAMP(8);
    {
      const unsigned yw = L0 + (48 * rq + l15) * 336 + (80 * ch + 4 * g) * 2;
#pragma unroll
      for (int rb = 0; rb < 3; ++rb)
#pragma unroll
        for (int cb = 0; cb < 5; ++cb) {
          u32x2 w = {cvtpk(acc[rb][cb][0], acc[rb][cb][1]), cvtpk(acc[rb][cb][2], acc[rb][cb][3])};
          *(LAS u32x2*)(size_t)(yw + rb * 16 * 336 + cb * 32) = w;
        }
    }
    BAR();
    {
      const __amdgpu_buffer_rsrc_t rsP = __builtin_amdgcn_make_buffer_rsrc(ws + WS_P, (short)0, 2 * 8 * 4 * 8 * 30720, 0x00020000);
#pragma unroll
      for (int i = 0; i < 8; ++i) {
        const int j = wave + 8 * i;
        if (j < 60) {
          const int n = 64 * j + ln, row = n / 20, pc = n % 20, un = j >= 30 ? 1 : 0;
          const u32x4 v = *(const LAS u32x4*)(size_t)(L0 + row * 336 + pc * 16);
          __builtin_amdgcn_raw_buffer_store_b128(v, rsP, (((un * 8 + b0) * 4 + hq) * 8 + xj) * 30720 + (n - 1920 * un) * 16, 0, 16);
        }
      }
      WAITV(0);
      BAR();
      if (wave == 0) {
        if (ln < 8) __hip_atomic_store(FL + (size_t)((int)ln + 8 * xi) * 32 + 8 + xj, TAG, __ATOMIC_RELAXED, __HIP_MEMORY_SCOPE_AGENT);
        poll_flags(FL + (size_t)blockIdx.x * 32 + 8, 8, -1, TAG, ln);
      }
      BAR();
      STAMP(9);
      const int tz = wave * 64 + (int)ln;
      if (tz < 480) {
        const int u = tz / 240, e = tz % 240;
        const int pp = (((u * 8 + b0) * 4 + hq) * 8) * 30720 + 12 * xj * 320 + e * 16;
        u32x4 v[8];
#pragma unroll
        for (int j = 0; j < 8; ++j) v[j] = __builtin_amdgcn_raw_buffer_load_b128(rsP, pp + j * 30720, 0, 16);
        float a[8];
#pragma unroll
        for (int k = 0; k < 4; ++k) {
          float lo = 0.f, hi2 = 0.f;
#pragma unroll
          for (int j = 0; j < 8; ++j) { lo += __uint_as_float(v[j][k] << 16); hi2 += __uint_as_float(v[j][k] & 0xffff0000u); }
          a[2 * k] = lo; a[2 * k + 1] = hi2;
        }
        u32x4 w = {cvtpk(a[0], a[1]), cvtpk(a[2], a[3]), cvtpk(a[4], a[5]), cvtpk(a[6], a[7])};
        const int row = e / 20, c8 = e % 20;
        *(u32x4*)(out + (size_t)((b0 + 8 * u) * SQ + 12 * xj + row) * DM + hq * 160 + c8 * 8) = w;
      }
    }
    STAMP(10);
  }
}

extern "C" void kernel_launch(void* const* d_in, const int* in_sizes, int n_in, void* d_out, int out_size, void* d_ws,
                              size_t ws_size, hipStream_t stream) {
  const u16* x = (const u16*)d_in[0];
  const u16* kc = (const u16*)d_in[1];
  const u16* vc = (const u16*)d_in[2];
  const u16* wq = (const u16*)d_in[3];
  const u16* wo = (const u16*)d_in[4];
  (void)hipFuncSetAttribute((const void*)attn_kernel, hipFuncAttributeMaxDynamicSharedMemorySize, LDS_A);
  attn_kernel<<<256, 512, LDS_A, stream>>>(x, kc, vc, wq, wo, (u16*)d_out, (char*)d_ws, (unsigned long long*)((char*)d_ws + (128u << 20)));
}
```

```cpp
#include <hip/hip_runtime.h>
#include <hip/hip_bf16.h>
#include <stdint.h>
#include <type_traits>

typedef unsigned short u16;
typedef short bf16x8 __attribute__((ext_vector_type(8)));
typedef short s16x4 __attribute__((ext_vector_type(4)));
typedef float f32x16 __attribute__((ext_vector_type(16)));
typedef float f32x4 __attribute__((ext_vector_type(4)));
typedef float f32x2 __attribute__((ext_vector_type(2)));
typedef __bf16 bf16x2 __attribute__((ext_vector_type(2)));
typedef unsigned u32x4 __attribute__((ext_vector_type(4)));
typedef unsigned u32x2 __attribute__((ext_vector_type(2)));
#define LAS __attribute__((address_space(3)))
#define DI __device__ __forceinline__

constexpr int NB = 16, SQ = 96, DM = 640, NH = 32, SK = 1024, DH = 192;
constexpr int HD = NH * DH;

#define MFMA32(a, b, c) __builtin_amdgcn_mfma_f32_32x32x16_bf16((a), (b), (c), 0, 0, 0)
#define MFMA16(a, b, c) __builtin_amdgcn_mfma_f32_16x16x32_bf16((a), (b), (c), 0, 0, 0)
#define BAR() do { asm volatile("" ::: "memory"); __builtin_amdgcn_s_waitcnt(0xC07F); __builtin_amdgcn_s_barrier(); asm volatile("" ::: "memory"); } while (0)
#define WAITV(n) asm volatile("s_waitcnt vmcnt(%0)" ::"i"(n) : "memory")
#define SBAR() __builtin_amdgcn_sched_barrier(0)
#define MAX3(a, b, c) fmaxf(fmaxf((a), (b)), (c))
#define LANEID(v) asm volatile("v_mbcnt_lo_u32_b32 %0, -1, 0\n\tv_mbcnt_hi_u32_b32 %0, -1, %0" : "=v"(v))

DI unsigned cvtpk(float lo, float hi) {
  f32x2 v = {lo, hi};
  bf16x2 b = __builtin_convertvector(v, bf16x2);
  return __builtin_bit_cast(unsigned, b);
}
DI void dma16nt(unsigned lds_addr, const void* sbase, unsigned voff) {
  asm volatile("s_mov_b32 m0, %0\n\ts_nop 0\n\tglobal_load_lds_dwordx4 %1, %2 nt" ::"s"(lds_addr), "v"(voff), "s"(sbase) : "memory");
}
DI void bdma(unsigned lds_addr, __amdgpu_buffer_rsrc_t rs, unsigned voff, unsigned soff) {
  asm volatile("s_mov_b32 m0, %0\n\ts_nop 0\n\tbuffer_load_dwordx4 %1, %2, %3 offen lds" ::"s"(lds_addr), "v"(voff), "s"(rs), "s"(soff) : "memory");
}
DI void bdma1(unsigned lds_addr, __amdgpu_buffer_rsrc_t rs, unsigned voff, unsigned soff) {
  asm volatile("s_mov_b32 m0, %0\n\ts_nop 0\n\tbuffer_load_dwordx4 %1, %2, %3 offen sc1 lds" ::"s"(lds_addr), "v"(voff), "s"(rs), "s"(soff) : "memory");
}
DI void dma16(unsigned lds_addr, const void* sbase, unsigned voff) {
  asm volatile("s_mov_b32 m0, %0\n\ts_nop 0\n\tglobal_load_lds_dwordx4 %1, %2" ::"s"(lds_addr), "v"(voff), "s"(sbase) : "memory");
}
template <int I, int N, class F>
DI void sfor(F&& f) {
  if constexpr (I < N) { f(std::integral_constant<int, I>{}); sfor<I + 1, N>(f); }
}
DI bf16x8 lds128(unsigned a) { return *(const LAS bf16x8*)(size_t)a; }
DI s16x4 ldstr(unsigned a) { return __builtin_amdgcn_ds_read_tr16_b64_v4i16((LAS s16x4*)(size_t)a); }
DI bf16x8 cat(s16x4 a, s16x4 b) { return __builtin_shufflevector(a, b, 0, 1, 2, 3, 4, 5, 6, 7); }

constexpr unsigned ITB = 12288;
constexpr unsigned NSL = 5;
constexpr unsigned GRB = NSL * ITB;
constexpr unsigned SIDE = 2 * GRB;
constexpr unsigned MISC = SIDE + 36864;
constexpr unsigned CTL = MISC + 2048;
constexpr unsigned HB = MISC - 49152;
constexpr unsigned C_L = CTL;
constexpr unsigned C_R = CTL + 16;
constexpr unsigned C_QRDY = CTL + 48;
constexpr unsigned C_XF = CTL + 64;
constexpr unsigned C_H = CTL + 128;
constexpr unsigned C_OR = CTL + 160;
constexpr unsigned C_OD = CTL + 192;
constexpr unsigned LDS_A = 163840;
constexpr float CSC = 0.07216878364870322f * 1.4426950408889634f;
constexpr float THR_RAW = 8.0f / CSC;
constexpr int SPIN_MAX = 1 << 20;
#define HD_D 4
#define TL_D 4
#define KV0 2

#define STAMP(i)
#define EST(i)
#define FST(i)

DI void lds_post(unsigned a, unsigned v) {
  asm volatile("s_waitcnt lgkmcnt(0)\n\tds_write_b32 %0, %1" ::"v"(a), "v"(v) : "memory");
}
DI void lds_post_nw(unsigned a, unsigned v) {
  asm volatile("ds_write_b32 %0, %1" ::"v"(a), "v"(v) : "memory");
}
DI void lds_wait_ge(unsigned a, unsigned n) {
#pragma nounroll
  for (int it = 0; it < SPIN_MAX; ++it) {
    unsigned v;
    asm volatile("ds_read_b32 %0, %1\n\ts_waitcnt lgkmcnt(0)" : "=v"(v) : "v"(a) : "memory");
    if (__builtin_amdgcn_readfirstlane(v) >= n) break;
    __builtin_amdgcn_s_sleep(1);
  }
}
DI void lds_wait_min4(unsigned a, unsigned n) {
#pragma nounroll
  for (int it = 0; it < SPIN_MAX; ++it) {
    u32x4 d;
    asm volatile("ds_read_b128 %0, %1\n\ts_waitcnt lgkmcnt(0)" : "=v"(d) : "v"(a) : "memory");
    const unsigned mn = min(min(d[0], d[1]), min(d[2], d[3]));
    if (__builtin_amdgcn_readfirstlane(mn) >= n) break;
    __builtin_amdgcn_s_sleep(1);
  }
}
DI unsigned getvm() {
  unsigned sts;
  asm volatile("s_getreg_b32 %0, hwreg(HW_REG_IB_STS)" : "=s"(sts)::"memory");
  return (sts & 15u) | ((sts >> 18) & 0x30u);
}
DI unsigned lds_peek(unsigned a) { return *(volatile LAS unsigned*)(size_t)a; }

typedef __attribute__((address_space(1))) unsigned long long gu64;
extern "C" __device__ unsigned long long amd_dispatch_id() __asm("llvm.amdgcn.dispatch.id");
constexpr size_t WS_O = 0, WS_P = 32u << 20, WS_F = 96u << 20;

DI bool poll_flags(gu64* f, int nl, int skip, unsigned long long T, int lane) {
#pragma nounroll
  for (int it = 0; it < SPIN_MAX; ++it) {
    unsigned long long v = T;
    if (lane < nl && lane != skip) v = __hip_atomic_load(f + lane, __ATOMIC_RELAXED, __HIP_MEMORY_SCOPE_AGENT);
    if (__all(v == T)) {
      if (lane < nl && lane != skip) __hip_atomic_store(f + lane, 0ull, __ATOMIC_RELAXED, __HIP_MEMORY_SCOPE_AGENT);
      return true;
    }
    __builtin_amdgcn_s_sleep(2);
  }
  return false;
}

constexpr int hNL(int j) { return j < 20 ? 3 : 0; }
constexpr int hKV(int p) { return (p >= KV0 && p < KV0 + 12) ? 1 : 0; }
constexpr int hPend(int j) {
  int cnt = 0, first = 0;
  if (j < HD_D) { cnt = 3 * (HD_D - 1 - j); first = 0; }
  else { cnt = hKV(j - HD_D); first = j - HD_D + 1; }
  for (int p = first; p < j; ++p) cnt += hNL(p + HD_D) + hKV(p);
  return cnt;
}
constexpr int tNL(int j) { return j < 24 ? 3 : 0; }
constexpr int tPend(int j) {
  int cnt = 0, first = 0;
  if (j < TL_D) { cnt = 3 * (TL_D - 1 - j); first = 0; }
  else { cnt = 0; first = j - TL_D + 1; }
  for (int p = first; p < j; ++p) cnt += tNL(p + TL_D);
  return cnt;
}

__global__ __launch_bounds__(512, 2) void attn_kernel(const u16* x, const u16* kc, const u16* vc, const u16* wq, const u16* wo,
                                                      u16* out, char* ws, unsigned long long* stamps) {
  extern __shared__ __attribute__((aligned(16))) char lds[];
  const unsigned L0 = (unsigned)(size_t)(LAS char*)lds;
  const int tid = threadIdx.x, lane = tid & 63;
  const int wave = __builtin_amdgcn_readfirstlane(tid >> 6);
  const int xj = blockIdx.x & 7, xi = blockIdx.x >> 3;
  const int hq = xi & 3, h = 4 * xj + hq, b0 = xi >> 2;
  const size_t UB0 = (size_t)(b0 * NH + h) * SK * 384, UB1 = (size_t)((b0 + 8) * NH + h) * SK * 384;
  unsigned long long TAG;
  {
    const unsigned long long did = amd_dispatch_id(), dp = (unsigned long long)__builtin_amdgcn_dispatch_ptr();
    const unsigned t32 = (unsigned)(did * 0x9E3779B1ull) ^ (unsigned)(dp >> 6) * 0x85EBCA77u ^ (unsigned)(did >> 32);
    TAG = ((unsigned long long)(t32 ^ 0x5A17C3E9u) << 32) | t32;
  }
  const int qbase = xj + 32 * b0;
  gu64* FL = (gu64*)(ws + WS_F);
  const int kh = (0x1A >> wave) & 1, r = (0x102210 >> (4 * wave)) & 3;
  STAMP(0);
  unsigned cpf = 0;
  if (wave == 7 && (xi & 7) < 6) {
    const char* pc;
    asm volatile("s_getpc_b64 %0" : "=s"(pc));
    asm volatile("global_load_dword %0, %1, %2" : "=v"(cpf) : "v"((unsigned)((xi & 7) * 8192 + lane * 128)), "s"(pc) : "memory");
  }
  bf16x8 qf[12];

  unsigned offK[6], offV[3];
  {
    if (wave == 6 && lane < 64) *(LAS unsigned*)(size_t)(L0 + CTL + lane * 4) = (lane == 46 || lane == 47) ? 1u : 0u;
    const int rot = (b0 * 5 / 4 + hq * 5 / 2) % 10;
    const int dh = wave & 1, rq = wave >> 1;
    const unsigned g = lane >> 4, q4 = (lane & 15) >> 2, p4 = lane & 3, l15 = lane & 15;
    const __amdgpu_buffer_rsrc_t rsW = __builtin_amdgcn_make_buffer_rsrc((void*)((const char*)wq + h * DH * 2), (short)0, DM * HD * 2 - h * DH * 2, 0x00020000);
    const __amdgpu_buffer_rsrc_t rsX = __builtin_amdgcn_make_buffer_rsrc((void*)((const char*)x + b0 * SQ * DM * 2), (short)0, (NB - b0) * SQ * DM * 2, 0x00020000);
    unsigned gA, gB, gC;
    {
      auto wpiece = [&](int p) {
        const int n = 64 * p + lane, row = n / 24, pc = n % 24;
        const int sw = ((row >> 1) & 1) | (((row >> 3) & 1) << 1);
        return (unsigned)(row * (HD * 2) + ((pc ^ (sw << 1)) * 16));
      };
      auto xpiece = [&](int p) {
        const int row = 8 * p + (lane >> 3);
        return (unsigned)(row * (DM * 2) + (((lane & 7) ^ ((row >> 1) & 7)) * 16));
      };
      gA = wpiece(wave);
      gB = wave < 4 ? wpiece(8 + wave) : xpiece(wave - 4);
      gC = xpiece(4 + wave);
    }
#pragma unroll
    for (int j = 0; j < 6; ++j) {
      int n = 64 * j + lane, row = n / 24, pc = n % 24;
      offK[j] = row * 384 + ((pc ^ ((row >> 1) & 7)) * 16);
    }
#pragma unroll
    for (int j = 0; j < 3; ++j) {
      int n = 64 * j + lane, row = n / 24, pc = n % 24;
      offV[j] = row * 384 + ((pc ^ (((row >> 1) & 1) << 2)) * 16);
    }
    unsigned hK[3], hV[3];
#pragma unroll
    for (int j = 0; j < 3; ++j) {
      const int n = 64 * (3 * (wave & 3) + j) + lane, row = n / 24, pc = n % 24;
      hK[j] = row * 384 + ((pc ^ ((row >> 1) & 7)) * 16);
      hV[j] = row * 384 + ((pc ^ (((row >> 1) & 1) << 2)) * 16);
    }
    constexpr unsigned WB = 3 * ITB, G1X = GRB + 2 * ITB, U1 = 8 * SQ * DM * 2;
    constexpr unsigned WSTEP = 32 * HD * 2;
    const unsigned mW = L0 + WB + wave * 1024;
    const unsigned mX = L0 + (4 + wave) * 1024;
    const unsigned s2 = ((q4 >> 1) & 1) | ((g & 1) << 1);
    unsigned E[4];
#pragma unroll
    for (int j = 0; j < 4; ++j) E[j] = L0 + WB + (8 * g + q4) * 384 + 32 * ((unsigned)(6 * dh + j) ^ s2) + 8 * p4;
    const unsigned XA0 = L0 + (rq >> 1) * G1X + (48 * (rq & 1) + l15) * 128 + ((g ^ ((l15 >> 1) & 7)) * 16), XA1 = XA0 ^ 64;
    f32x4 acc[6][3] = {};
    bf16x8 af[2][6], bfr[2][3];
    unsigned xa;
    auto rdA = [&](int par, int c1, int db) {
      const unsigned a = E[db & 3] + (db >> 2) * 128 + (c1 & 3) * ITB;
      af[par][db] = cat(ldstr(a), ldstr(a + 4 * 384));
    };
    auto rdB = [&](int par, int rb) { bfr[par][rb] = lds128(xa + rb * 2048); };
    unsigned woff = (unsigned)(2 * rot) * WSTEP, xoff = (unsigned)rot * 128;
    auto advW = [&]() { woff += WSTEP; woff = woff == 20 * WSTEP ? 0u : woff; };
    auto advX = [&]() { xoff += 128; xoff = xoff == 1280 ? 0u : xoff; };
    auto request = [&](auto VV, int k, int wsl, unsigned xs, int un) {
      constexpr int V = decltype(VV)::value;
      if (k == 0) bdma(mW + wsl * ITB, rsW, gA, woff);
      if (k == 1) {
        if (V == 0) bdma(mW + 8192 + wsl * ITB, rsW, gB, woff);
        else bdma(mX - 8192 + un * G1X + xs, rsX, gB, xoff + un * U1);
      }
      if (k == 2) bdma(mX + un * G1X + xs, rsX, gC, xoff + un * U1);
    };
    unsigned xr = 2 * ITB;
    unsigned xw = 1 * ITB;
    auto body = [&](auto VV) {
      constexpr int V = decltype(VV)::value;
      if (V == 1) bdma(mX - 8192 + 2 * ITB, rsX, gB, xoff);
      bdma(mX + 2 * ITB, rsX, gC, xoff);
#pragma unroll
      for (int c = -4; c < 0; ++c) {
        if ((c & 1) != 0) advX();
        const unsigned xs = (unsigned)((((c + 1) >> 1) + 2 + 2) % 3) * ITB;
#pragma unroll
        for (int k = 0; k < 3; ++k) request(VV, k, c & 3, xs, (c + 1) & 1);
        advW();
      }
      WAITV(9);
      BAR();
      xa = XA0 + 2 * ITB;
#pragma unroll
      for (int rb = 0; rb < 3; ++rb) rdB(0, rb);
#pragma unroll
      for (int db = 0; db < 6; ++db) rdA(0, 0, db);
      WAITV(6);
      BAR();
      STAMP(4);
      auto phase = [&](int c, auto QQ, auto MODE) {
        constexpr int q = decltype(QQ)::value, mode = decltype(MODE)::value, cur = q & 1;
        if (cur == 1) {
          xr = xr == 2 * ITB ? 0 : xr + ITB;
          if (mode == 0) { xw = xw == 2 * ITB ? 0 : xw + ITB; advX(); }
        }
        xa = (cur == 1 ? XA0 : XA1) + xr;
        constexpr int R0 = V == 0 ? 0 : 5, D0 = V == 0 ? 10 : 0;
#pragma unroll
        for (int i = 0; i < 18; ++i) {
          acc[i / 3][i % 3] = MFMA16(af[cur][i / 3], bfr[cur][i % 3], acc[i / 3][i % 3]);
          SBAR();
          if (i >= R0 && i < R0 + 3) rdB(cur ^ 1, i - R0);
          if (i >= R0 + 3 && i < R0 + 9) rdA(cur ^ 1, q + 1, i - R0 - 3);
          if (mode == 0 && i >= D0 && i < D0 + 6 && !((i - D0) & 1)) request(VV, (i - D0) >> 1, q, xw, (q + 1) & 1);
          if (mode == 0 && i == D0 + 5) advW();
          if (mode == 2 && i >= D0 && i < D0 + 6 && !((i - D0) & 1)) {
            const int k = (i - D0) >> 1, li = c - 17;
            const unsigned dst = L0 + V * G1X + li * ITB + (3 * (wave & 3) + k) * 1024;
            if (li == 2) dma16nt(dst, (const char*)vc + UB0 + (size_t)(32 * V) * 384, hV[k]);
            else dma16nt(dst, (const char*)kc + UB0 + (size_t)(64 * li + 32 * V) * 384, hK[k]);
          }
          SBAR();
        }
        if (mode == 0) WAITV(6);
        if (mode == 1) WAITV(0);
        BAR();
      };
      using I0 = std::integral_constant<int, 0>; using I1 = std::integral_constant<int, 1>;
      using I2 = std::integral_constant<int, 2>; using I3 = std::integral_constant<int, 3>;
#pragma unroll 1
      for (int c = 0; c < 16; c += 4) {
        phase(c, I0{}, I0{});
        phase(c + 1, I1{}, I0{});
        phase(c + 2, I2{}, I0{});
        phase(c + 3, I3{}, I0{});
      }
      STAMP(5);
      phase(16, I0{}, I1{});
      phase(17, I1{}, I2{});
      phase(18, I2{}, I2{});
      phase(19, I3{}, I2{});
    };
    if (wave < 4) body(std::integral_constant<int, 0>{}); else body(std::integral_constant<int, 1>{});
    asm volatile("" ::"v"(cpf));
    STAMP(11);
    auto qstore = [&]() {
      unsigned lz; LANEID(lz);
      const unsigned zg = lz >> 4, zl = lz & 15;
#pragma unroll
      for (int rb = 0; rb < 3; ++rb) {
        const int R16 = 3 * (rq & 1) + rb;
        const unsigned qb = L0 + SIDE + ((R16 >> 1) * 12 + 6 * dh) * 1024 + (16 * (R16 & 1) + zl + 32 * (zg >> 1)) * 16 + 8 * (zg & 1);
#pragma unroll
        for (int db = 0; db < 6; ++db) {
          u32x2 w = {cvtpk(acc[db][rb][0], acc[db][rb][1]), cvtpk(acc[db][rb][2], acc[db][rb][3])};
          *(LAS u32x2*)(size_t)(qb + db * 1024) = w;
        }
      }
    };
    if (wave < 4) qstore();
    BAR();
    if (wave < 6) {
      unsigned lz; LANEID(lz);
#pragma unroll
      for (int ks = 0; ks < 12; ++ks) qf[ks] = lds128(L0 + SIDE + ((r * 12 + ks) * 64 + lz) * 16);
    }
    BAR();
    if (wave >= 4) { qstore(); lds_post(L0 + C_QRDY + 4 * (wave - 4), 1); }
  }
  STAMP(1);

  if (wave >= 6) {
    const int hw = wave - 6;
    __builtin_amdgcn_s_setprio(3);
    unsigned ll; LANEID(ll);
    const unsigned aL = L0 + C_L + 4 * hw, aR = L0 + C_R + 16 * hw;
    auto issue_item = [&](unsigned li_, unsigned slot_) {
      const unsigned j = li_ & 31;
      const bool isK = (j == 0) || ((j & 1) && j < 31);
      const unsigned tile = isK ? (j + 1) >> 1 : (j - 1) >> 1;
      const size_t ub = ((li_ >> 5) ? UB1 : UB0) + (size_t)(64 * tile + 32 * hw) * 384;
      const unsigned dst = L0 + hw * GRB + slot_;
      if (isK) {
        const char* src = (const char*)kc + ub;
#pragma unroll
        for (int q = 0; q < 12; ++q) dma16nt(dst + q * 1024, src + (q / 6) * 6144, offK[q % 6]);
      } else {
        const char* src = (const char*)vc + ub;
#pragma unroll
        for (int q = 0; q < 12; ++q) dma16nt(dst + q * 1024, src + (q / 3) * 3072, offV[q % 3]);
      }
    };
    issue_item(3, hw ? 0 : 3 * ITB);
    issue_item(4, hw ? ITB : 4 * ITB);
    WAITV(30); lds_post_nw(L0 + C_H + 4 * wave, 1); lds_wait_min4(L0 + C_H + 16 * hw, 1); lds_post_nw(aL, 1);
    WAITV(27); lds_post_nw(L0 + C_H + 4 * wave, 2); lds_wait_min4(L0 + C_H + 16 * hw, 2); lds_post_nw(aL, 2);
    WAITV(24); lds_post_nw(L0 + C_H + 4 * wave, 3); lds_wait_min4(L0 + C_H + 16 * hw, 3); lds_post_nw(aL, 3);
    unsigned li = 5, lpub = 3, slot = hw ? 2 * ITB : 0;
    unsigned sb = 0, ost = 0;
    bool ordy = false;
    const __amdgpu_buffer_rsrc_t rsO = __builtin_amdgcn_make_buffer_rsrc(ws + WS_O, (short)0, 2 * 256 * 36864, 0x00020000);
    auto obatch = [&]() {
#pragma unroll
      for (int k = 0; k < 3; ++k) {
        const unsigned p = 18 * hw + 3 * ost + k, rr = p / 12, q = p % 12;
        const unsigned c6 = (0x305241u >> (4 * (q >> 1))) & 7;
        const u32x4 v = *(const LAS u32x4*)(size_t)(L0 + SIDE + p * 1024 + ll * 16);
        __builtin_amdgcn_raw_buffer_store_b128(v, rsO, (int)(blockIdx.x * 36864 + c6 * 6144 + rr * 2048 + (q & 1) * 1024 + ll * 16), 0, 16);
      }
      ++ost;
      if (ost == 6) lds_post(L0 + C_OD + 4 * hw, 1);
    };
    auto ocheck = [&]() {
      u32x4 d0, d1;
      asm volatile("ds_read_b128 %0, %2\n\tds_read_b128 %1, %2 offset:16\n\ts_waitcnt lgkmcnt(0)" : "=&v"(d0), "=&v"(d1) : "v"(L0 + C_OR) : "memory");
      const unsigned mn = min(min(min(d0[0], d0[1]), min(d0[2], d0[3])), min(min(d1[0], d1[1]), min(d1[2], d1[3])));
      return __builtin_amdgcn_readfirstlane(mn) >= 1;
    };
#pragma nounroll
    for (int it = 0; it < (SPIN_MAX << 2); ++it) {
      bool idle = true;
      if (li < 64) {
        u32x4 d;
        asm volatile("ds_read_b128 %0, %1\n\ts_waitcnt lgkmcnt(0)" : "=v"(d) : "v"(aR) : "memory");
        const unsigned mn = __builtin_amdgcn_readfirstlane(min(min(d[0], d[1]), d[2]));
        if (mn + NSL > li && getvm() <= 48) {
          issue_item(li, slot);
          if (li >= 32 && ost < 6) {
            if (!ordy) ordy = ocheck();
            if (ordy) { obatch(); sb |= 1u << (li & 31); }
          }
          ++li;
          slot = slot + ITB == GRB ? 0 : slot + ITB;
          idle = false;
        }
      }
      if (lpub < li) {
        const unsigned vm = getvm();
        unsigned done = lpub;
#pragma nounroll
        while (done < li) {
          const unsigned n = li - done;
          const unsigned rotm = (sb >> (done & 31)) | (sb << ((32 - (done & 31)) & 31));
          const unsigned after = 12 * (n - 1) + 3 * __builtin_popcount(rotm & ((1u << n) - 1));
          if (vm > after) break;
          sb &= ~(1u << (done & 31));
          ++done;
        }
        if (done > lpub) {
          lpub = done;
          lds_post_nw(aL, lpub);
          idle = false;
        }
      } else if (li == 64) {
        break;
      }
      if (idle) __builtin_amdgcn_s_sleep(1);
    }
    __builtin_amdgcn_s_setprio(0);
#pragma nounroll
    for (int it = 0; it < SPIN_MAX && ost < 6; ++it) { if (ocheck()) obatch(); else __builtin_amdgcn_s_sleep(1); }
    WAITV(0);
    lds_post(L0 + C_OD + 4 * hw, 2);
    {
      unsigned a = 0;
#pragma unroll
      for (int i = 0; i < 4; ++i) {
        const unsigned n = ll + 64 * i, row = n / 5, sg = n % 5;
        if (n < 240) a ^= *(const unsigned*)((const char*)wo + (size_t)(4 * xj * DH + 96 * b0 + 48 * hw + row) * (DM * 2) + hq * 320 + sg * 64);
      }
      asm volatile("" ::"v"(a));
    }
    {
      constexpr unsigned TWO = 10240, TOB = 61440;
      u32x4 d;
#pragma nounroll
      for (int it = 0; it < SPIN_MAX; ++it) {
        asm volatile("ds_read_b128 %0, %1\n\ts_waitcnt lgkmcnt(0)" : "=v"(d) : "v"(aR) : "memory");
        if (__builtin_amdgcn_readfirstlane(min(min(d[0], d[1]), d[2])) >= 64) break;
        __builtin_amdgcn_s_sleep(1);
      }
      if (hw == 0) {
        const __amdgpu_buffer_rsrc_t rsWo = __builtin_amdgcn_make_buffer_rsrc((void*)((const char*)wo + 4 * xj * DH * DM * 2 + hq * 320), (short)0, HD * DM * 2 - (4 * xj * DH * DM * 2 + hq * 320), 0x00020000);
#pragma unroll
        for (int p = 0; p < 10; ++p) {
          const int n = 64 * p + ll, row = n / 20, pc = n % 20;
          const unsigned go = row * (DM * 2) + ((pc ^ (((row >> 3) & 1) << 1)) * 16);
#pragma unroll
          for (int c6 = 0; c6 < 6; ++c6) bdma(L0 + c6 * TWO + p * 1024, rsWo, go, (hq * DH + 32 * c6) * (DM * 2));
        }
      } else {
        lds_wait_ge(L0 + C_OD, 2);
#pragma unroll
        for (int c6 = 0; c6 < 6; ++c6)
#pragma unroll
          for (int p = 0; p < 6; ++p) bdma1(L0 + TOB + c6 * ITB + p * 1024, rsO, ll * 16, blockIdx.x * 36864 + c6 * 6144 + p * 1024);
      }
    }
  } else {
    unsigned ln; LANEID(ln);
    unsigned kof[4], vE, vO;
    {
      const unsigned a32 = ln & 31, ahi = ln >> 5, ablk = (ln >> 4) & 1, aq4 = (ln & 15) >> 2, ap4 = ln & 3, axq = (aq4 >> 1) & 1;
#pragma unroll
      for (int i = 0; i < 4; ++i) kof[i] = L0 + kh * GRB + a32 * 384 + (((2 * i + ahi) ^ ((a32 >> 1) & 7)) * 16);
      vE = L0 + kh * GRB + (4 * ahi + aq4) * 384 + 64 * axq + 32 * ablk + 8 * ap4;
      vO = L0 + kh * GRB + (4 * ahi + aq4) * 384 - 64 * axq + 32 * ablk + 8 * ap4;
    }
    const unsigned aF = L0 + C_L + 4 * kh, aR = L0 + C_R + 16 * kh + 4 * r;
    const int pw = (0x102354 >> (4 * wave)) & 7;
    const unsigned fme = L0 + C_XF + 8 * wave, fpa = L0 + C_XF + 8 * pw;
    unsigned cur = kh ? 2 * ITB : 0;
    unsigned cnt = 0;
#define ADV() do { cur = (cur + ITB == GRB) ? 0u : cur + ITB; ++cnt; } while (0)
#define KADDR(ks) (((ks) & 3) == 0 ? k0 : ((ks) & 3) == 1 ? k1 : ((ks) & 3) == 2 ? k2 : k3) + 128 * ((ks) >> 2)
#define VADDR(j) ((((j) % 6) & 1) ? vbO : vbE) + (16 * ((j) / 6)) * 384 + 64 * ((j) % 6)

#pragma unroll 1
    for (int u = 0; u < 2; ++u) {
      float m, l = 0.f;
      f32x16 o[6] = {};
      f32x16 sB;
      float pe[16];
      bf16x8 kf[12], vf[12];
      unsigned k0, k1, k2, k3, vbE, vbO;
      if (u == 0) { WAITV(6); lds_post_nw(L0 + C_H + 4 * wave, 1); }
      lds_wait_ge(aF, cnt + 1);
      k0 = kof[0] + cur; k1 = kof[1] + cur; k2 = kof[2] + cur; k3 = kof[3] + cur;
#pragma unroll
      for (int ks = 0; ks < 12; ++ks) kf[ks] = lds128(KADDR(ks));
      ADV();
      lds_post_nw(aR, cnt);
      SBAR();
      {
        f32x16 z = {};
        sB = MFMA32(kf[0], qf[0], z);
#pragma unroll
        for (int ks = 1; ks < 12; ++ks) sB = MFMA32(kf[ks], qf[ks], sB);
      }
      SBAR();
      if (u == 0) { WAITV(3); lds_post_nw(L0 + C_H + 4 * wave, 2); }
      lds_wait_ge(aF, cnt + 1);
      if (u == 0) { WAITV(0); lds_post_nw(L0 + C_H + 4 * wave, 3); }
      k0 = kof[0] + cur; k1 = kof[1] + cur; k2 = kof[2] + cur; k3 = kof[3] + cur;
#pragma unroll
      for (int ks = 0; ks < 4; ++ks) kf[ks] = lds128(KADDR(ks));
      {
        float pmax = sB[0];
#pragma unroll
        for (int i = 1; i < 16; ++i) pmax = fmaxf(pmax, sB[i]);
        auto rr = __builtin_amdgcn_permlane32_swap(__float_as_uint(pmax), __float_as_uint(pmax), false, false);
        m = fmaxf(__uint_as_float(rr[0]), __uint_as_float(rr[1]));
        const float mc = -m * CSC;
#pragma unroll
        for (int i = 0; i < 16; ++i) pe[i] = fmaf(sB[i], CSC, mc);
      }
      SBAR();
      bf16x8 pa[2];
      auto expstep = [&](int i) {
        pe[2 * i] = __builtin_amdgcn_exp2f(pe[2 * i]);
        pe[2 * i + 1] = __builtin_amdgcn_exp2f(pe[2 * i + 1]);
        l += pe[2 * i];
        l += pe[2 * i + 1];
        asm volatile("" : "+v"(pe[2 * i]), "+v"(pe[2 * i + 1]), "+v"(l));
      };
      auto cvtstep = [&](int s) {
        u32x4 w = {cvtpk(pe[8 * s], pe[8 * s + 1]), cvtpk(pe[8 * s + 2], pe[8 * s + 3]), cvtpk(pe[8 * s + 4], pe[8 * s + 5]),
                   cvtpk(pe[8 * s + 6], pe[8 * s + 7])};
        asm volatile("" : "+v"(w));
        pa[s] = __builtin_bit_cast(bf16x8, w);
      };
#pragma unroll 1
      for (int t = 0; t < 15; ++t) {
        unsigned fl = 0;
#pragma unroll
        for (int i = 0; i < 12; ++i) {
          if (i == 0) { f32x16 z = {}; sB = MFMA32(kf[0], qf[0], z); }
          else sB = MFMA32(kf[i], qf[i], sB);
          if (i < 8) kf[i + 4] = lds128(KADDR(i + 4));
          if (i == 2) fl = lds_peek(aF);
          if (i == 7) { ADV(); lds_post_nw(aR, cnt); }
          if (i < 8) expstep(i);
          if (i == 8) {
            if (__builtin_amdgcn_readfirstlane(fl) < cnt + 1) lds_wait_ge(aF, cnt + 1);
            vbE = vE + cur; vbO = vO + cur;
            cvtstep(0);
          }
          if (i == 9) cvtstep(1);
          if (i >= 8) vf[i - 8] = cat(ldstr(VADDR(i - 8)), ldstr(VADDR(i - 8) + 8 * 384));
          SBAR();
        }
        float pmax = 0.f, alpha = 1.f, mc = 0.f;
        bool resc = false;
#pragma unroll
        for (int j = 0; j < 12; ++j) {
          o[j % 6] = MFMA32(vf[j], pa[j / 6], o[j % 6]);
          if (j < 8) vf[j + 4] = cat(ldstr(VADDR(j + 4)), ldstr(VADDR(j + 4) + 8 * 384));
          if (j == 2) fl = lds_peek(aF);
          if (j == 7) { ADV(); lds_post_nw(aR, cnt); }
          if (j == 1) {
            asm volatile("" : "+v"(sB));
            pmax = MAX3(MAX3(sB[0], sB[1], sB[2]), MAX3(sB[3], sB[4], sB[5]), MAX3(sB[6], sB[7], sB[8]));
            asm volatile("" : "+v"(pmax));
          }
          if (j == 2) {
            pmax = MAX3(MAX3(sB[9], sB[10], sB[11]), MAX3(sB[12], sB[13], sB[14]), MAX3(sB[15], sB[15], pmax));
            asm volatile("" : "+v"(pmax));
          }
          if (j == 3) {
            auto rr = __builtin_amdgcn_permlane32_swap(__float_as_uint(pmax), __float_as_uint(pmax), false, false);
            pmax = fmaxf(__uint_as_float(rr[0]), __uint_as_float(rr[1]));
            resc = pmax - m > THR_RAW;
            const float mn = resc ? pmax : m;
            alpha = __builtin_amdgcn_exp2f((m - mn) * CSC);
            m = mn;
            mc = -mn * CSC;
            l *= alpha;
            asm volatile("" : "+v"(m), "+v"(mc), "+v"(l), "+v"(alpha));
          }
          if (j >= 4 && j < 8) {
#pragma unroll
            for (int i = 4 * (j - 4); i < 4 * (j - 4) + 4; ++i) pe[i] = fmaf(sB[i], CSC, mc);
            asm volatile("" : "+v"(pe[4 * (j - 4)]), "+v"(pe[4 * (j - 4) + 1]), "+v"(pe[4 * (j - 4) + 2]), "+v"(pe[4 * (j - 4) + 3]));
          }
          if (j == 8) {
            if (t < 14 && __builtin_amdgcn_readfirstlane(fl) < cnt + 1) lds_wait_ge(aF, cnt + 1);
            k0 = kof[0] + cur; k1 = kof[1] + cur; k2 = kof[2] + cur; k3 = kof[3] + cur;
          }
          if (j >= 8) kf[j - 8] = lds128(KADDR(j - 8));
          SBAR();
        }
        if (__any(resc)) {
#pragma unroll
          for (int d = 0; d < 6; ++d)
#pragma unroll
            for (int i = 0; i < 16; ++i) o[d][i] *= alpha;
        }
      }
      {
        lds_wait_ge(aF, cnt + 1);
        vbE = vE + cur; vbO = vO + cur;
#pragma unroll
        for (int j = 0; j < 6; ++j) vf[j] = cat(ldstr(VADDR(j)), ldstr(VADDR(j) + 8 * 384));
        SBAR();
#pragma unroll
        for (int i = 0; i < 8; ++i) expstep(i);
        cvtstep(0);
        cvtstep(1);
        SBAR();
#pragma unroll
        for (int j = 0; j < 12; ++j) {
          o[j % 6] = MFMA32(vf[j], pa[j / 6], o[j % 6]);
          if (j < 6) vf[j + 6] = cat(ldstr(VADDR(j + 6)), ldstr(VADDR(j + 6) + 8 * 384));
          if (j == 5) { ADV(); lds_post_nw(aR, cnt); }
          SBAR();
        }
        if (u == 0) {
          lds_wait_min4(L0 + C_QRDY, 1);
#pragma unroll
          for (int ks = 0; ks < 12; ++ks) qf[ks] = lds128(L0 + SIDE + ((r * 12 + ks) * 64 + ln) * 16);
        }
        lds_post(fme + 4, 4 * u + 1);
      }
      STAMP(2 + 4 * u);
      EST(0);
      {
        auto rr = __builtin_amdgcn_permlane32_swap(__float_as_uint(l), __float_as_uint(l), false, false);
        l = __uint_as_float(rr[0]) + __uint_as_float(rr[1]);
      }
      unsigned lz; LANEID(lz);
      const int zc = lz & 31, zh = lz >> 5;
      if (zh == 0) *(LAS f32x2*)(size_t)(L0 + MISC + (wave * 32 + zc) * 8) = f32x2{m, l};
      float ws_ = 0.f, wo_ = 0.f;
      const __amdgpu_buffer_rsrc_t rsO = __builtin_amdgcn_make_buffer_rsrc(ws + WS_O, (short)0, 2 * 256 * 36864, 0x00020000);
      const int osw = ((zc >> 2) & 1) << 1;
      const int obase = (256 + blockIdx.x) * 36864 + (32 * r + zc) * 64;
      const unsigned xb_ = L0 + SIDE + (u == 0 ? r * ITB : ITB + r * 8192);
      const unsigned xw = xb_ + kh * 4096 + lz * 16, xr = xb_ + (1 - kh) * 4096 + lz * 16;
      if (u == 1) {
        lds_wait_ge(L0 + C_OD, 1);
        lds_wait_ge(L0 + C_OD + 4, 1);
#pragma nounroll
        for (int it = 0; it < SPIN_MAX; ++it) {
          u32x4 d;
          asm volatile("ds_read_b128 %0, %1\n\ts_waitcnt lgkmcnt(0)" : "=v"(d) : "v"(L0 + C_R + 16) : "memory");
          if (__builtin_amdgcn_readfirstlane(min(min(d[0], d[1]), d[2])) >= 64) break;
          __builtin_amdgcn_s_sleep(1);
        }
      }
      EST(1);
      u32x4 hold[2][2];
#pragma unroll
      for (int rd = 0; rd < 3; ++rd) {
        lds_wait_ge(fpa + 4, 4 * u + rd + 1);
#pragma unroll
        for (int i = 0; i < 4; ++i) {
          f32x4 v;
          if (kh == 0) v = f32x4{o[3 + rd][4 * i], o[3 + rd][4 * i + 1], o[3 + rd][4 * i + 2], o[3 + rd][4 * i + 3]};
          else v = f32x4{o[rd][4 * i], o[rd][4 * i + 1], o[rd][4 * i + 2], o[rd][4 * i + 3]};
          *(LAS f32x4*)(size_t)(xw + i * 1024) = v;
        }
        lds_post(fme, 4 * u + rd + 1);
        lds_wait_ge(fpa, 4 * u + rd + 1);
        if (rd == 0) EST(2);
        f32x4 rv[4];
#pragma unroll
        for (int i = 0; i < 4; ++i) rv[i] = *(const LAS f32x4*)(size_t)(xr + i * 1024);
        if (rd == 0) {
          f32x2 stt = *(const LAS f32x2*)(size_t)(L0 + MISC + (pw * 32 + zc) * 8);
          const float M = fmaxf(m, stt[0]);
          const float as = __builtin_amdgcn_exp2f((m - M) * CSC), ao = __builtin_amdgcn_exp2f((stt[0] - M) * CSC);
          const float inv = 1.0f / (as * l + ao * stt[1]);
          ws_ = as * inv;
          wo_ = ao * inv;
        }
        lds_post(fme + 4, 4 * u + rd + 2);
#pragma unroll
        for (int i = 0; i < 4; i += 2) {
          unsigned pk[4];
#pragma unroll
          for (int j = 0; j < 2; ++j) {
            float v[4];
#pragma unroll
            for (int k = 0; k < 4; ++k) {
              const float own = kh == 0 ? o[rd][4 * (i + j) + k] : o[3 + rd][4 * (i + j) + k];
              v[k] = own * ws_ + rv[i + j][k] * wo_;
            }
            pk[2 * j] = cvtpk(v[0], v[1]);
            pk[2 * j + 1] = cvtpk(v[2], v[3]);
          }
          auto r0 = __builtin_amdgcn_permlane32_swap(pk[0], pk[2], false, false);
          auto r1 = __builtin_amdgcn_permlane32_swap(pk[1], pk[3], false, false);
          u32x4 w = {r0[0], r1[0], r0[1], r1[1]};
          const unsigned rowoff = zc * 64 + (((i + zh) ^ osw) * 16);
          if (u == 0) {
            if (rd == 0) *(LAS u32x4*)(size_t)(L0 + SIDE + r * ITB + 8192 + kh * 2048 + rowoff) = w;
            else hold[rd - 1][i >> 1] = w;
          } else {
            *(LAS u32x4*)(size_t)(L0 + 61440 + (3 * kh + rd) * ITB + 6144 + r * 2048 + rowoff) = w;
            __builtin_amdgcn_raw_buffer_store_b128(w, rsO, obase + (3 * kh + rd) * 6144 + (((i + zh) ^ osw) * 16), 0, 16);
          }
        }
      }
      EST(3);
      if (u == 0) {
        lds_wait_ge(fpa + 4, 4);
#pragma unroll
        for (int rd = 1; rd < 3; ++rd)
#pragma unroll
          for (int i = 0; i < 2; ++i)
            *(LAS u32x4*)(size_t)(L0 + SIDE + r * ITB + (rd - 1) * 4096 + kh * 2048 + zc * 64 + (((2 * i + zh) ^ osw) * 16)) = hold[rd - 1][i];
        lds_post(L0 + C_OR + 4 * wave, 1);
      }
      EST(4);
      STAMP(3 + 4 * u);
    }
  }
  {
    unsigned ln; LANEID(ln);
    constexpr unsigned TWO = 10240, TOB = 61440;
    const int ch = wave & 1, rq = wave >> 1;
    const unsigned g = ln >> 4, q4 = (ln & 15) >> 2, p4 = ln & 3, l15 = ln & 15;
    unsigned gA, gB;
    {
      auto wpiece = [&](int p) {
        const int n = 64 * p + ln, row = n / 20, pc = n % 20;
        return (unsigned)(row * (DM * 2) + ((pc ^ (((row >> 3) & 1) << 1)) * 16));
      };
      gA = wpiece(wave);
      gB = wave < 2 ? wpiece(8 + wave) : ln * 16 + (wave - 2) * 1024;
    }
    const int pc3 = wave < 6 ? wave : wave - 2;
    const unsigned gC = ln * 16 + pc3 * 1024 + 256 * 36864;
    const __amdgpu_buffer_rsrc_t rsWo = __builtin_amdgcn_make_buffer_rsrc((void*)((const char*)wo + 4 * xj * DH * DM * 2 + hq * 320), (short)0, HD * DM * 2 - (4 * xj * DH * DM * 2 + hq * 320), 0x00020000);
    const __amdgpu_buffer_rsrc_t rsOt = __builtin_amdgcn_make_buffer_rsrc(ws + WS_O + (size_t)qbase * 36864, (short)0, (512 - qbase) * 36864, 0x00020000);
    const __amdgpu_buffer_rsrc_t rsB = wave < 2 ? rsWo : rsOt;
    const unsigned mA = L0 + wave * 1024, mC = L0 + TOB + 6144 + pc3 * 1024;
    const unsigned mB = wave < 2 ? L0 + (8 + wave) * 1024 : L0 + TOB + (wave - 2) * 1024;
    const unsigned stepB = wave < 2 ? 32 * DM * 2 : 6144;
    const unsigned slotB = wave < 2 ? TWO : ITB;
    unsigned wsrc, osrc, bsrc;
    auto sethead = [&](int hs) { wsrc = hs * (DH * DM * 2); osrc = hs * (8 * 36864); bsrc = wave < 2 ? wsrc : osrc; };
    auto request = [&](int c6, int k) {
      if (k == 0) bdma(mA + c6 * TWO, rsWo, gA, wsrc + c6 * (32 * DM * 2));
      if (k == 1) bdma1(mB + c6 * slotB, rsB, gB, bsrc + c6 * stepB);
      if (k == 2) bdma1(mC + c6 * ITB, rsOt, gC, osrc + c6 * 6144);
    };
    const unsigned WAb = L0 + (8 * g + q4) * 320 + 160 * ch + 8 * p4;
    const unsigned WA0 = WAb + ((ch & 1) ? -32 * (int)(g & 1) : 32 * (int)(g & 1));
    const unsigned WA1 = WAb + ((ch & 1) ? 32 * (int)(g & 1) : -32 * (int)(g & 1));
    const unsigned OA = L0 + TOB + (48 * rq + l15) * 64 + ((g ^ (((l15 >> 2) & 1) << 1)) * 16);
    const unsigned OA2 = OA + 3 * ITB;
    f32x4 acc[3][5] = {};
    bf16x8 af[2][5], bfr[2][3];
    auto rdA = [&](int par, int sl, int cb) {
      const unsigned a = ((cb & 1) ? WA1 : WA0) + sl * TWO + cb * 32;
      af[par][cb] = cat(ldstr(a), ldstr(a + 4 * 320));
    };
    auto rdB = [&](int par, int sl, int rb) { bfr[par][rb] = lds128((sl < 3 ? OA : OA2) + (sl % 3) * ITB + rb * 1024); };
    if (wave >= 6) WAITV(0);
    BAR();
    auto tbody = [&](auto VV) {
      constexpr int V = decltype(VV)::value;
#pragma unroll
      for (int rb = 0; rb < 3; ++rb) rdB(0, 0, rb);
#pragma unroll
      for (int cb = 0; cb < 5; ++cb) rdA(0, 0, cb);
      BAR();
      STAMP(12);
      auto phase = [&](auto QQ, auto MODE) {
        constexpr int q = decltype(QQ)::value, mode = decltype(MODE)::value, cur = q & 1;
        constexpr int R0 = V == 0 ? 0 : 5, D0 = V == 0 ? 9 : 0;
#pragma unroll
        for (int i = 0; i < 15; ++i) {
          acc[i % 3][i / 3] = MFMA16(af[cur][i / 3], bfr[cur][i % 3], acc[i % 3][i / 3]);
          SBAR();
          if (i >= R0 && i < R0 + 3) rdB(cur ^ 1, (q + 1) % 6, i - R0);
          if (i >= R0 + 3 && i < R0 + 8) rdA(cur ^ 1, (q + 1) % 6, i - R0 - 3);
          if (mode == 0 && i >= D0 && i < D0 + 6 && !((i - D0) & 1)) request(q, (i - D0) >> 1);
          if (mode == 2 && q >= 3 && i >= D0 && i < D0 + 6) request(2 * (q - 3) + (i - D0) / 3, (i - D0) % 3);
          SBAR();
        }
        if (mode == 0) WAITV(12);
        if (mode == 1) { if (q == 0) WAITV(9); else if (q == 1) WAITV(6); else if (q == 2) WAITV(3); else WAITV(0); }
        if (mode == 2) {
          if (q == 0) WAITV(0);
          if (q == 2 && wave == 7) poll_flags(FL + (size_t)blockIdx.x * 32, 4, hq, TAG, ln);
          if (q == 4) WAITV(9);
          if (q == 5) WAITV(12);
        }
        BAR();
        if (mode == 2 && q == 0 && wave == 0 && ln < 4 && (int)ln != hq)
          __hip_atomic_store(FL + (size_t)(qbase + 8 * ln) * 32 + hq, TAG, __ATOMIC_RELAXED, __HIP_MEMORY_SCOPE_AGENT);
      };
      auto head6 = [&](auto MODE) {
        phase(std::integral_constant<int, 0>{}, MODE);
        phase(std::integral_constant<int, 1>{}, MODE);
        phase(std::integral_constant<int, 2>{}, MODE);
        phase(std::integral_constant<int, 3>{}, MODE);
        phase(std::integral_constant<int, 4>{}, MODE);
        phase(std::integral_constant<int, 5>{}, MODE);
      };
      sethead((hq + 1) & 3);
      head6(std::integral_constant<int, 2>{});
#pragma unroll 1
      for (int i = 2; i < 4; ++i) {
        sethead((hq + i) & 3);
        head6(std::integral_constant<int, 0>{});
      }
      STAMP(13);
      head6(std::integral_constant<int, 1>{});
    };
    if (wave < 4) tbody(std::integral_constant<int, 0>{}); else tbody(std::integral_constant<int, 1>{});
    STAMP(8);
    {
      const unsigned yw = L0 + (48 * rq + l15) * 336 + (80 * ch + 4 * g) * 2;
#pragma unroll
      for (int rb = 0; rb < 3; ++rb)
#pragma unroll
        for (int cb = 0; cb < 5; ++cb) {
          u32x2 w = {cvtpk(acc[rb][cb][0], acc[rb][cb][1]), cvtpk(acc[rb][cb][2], acc[rb][cb][3])};
          *(LAS u32x2*)(size_t)(yw + rb * 16 * 336 + cb * 32) = w;
        }
    }
    BAR();
    {
      const __amdgpu_buffer_rsrc_t rsP = __builtin_amdgcn_make_buffer_rsrc(ws + WS_P, (short)0, 2 * 8 * 4 * 8 * 30720, 0x00020000);
#pragma unroll
      for (int i = 0; i < 8; ++i) {
        const int j = wave + 8 * i;
        if (j < 60) {
          const int n = 64 * j + ln, row = n / 20, pc = n % 20, un = j >= 30 ? 1 : 0;
          const u32x4 v = *(const LAS u32x4*)(size_t)(L0 + row * 336 + pc * 16);
          __builtin_amdgcn_raw_buffer_store_b128(v, rsP, (((un * 8 + b0) * 4 + hq) * 8 + xj) * 30720 + (n - 1920 * un) * 16, 0, 16);
        }
      }
      WAITV(0);
      BAR();
      if (wave == 0) {
        if (ln < 8) __hip_atomic_store(FL + (size_t)((int)ln + 8 * xi) * 32 + 8 + xj, TAG, __ATOMIC_RELAXED, __HIP_MEMORY_SCOPE_AGENT);
        poll_flags(FL + (size_t)blockIdx.x * 32 + 8, 8, -1, TAG, ln);
      }
      BAR();
      STAMP(9);
      const int tz = wave * 64 + (int)ln;
      if (tz < 480) {
        const int u = tz / 240, e = tz % 240;
        const int pp = (((u * 8 + b0) * 4 + hq) * 8) * 30720 + 12 * xj * 320 + e * 16;
        u32x4 v[8];
#pragma unroll
        for (int j = 0; j < 8; ++j) v[j] = __builtin_amdgcn_raw_buffer_load_b128(rsP, pp + j * 30720, 0, 16);
        float a[8];
#pragma unroll
        for (int k = 0; k < 4; ++k) {
          float lo = 0.f, hi2 = 0.f;
#pragma unroll
          for (int j = 0; j < 8; ++j) { lo += __uint_as_float(v[j][k] << 16); hi2 += __uint_as_float(v[j][k] & 0xffff0000u); }
          a[2 * k] = lo; a[2 * k + 1] = hi2;
        }
        u32x4 w = {cvtpk(a[0], a[1]), cvtpk(a[2], a[3]), cvtpk(a[4], a[5]), cvtpk(a[6], a[7])};
        const int row = e / 20, c8 = e % 20;
        *(u32x4*)(out + (size_t)((b0 + 8 * u) * SQ + 12 * xj + row) * DM + hq * 160 + c8 * 8) = w;
      }
    }
    STAMP(10);
  }
}

extern "C" void kernel_launch(void* const* d_in, const int* in_sizes, int n_in, void* d_out, int out_size, void* d_ws,
                              size_t ws_size, hipStream_t stream) {
  const u16* x = (const u16*)d_in[0];
  const u16* kc = (const u16*)d_in[1];
  const u16* vc = (const u16*)d_in[2];
  const u16* wq = (const u16*)d_in[3];
  const u16* wo = (const u16*)d_in[4];
  (void)hipFuncSetAttribute((const void*)attn_kernel, hipFuncAttributeMaxDynamicSharedMemorySize, LDS_A);
  attn_kernel<<<256, 512, LDS_A, stream>>>(x, kc, vc, wq, wo, (u16*)d_out, (char*)d_ws, (unsigned long long*)((char*)d_ws + (128u << 20)));
}
```

```cpp
#include <hip/hip_runtime.h>
#include <hip/hip_bf16.h>
#include <stdint.h>
#include <type_traits>

typedef unsigned short u16;
typedef short bf16x8 __attribute__((ext_vector_type(8)));
typedef short s16x4 __attribute__((ext_vector_type(4)));
typedef float f32x16 __attribute__((ext_vector_type(16)));
typedef float f32x4 __attribute__((ext_vector_type(4)));
typedef float f32x2 __attribute__((ext_vector_type(2)));
typedef __bf16 bf16x2 __attribute__((ext_vector_type(2)));
typedef unsigned u32x4 __attribute__((ext_vector_type(4)));
typedef unsigned u32x2 __attribute__((ext_vector_type(2)));
#define LAS __attribute__((address_space(3)))
#define DI __device__ __forceinline__

constexpr int NB = 16, SQ = 96, DM = 640, NH = 32, SK = 1024, DH = 192;
constexpr int HD = NH * DH;

#define MFMA32(a, b, c) __builtin_amdgcn_mfma_f32_32x32x16_bf16((a), (b), (c), 0, 0, 0)
#define MFMA16(a, b, c) __builtin_amdgcn_mfma_f32_16x16x32_bf16((a), (b), (c), 0, 0, 0)
#define BAR() do { asm volatile("" ::: "memory"); __builtin_amdgcn_s_waitcnt(0xC07F); __builtin_amdgcn_s_barrier(); asm volatile("" ::: "memory"); } while (0)
#define WAITV(n) asm volatile("s_waitcnt vmcnt(%0)" ::"i"(n) : "memory")
#define SBAR() __builtin_amdgcn_sched_barrier(0)
#define MAX3(a, b, c) fmaxf(fmaxf((a), (b)), (c))
#define LANEID(v) asm volatile("v_mbcnt_lo_u32_b32 %0, -1, 0\n\tv_mbcnt_hi_u32_b32 %0, -1, %0" : "=v"(v))

DI unsigned cvtpk(float lo, float hi) {
  f32x2 v = {lo, hi};
  bf16x2 b = __builtin_convertvector(v, bf16x2);
  return __builtin_bit_cast(unsigned, b);
}
DI void dma16nt(unsigned lds_addr, const void* sbase, unsigned voff) {
  asm volatile("s_mov_b32 m0, %0\n\ts_nop 0\n\tglobal_load_lds_dwordx4 %1, %2 nt" ::"s"(lds_addr), "v"(voff), "s"(sbase) : "memory");
}
DI void bdma(unsigned lds_addr, __amdgpu_buffer_rsrc_t rs, unsigned voff, unsigned soff) {
  asm volatile("s_mov_b32 m0, %0\n\ts_nop 0\n\tbuffer_load_dwordx4 %1, %2, %3 offen lds" ::"s"(lds_addr), "v"(voff), "s"(rs), "s"(soff) : "memory");
}
DI void bdma1(unsigned lds_addr, __amdgpu_buffer_rsrc_t rs, unsigned voff, unsigned soff) {
  asm volatile("s_mov_b32 m0, %0\n\ts_nop 0\n\tbuffer_load_dwordx4 %1, %2, %3 offen sc1 lds" ::"s"(lds_addr), "v"(voff), "s"(rs), "s"(soff) : "memory");
}
DI void dma16(unsigned lds_addr, const void* sbase, unsigned voff) {
  asm volatile("s_mov_b32 m0, %0\n\ts_nop 0\n\tglobal_load_lds_dwordx4 %1, %2" ::"s"(lds_addr), "v"(voff), "s"(sbase) : "memory");
}
template <int I, int N, class F>
DI void sfor(F&& f) {
  if constexpr (I < N) { f(std::integral_constant<int, I>{}); sfor<I + 1, N>(f); }
}
DI bf16x8 lds128(unsigned a) { return *(const LAS bf16x8*)(size_t)a; }
DI s16x4 ldstr(unsigned a) { return __builtin_amdgcn_ds_read_tr16_b64_v4i16((LAS s16x4*)(size_t)a); }
DI bf16x8 cat(s16x4 a, s16x4 b) { return __builtin_shufflevector(a, b, 0, 1, 2, 3, 4, 5, 6, 7); }

constexpr unsigned ITB = 12288;
constexpr unsigned NSL = 5;
constexpr unsigned GRB = NSL * ITB;
constexpr unsigned SIDE = 2 * GRB;
constexpr unsigned MISC = SIDE + 36864;
constexpr unsigned CTL = MISC + 2048;
constexpr unsigned HB = MISC - 49152;
constexpr unsigned C_L = CTL;
constexpr unsigned C_R = CTL + 16;
constexpr unsigned C_QRDY = CTL + 48;
constexpr unsigned C_XF = CTL + 64;
constexpr unsigned C_H = CTL + 128;
constexpr unsigned C_OR = CTL + 160;
constexpr unsigned C_OD = CTL + 192;
constexpr unsigned LDS_A = 163840;
constexpr float CSC = 0.07216878364870322f * 1.4426950408889634f;
constexpr float THR_RAW = 8.0f / CSC;
constexpr int SPIN_MAX = 1 << 20;
#define HD_D 4
#define TL_D 4
#define KV0 2

#define STAMP(i)
#define EST(i)
#define FST(i)

DI void lds_post(unsigned a, unsigned v) {
  asm volatile("s_waitcnt lgkmcnt(0)\n\tds_write_b32 %0, %1" ::"v"(a), "v"(v) : "memory");
}
DI void lds_post_nw(unsigned a, unsigned v) {
  asm volatile("ds_write_b32 %0, %1" ::"v"(a), "v"(v) : "memory");
}
DI void lds_wait_ge(unsigned a, unsigned n) {
#pragma nounroll
  for (int it = 0; it < SPIN_MAX; ++it) {
    unsigned v;
    asm volatile("ds_read_b32 %0, %1\n\ts_waitcnt lgkmcnt(0)" : "=v"(v) : "v"(a) : "memory");
    if (__builtin_amdgcn_readfirstlane(v) >= n) break;
    __builtin_amdgcn_s_sleep(1);
  }
}
DI void lds_wait_min4(unsigned a, unsigned n) {
#pragma nounroll
  for (int it = 0; it < SPIN_MAX; ++it) {
    u32x4 d;
    asm volatile("ds_read_b128 %0, %1\n\ts_waitcnt lgkmcnt(0)" : "=v"(d) : "v"(a) : "memory");
    const unsigned mn = min(min(d[0], d[1]), min(d[2], d[3]));
    if (__builtin_amdgcn_readfirstlane(mn) >= n) break;
    __builtin_amdgcn_s_sleep(1);
  }
}
DI unsigned getvm() {
  unsigned sts;
  asm volatile("s_getreg_b32 %0, hwreg(HW_REG_IB_STS)" : "=s"(sts)::"memory");
  return (sts & 15u) | ((sts >> 18) & 0x30u);
}
DI unsigned lds_peek(unsigned a) { return *(volatile LAS unsigned*)(size_t)a; }

typedef __attribute__((address_space(1))) unsigned long long gu64;
extern "C" __device__ unsigned long long amd_dispatch_id() __asm("llvm.amdgcn.dispatch.id");
constexpr size_t WS_O = 0, WS_P = 32u << 20, WS_F = 96u << 20;

DI bool poll_flags(gu64* f, int nl, int skip, unsigned long long T, int lane) {
#pragma nounroll
  for (int it = 0; it < SPIN_MAX; ++it) {
    unsigned long long v = T;
    if (lane < nl && lane != skip) v = __hip_atomic_load(f + lane, __ATOMIC_RELAXED, __HIP_MEMORY_SCOPE_AGENT);
    if (__all(v == T)) {
      if (lane < nl && lane != skip) __hip_atomic_store(f + lane, 0ull, __ATOMIC_RELAXED, __HIP_MEMORY_SCOPE_AGENT);
      return true;
    }
    __builtin_amdgcn_s_sleep(2);
  }
  return false;
}

constexpr int hNL(int j) { return j < 20 ? 3 : 0; }
constexpr int hKV(int p) { return (p >= KV0 && p < KV0 + 12) ? 1 : 0; }
constexpr int hPend(int j) {
  int cnt = 0, first = 0;
  if (j < HD_D) { cnt = 3 * (HD_D - 1 - j); first = 0; }
  else { cnt = hKV(j - HD_D); first = j - HD_D + 1; }
  for (int p = first; p < j; ++p) cnt += hNL(p + HD_D) + hKV(p);
  return cnt;
}
constexpr int tNL(int j) { return j < 24 ? 3 : 0; }
constexpr int tPend(int j) {
  int cnt = 0, first = 0;
  if (j < TL_D) { cnt = 3 * (TL_D - 1 - j); first = 0; }
  else { cnt = 0; first = j - TL_D + 1; }
  for (int p = first; p < j; ++p) cnt += tNL(p + TL_D);
  return cnt;
}

__global__ __launch_bounds__(512, 2) void attn_kernel(const u16* x, const u16* kc, const u16* vc, const u16* wq, const u16* wo,
                                                      u16* out, char* ws, unsigned long long* stamps) {
  extern __shared__ __attribute__((aligned(16))) char lds[];
  const unsigned L0 = (unsigned)(size_t)(LAS char*)lds;
  const int tid = threadIdx.x, lane = tid & 63;
  const int wave = __builtin_amdgcn_readfirstlane(tid >> 6);
  const int xj = blockIdx.x & 7, xi = blockIdx.x >> 3;
  const int hq = xi & 3, h = 4 * xj + hq, b0 = xi >> 2;
  const size_t UB0 = (size_t)(b0 * NH + h) * SK * 384, UB1 = (size_t)((b0 + 8) * NH + h) * SK * 384;
  unsigned long long TAG;
  {
    const unsigned long long did = amd_dispatch_id(), dp = (unsigned long long)__builtin_amdgcn_dispatch_ptr();
    const unsigned t32 = (unsigned)(did * 0x9E3779B1ull) ^ (unsigned)(dp >> 6) * 0x85EBCA77u ^ (unsigned)(did >> 32);
    TAG = ((unsigned long long)(t32 ^ 0x5A17C3E9u) << 32) | t32;
  }
  const int qbase = xj + 32 * b0;
  gu64* FL = (gu64*)(ws + WS_F);
  const int kh = (0x1A >> wave) & 1, r = (0x102210 >> (4 * wave)) & 3;
  STAMP(0);
  bf16x8 qf[12];

  unsigned offK[6], offV[3];
  {
    if (wave == 6 && lane < 64) *(LAS unsigned*)(size_t)(L0 + CTL + lane * 4) = (lane == 46 || lane == 47) ? 1u : 0u;
    const int rot = (b0 * 5 / 4 + hq * 5 / 2) % 10;
    const int dh = wave & 1, rq = wave >> 1;
    const unsigned g = lane >> 4, q4 = (lane & 15) >> 2, p4 = lane & 3, l15 = lane & 15;
    const __amdgpu_buffer_rsrc_t rsW = __builtin_amdgcn_make_buffer_rsrc((void*)((const char*)wq + h * DH * 2), (short)0, DM * HD * 2 - h * DH * 2, 0x00020000);
    const __amdgpu_buffer_rsrc_t rsX = __builtin_amdgcn_make_buffer_rsrc((void*)((const char*)x + b0 * SQ * DM * 2), (short)0, (NB - b0) * SQ * DM * 2, 0x00020000);
    unsigned gA, gB, gC;
    {
      auto wpiece = [&](int p) {
        const int n = 64 * p + lane, row = n / 24, pc = n % 24;
        const int sw = ((row >> 1) & 1) | (((row >> 3) & 1) << 1);
        return (unsigned)(row * (HD * 2) + ((pc ^ (sw << 1)) * 16));
      };
      auto xpiece = [&](int p) {
        const int row = 8 * p + (lane >> 3);
        return (unsigned)(row * (DM * 2) + (((lane & 7) ^ ((row >> 1) & 7)) * 16));
      };
      gA = wpiece(wave);
      gB = wave < 4 ? wpiece(8 + wave) : xpiece(wave - 4);
      gC = xpiece(4 + wave);
    }
    unsigned hK[3], hV[3];
    constexpr unsigned WB = 3 * ITB, G1X = GRB + 2 * ITB, U1 = 8 * SQ * DM * 2;
    constexpr unsigned WSTEP = 32 * HD * 2;
    const unsigned mW = L0 + WB + wave * 1024;
    const unsigned mX = L0 + (4 + wave) * 1024;
    const unsigned s2 = ((q4 >> 1) & 1) | ((g & 1) << 1);
    unsigned E[4];
    const unsigned XA0 = L0 + (rq >> 1) * G1X + (48 * (rq & 1) + l15) * 128 + ((g ^ ((l15 >> 1) & 7)) * 16), XA1 = XA0 ^ 64;
    f32x4 acc[6][3] = {};
    bf16x8 af[2][6], bfr[2][3];
    unsigned xa;
    auto rdA = [&](int par, int c1, int db) {
      const unsigned a = E[db & 3] + (db >> 2) * 128 + (c1 & 3) * ITB;
      af[par][db] = cat(ldstr(a), ldstr(a + 4 * 384));
    };
    auto rdB = [&](int par, int rb) { bfr[par][rb] = lds128(xa + rb * 2048); };
    unsigned woff = (unsigned)(2 * rot) * WSTEP, xoff = (unsigned)rot * 128;
    auto advW = [&]() { woff += WSTEP; woff = woff == 20 * WSTEP ? 0u : woff; };
    auto advX = [&]() { xoff += 128; xoff = xoff == 1280 ? 0u : xoff; };
    auto request = [&](auto VV, int k, int wsl, unsigned xs, int un) {
      constexpr int V = decltype(VV)::value;
      if (k == 0) bdma(mW + wsl * ITB, rsW, gA, woff);
      if (k == 1) {
        if (V == 0) bdma(mW + 8192 + wsl * ITB, rsW, gB, woff);
        else bdma(mX - 8192 + un * G1X + xs, rsX, gB, xoff + un * U1);
      }
      if (k == 2) bdma(mX + un * G1X + xs, rsX, gC, xoff + un * U1);
    };
    unsigned xr = 2 * ITB;
    unsigned xw = 1 * ITB;
    auto body = [&](auto VV) {
      constexpr int V = decltype(VV)::value;
      if (V == 1) bdma(mX - 8192 + 2 * ITB, rsX, gB, xoff);
      bdma(mX + 2 * ITB, rsX, gC, xoff);
#pragma unroll
      for (int c = -4; c < 0; ++c) {
        if ((c & 1) != 0) advX();
        const unsigned xs = (unsigned)((((c + 1) >> 1) + 2 + 2) % 3) * ITB;
#pragma unroll
        for (int k = 0; k < 3; ++k) request(VV, k, c & 3, xs, (c + 1) & 1);
        advW();
      }
      SBAR();
      asm volatile("" ::: "memory");
#pragma unroll
    for (int j = 0; j < 6; ++j) {
      int n = 64 * j + lane, row = n / 24, pc = n % 24;
      offK[j] = row * 384 + ((pc ^ ((row >> 1) & 7)) * 16);
    }
#pragma unroll
    for (int j = 0; j < 3; ++j) {
      int n = 64 * j + lane, row = n / 24, pc = n % 24;
      offV[j] = row * 384 + ((pc ^ (((row >> 1) & 1) << 2)) * 16);
    }
#pragma unroll
    for (int j = 0; j < 3; ++j) {
      const int n = 64 * (3 * (wave & 3) + j) + lane, row = n / 24, pc = n % 24;
      hK[j] = row * 384 + ((pc ^ ((row >> 1) & 7)) * 16);
      hV[j] = row * 384 + ((pc ^ (((row >> 1) & 1) << 2)) * 16);
    }
#pragma unroll
      for (int j = 0; j < 4; ++j) E[j] = L0 + WB + (8 * g + q4) * 384 + 32 * ((unsigned)(6 * dh + j) ^ s2) + 8 * p4;
      WAITV(9);
      BAR();
      xa = XA0 + 2 * ITB;
#pragma unroll
      for (int rb = 0; rb < 3; ++rb) rdB(0, rb);
#pragma unroll
      for (int db = 0; db < 6; ++db) rdA(0, 0, db);
      WAITV(6);
      BAR();
      STAMP(4);
      auto phase = [&](int c, auto QQ, auto MODE) {
        constexpr int q = decltype(QQ)::value, mode = decltype(MODE)::value, cur = q & 1;
        if (cur == 1) {
          xr = xr == 2 * ITB ? 0 : xr + ITB;
          if (mode == 0) { xw = xw == 2 * ITB ? 0 : xw + ITB; advX(); }
        }
        xa = (cur == 1 ? XA0 : XA1) + xr;
        constexpr int R0 = V == 0 ? 0 : 5, D0 = V == 0 ? 10 : 0;
#pragma unroll
        for (int i = 0; i < 18; ++i) {
          acc[i / 3][i % 3] = MFMA16(af[cur][i / 3], bfr[cur][i % 3], acc[i / 3][i % 3]);
          SBAR();
          if (i >= R0 && i < R0 + 3) rdB(cur ^ 1, i - R0);
          if (i >= R0 + 3 && i < R0 + 9) rdA(cur ^ 1, q + 1, i - R0 - 3);
          if (mode == 0 && i >= D0 && i < D0 + 6 && !((i - D0) & 1)) request(VV, (i - D0) >> 1, q, xw, (q + 1) & 1);
          if (mode == 0 && i == D0 + 5) advW();
          if (mode == 2 && i >= D0 && i < D0 + 6 && !((i - D0) & 1)) {
            const int k = (i - D0) >> 1, li = c - 17;
            const unsigned dst = L0 + V * G1X + li * ITB + (3 * (wave & 3) + k) * 1024;
            if (li == 2) dma16nt(dst, (const char*)vc + UB0 + (size_t)(32 * V) * 384, hV[k]);
            else dma16nt(dst, (const char*)kc + UB0 + (size_t)(64 * li + 32 * V) * 384, hK[k]);
          }
          SBAR();
        }
        if (mode == 0) WAITV(6);
        if (mode == 1) WAITV(0);
        BAR();
      };
      using I0 = std::integral_constant<int, 0>; using I1 = std::integral_constant<int, 1>;
      using I2 = std::integral_constant<int, 2>; using I3 = std::integral_constant<int, 3>;
#pragma unroll 1
      for (int c = 0; c < 16; c += 4) {
        phase(c, I0{}, I0{});
        phase(c + 1, I1{}, I0{});
        phase(c + 2, I2{}, I0{});
        phase(c + 3, I3{}, I0{});
      }
      STAMP(5);
      phase(16, I0{}, I1{});
      phase(17, I1{}, I2{});
      phase(18, I2{}, I2{});
      phase(19, I3{}, I2{});
    };
    if (wave < 4) body(std::integral_constant<int, 0>{}); else body(std::integral_constant<int, 1>{});
    STAMP(11);
    auto qstore = [&]() {
      unsigned lz; LANEID(lz);
      const unsigned zg = lz >> 4, zl = lz & 15;
#pragma unroll
      for (int rb = 0; rb < 3; ++rb) {
        const int R16 = 3 * (rq & 1) + rb;
        const unsigned qb = L0 + SIDE + ((R16 >> 1) * 12 + 6 * dh) * 1024 + (16 * (R16 & 1) + zl + 32 * (zg >> 1)) * 16 + 8 * (zg & 1);
#pragma unroll
        for (int db = 0; db < 6; ++db) {
          u32x2 w = {cvtpk(acc[db][rb][0], acc[db][rb][1]), cvtpk(acc[db][rb][2], acc[db][rb][3])};
          *(LAS u32x2*)(size_t)(qb + db * 1024) = w;
        }
      }
    };
    if (wave < 4) qstore();
    BAR();
    if (wave < 6) {
      unsigned lz; LANEID(lz);
#pragma unroll
      for (int ks = 0; ks < 12; ++ks) qf[ks] = lds128(L0 + SIDE + ((r * 12 + ks) * 64 + lz) * 16);
    }
    BAR();
    if (wave >= 4) { qstore(); lds_post(L0 + C_QRDY + 4 * (wave - 4), 1); }
  }
  STAMP(1);

  if (wave >= 6) {
    const int hw = wave - 6;
    __builtin_amdgcn_s_setprio(3);
    unsigned ll; LANEID(ll);
    const unsigned aL = L0 + C_L + 4 * hw, aR = L0 + C_R + 16 * hw;
    auto issue_item = [&](unsigned li_, unsigned slot_) {
      const unsigned j = li_ & 31;
      const bool isK = (j == 0) || ((j & 1) && j < 31);
      const unsigned tile = isK ? (j + 1) >> 1 : (j - 1) >> 1;
      const size_t ub = ((li_ >> 5) ? UB1 : UB0) + (size_t)(64 * tile + 32 * hw) * 384;
      const unsigned dst = L0 + hw * GRB + slot_;
      if (isK) {
        const char* src = (const char*)kc + ub;
#pragma unroll
        for (int q = 0; q < 12; ++q) dma16nt(dst + q * 1024, src + (q / 6) * 6144, offK[q % 6]);
      } else {
        const char* src = (const char*)vc + ub;
#pragma unroll
        for (int q = 0; q < 12; ++q) dma16nt(dst + q * 1024, src + (q / 3) * 3072, offV[q % 3]);
      }
    };
    issue_item(3, hw ? 0 : 3 * ITB);
    issue_item(4, hw ? ITB : 4 * ITB);
    WAITV(30); lds_post_nw(L0 + C_H + 4 * wave, 1); lds_wait_min4(L0 + C_H + 16 * hw, 1); lds_post_nw(aL, 1);
    WAITV(27); lds_post_nw(L0 + C_H + 4 * wave, 2); lds_wait_min4(L0 + C_H + 16 * hw, 2); lds_post_nw(aL, 2);
    WAITV(24); lds_post_nw(L0 + C_H + 4 * wave, 3); lds_wait_min4(L0 + C_H + 16 * hw, 3); lds_post_nw(aL, 3);
    unsigned li = 5, lpub = 3, slot = hw ? 2 * ITB : 0;
    unsigned sb = 0, ost = 0;
    bool ordy = false;
    const __amdgpu_buffer_rsrc_t rsO = __builtin_amdgcn_make_buffer_rsrc(ws + WS_O, (short)0, 2 * 256 * 36864, 0x00020000);
    auto obatch = [&]() {
#pragma unroll
      for (int k = 0; k < 3; ++k) {
        const unsigned p = 18 * hw + 3 * ost + k, rr = p / 12, q = p % 12;
        const unsigned c6 = (0x305241u >> (4 * (q >> 1))) & 7;
        const u32x4 v = *(const LAS u32x4*)(size_t)(L0 + SIDE + p * 1024 + ll * 16);
        __builtin_amdgcn_raw_buffer_store_b128(v, rsO, (int)(blockIdx.x * 36864 + c6 * 6144 + rr * 2048 + (q & 1) * 1024 + ll * 16), 0, 16);
      }
      ++ost;
      if (ost == 6) lds_post(L0 + C_OD + 4 * hw, 1);
    };
    auto ocheck = [&]() {
      u32x4 d0, d1;
      asm volatile("ds_read_b128 %0, %2\n\tds_read_b128 %1, %2 offset:16\n\ts_waitcnt lgkmcnt(0)" : "=&v"(d0), "=&v"(d1) : "v"(L0 + C_OR) : "memory");
      const unsigned mn = min(min(min(d0[0], d0[1]), min(d0[2], d0[3])), min(min(d1[0], d1[1]), min(d1[2], d1[3])));
      return __builtin_amdgcn_readfirstlane(mn) >= 1;
    };
#pragma nounroll
    for (int it = 0; it < (SPIN_MAX << 2); ++it) {
      bool idle = true;
      if (li < 64) {
        u32x4 d;
        asm volatile("ds_read_b128 %0, %1\n\ts_waitcnt lgkmcnt(0)" : "=v"(d) : "v"(aR) : "memory");
        const unsigned mn = __builtin_amdgcn_readfirstlane(min(min(d[0], d[1]), d[2]));
        if (mn + NSL > li && getvm() <= 48) {
          issue_item(li, slot);
          if (li >= 32 && ost < 6) {
            if (!ordy) ordy = ocheck();
            if (ordy) { obatch(); sb |= 1u << (li & 31); }
          } else if (li == 48) {
            const char* wsrc = (const char*)wo + (size_t)(4 * xj * DH + 96 * b0 + 48 * hw) * (DM * 2) + hq * 320;
            const unsigned o0 = (ll >> 2) * (DM * 2) + min((ll & 3) * 128u, 316u);
#pragma unroll
            for (int z = 0; z < 3; ++z)
              asm volatile("s_mov_b32 m0, %0\n\ts_nop 0\n\tglobal_load_lds_dword %1, %2" ::"s"(L0 + MISC + 1536), "v"(o0 + z * 16 * DM * 2), "s"(wsrc) : "memory");
            sb |= 1u << (li & 31);
          }
          ++li;
          slot = slot + ITB == GRB ? 0 : slot + ITB;
          idle = false;
        }
      }
      if (lpub < li) {
        const unsigned vm = getvm();
        unsigned done = lpub;
#pragma nounroll
        while (done < li) {
          const unsigned n = li - done;
          const unsigned rotm = (sb >> (done & 31)) | (sb << ((32 - (done & 31)) & 31));
          const unsigned after = 12 * (n - 1) + 3 * __builtin_popcount(rotm & ((1u << n) - 1));
          if (vm > after) break;
          sb &= ~(1u << (done & 31));
          ++done;
        }
        if (done > lpub) {
          lpub = done;
          lds_post_nw(aL, lpub);
          idle = false;
        }
      } else if (li == 64) {
        break;
      }
      if (idle) __builtin_amdgcn_s_sleep(1);
    }
    __builtin_amdgcn_s_setprio(0);
#pragma nounroll
    for (int it = 0; it < SPIN_MAX && ost < 6; ++it) { if (ocheck()) obatch(); else __builtin_amdgcn_s_sleep(1); }
    WAITV(0);
    lds_post(L0 + C_OD + 4 * hw, 2);
    {
      constexpr unsigned TWO = 10240, TOB = 61440;
      u32x4 d;
#pragma nounroll
      for (int it = 0; it < SPIN_MAX; ++it) {
        asm volatile("ds_read_b128 %0, %1\n\ts_waitcnt lgkmcnt(0)" : "=v"(d) : "v"(aR) : "memory");
        if (__builtin_amdgcn_readfirstlane(min(min(d[0], d[1]), d[2])) >= 64) break;
        __builtin_amdgcn_s_sleep(1);
      }
      lds_post_nw(L0 + C_H + 4 * wave, 8);
      lds_wait_ge(L0 + C_H + 4 * (13 - wave), 8);
      lds_wait_ge(L0 + C_OD + 4 * (1 - hw), 2);
      const __amdgpu_buffer_rsrc_t rsWo = __builtin_amdgcn_make_buffer_rsrc((void*)((const char*)wo + 4 * xj * DH * DM * 2 + hq * 320), (short)0, HD * DM * 2 - (4 * xj * DH * DM * 2 + hq * 320), 0x00020000);
      unsigned go[5];
#pragma unroll
      for (int p = 0; p < 5; ++p) {
        const int n = 64 * p + ll, row = n / 20, pc = n % 20;
        go[p] = row * (DM * 2) + ((pc ^ (((row >> 3) & 1) << 1)) * 16);
      }
#pragma unroll 1
      for (int c6 = 0; c6 < 6; ++c6) {
#pragma unroll
        for (int p = 0; p < 5; ++p) bdma(L0 + c6 * TWO + (5 * hw + p) * 1024, rsWo, go[p], (hq * DH + 32 * c6 + 16 * hw) * (DM * 2));
#pragma unroll
        for (int p = 0; p < 3; ++p) bdma1(L0 + TOB + c6 * ITB + (3 * hw + p) * 1024, rsO, ll * 16, blockIdx.x * 36864 + c6 * 6144 + (3 * hw + p) * 1024);
      }
    }
  } else {
    unsigned ln; LANEID(ln);
    unsigned kof[4], vE, vO;
    {
      const unsigned a32 = ln & 31, ahi = ln >> 5, ablk = (ln >> 4) & 1, aq4 = (ln & 15) >> 2, ap4 = ln & 3, axq = (aq4 >> 1) & 1;
#pragma unroll
      for (int i = 0; i < 4; ++i) kof[i] = L0 + kh * GRB + a32 * 384 + (((2 * i + ahi) ^ ((a32 >> 1) & 7)) * 16);
      vE = L0 + kh * GRB + (4 * ahi + aq4) * 384 + 64 * axq + 32 * ablk + 8 * ap4;
      vO = L0 + kh * GRB + (4 * ahi + aq4) * 384 - 64 * axq + 32 * ablk + 8 * ap4;
    }
    const unsigned aF = L0 + C_L + 4 * kh, aR = L0 + C_R + 16 * kh + 4 * r;
    const int pw = (0x102354 >> (4 * wave)) & 7;
    const unsigned fme = L0 + C_XF + 8 * wave, fpa = L0 + C_XF + 8 * pw;
    unsigned cur = kh ? 2 * ITB : 0;
    unsigned cnt = 0;
#define ADV() do { cur = (cur + ITB == GRB) ? 0u : cur + ITB; ++cnt; } while (0)
#define KADDR(ks) (((ks) & 3) == 0 ? k0 : ((ks) & 3) == 1 ? k1 : ((ks) & 3) == 2 ? k2 : k3) + 128 * ((ks) >> 2)
#define VADDR(j) ((((j) % 6) & 1) ? vbO : vbE) + (16 * ((j) / 6)) * 384 + 64 * ((j) % 6)

#pragma unroll 1
    for (int u = 0; u < 2; ++u) {
      float m, l = 0.f;
      f32x16 o[6] = {};
      f32x16 sB;
      float pe[16];
      bf16x8 kf[12], vf[12];
      unsigned k0, k1, k2, k3, vbE, vbO;
      if (u == 0) { WAITV(6); lds_post_nw(L0 + C_H + 4 * wave, 1); }
      lds_wait_ge(aF, cnt + 1);
      k0 = kof[0] + cur; k1 = kof[1] + cur; k2 = kof[2] + cur; k3 = kof[3] + cur;
#pragma unroll
      for (int ks = 0; ks < 12; ++ks) kf[ks] = lds128(KADDR(ks));
      ADV();
      lds_post_nw(aR, cnt);
      SBAR();
      {
        f32x16 z = {};
        sB = MFMA32(kf[0], qf[0], z);
#pragma unroll
        for (int ks = 1; ks < 12; ++ks) sB = MFMA32(kf[ks], qf[ks], sB);
      }
      SBAR();
      if (u == 0) { WAITV(3); lds_post_nw(L0 + C_H + 4 * wave, 2); }
      lds_wait_ge(aF, cnt + 1);
      if (u == 0) { WAITV(0); lds_post_nw(L0 + C_H + 4 * wave, 3); }
      k0 = kof[0] + cur; k1 = kof[1] + cur; k2 = kof[2] + cur; k3 = kof[3] + cur;
#pragma unroll
      for (int ks = 0; ks < 4; ++ks) kf[ks] = lds128(KADDR(ks));
      {
        float pmax = sB[0];
#pragma unroll
        for (int i = 1; i < 16; ++i) pmax = fmaxf(pmax, sB[i]);
        auto rr = __builtin_amdgcn_permlane32_swap(__float_as_uint(pmax), __float_as_uint(pmax), false, false);
        m = fmaxf(__uint_as_float(rr[0]), __uint_as_float(rr[1]));
        const float mc = -m * CSC;
#pragma unroll
        for (int i = 0; i < 16; ++i) pe[i] = fmaf(sB[i], CSC, mc);
      }
      SBAR();
      bf16x8 pa[2];
      auto expstep = [&](int i) {
        pe[2 * i] = __builtin_amdgcn_exp2f(pe[2 * i]);
        pe[2 * i + 1] = __builtin_amdgcn_exp2f(pe[2 * i + 1]);
        l += pe[2 * i];
        l += pe[2 * i + 1];
        asm volatile("" : "+v"(pe[2 * i]), "+v"(pe[2 * i + 1]), "+v"(l));
      };
      auto cvtstep = [&](int s) {
        u32x4 w = {cvtpk(pe[8 * s], pe[8 * s + 1]), cvtpk(pe[8 * s + 2], pe[8 * s + 3]), cvtpk(pe[8 * s + 4], pe[8 * s + 5]),
                   cvtpk(pe[8 * s + 6], pe[8 * s + 7])};
        asm volatile("" : "+v"(w));
        pa[s] = __builtin_bit_cast(bf16x8, w);
      };
#pragma unroll 1
      for (int t = 0; t < 15; ++t) {
        unsigned fl = 0;
#pragma unroll
        for (int i = 0; i < 12; ++i) {
          if (i == 0) { f32x16 z = {}; sB = MFMA32(kf[0], qf[0], z); }
          else sB = MFMA32(kf[i], qf[i], sB);
          if (i < 8) kf[i + 4] = lds128(KADDR(i + 4));
          if (i == 2) fl = lds_peek(aF);
          if (i == 7) { ADV(); lds_post_nw(aR, cnt); }
          if (i < 8) expstep(i);
          if (i == 8) {
            if (__builtin_amdgcn_readfirstlane(fl) < cnt + 1) lds_wait_ge(aF, cnt + 1);
            vbE = vE + cur; vbO = vO + cur;
            cvtstep(0);
          }
          if (i == 9) cvtstep(1);
          if (i >= 8) vf[i - 8] = cat(ldstr(VADDR(i - 8)), ldstr(VADDR(i - 8) + 8 * 384));
          SBAR();
        }
        float pmax = 0.f, alpha = 1.f, mc = 0.f;
        bool resc = false;
#pragma unroll
        for (int j = 0; j < 12; ++j) {
          o[j % 6] = MFMA32(vf[j], pa[j / 6], o[j % 6]);
          if (j < 8) vf[j + 4] = cat(ldstr(VADDR(j + 4)), ldstr(VADDR(j + 4) + 8 * 384));
          if (j == 2) fl = lds_peek(aF);
          if (j == 7) { ADV(); lds_post_nw(aR, cnt); }
          if (j == 1) {
            asm volatile("" : "+v"(sB));
            pmax = MAX3(MAX3(sB[0], sB[1], sB[2]), MAX3(sB[3], sB[4], sB[5]), MAX3(sB[6], sB[7], sB[8]));
            asm volatile("" : "+v"(pmax));
          }
          if (j == 2) {
            pmax = MAX3(MAX3(sB[9], sB[10], sB[11]), MAX3(sB[12], sB[13], sB[14]), MAX3(sB[15], sB[15], pmax));
            asm volatile("" : "+v"(pmax));
          }
          if (j == 3) {
            auto rr = __builtin_amdgcn_permlane32_swap(__float_as_uint(pmax), __float_as_uint(pmax), false, false);
            pmax = fmaxf(__uint_as_float(rr[0]), __uint_as_float(rr[1]));
            resc = pmax - m > THR_RAW;
            const float mn = resc ? pmax : m;
            alpha = __builtin_amdgcn_exp2f((m - mn) * CSC);
            m = mn;
            mc = -mn * CSC;
            l *= alpha;
            asm volatile("" : "+v"(m), "+v"(mc), "+v"(l), "+v"(alpha));
          }
          if (j >= 4 && j < 8) {
#pragma unroll
            for (int i = 4 * (j - 4); i < 4 * (j - 4) + 4; ++i) pe[i] = fmaf(sB[i], CSC, mc);
            asm volatile("" : "+v"(pe[4 * (j - 4)]), "+v"(pe[4 * (j - 4) + 1]), "+v"(pe[4 * (j - 4) + 2]), "+v"(pe[4 * (j - 4) + 3]));
          }
          if (j == 8) {
            if (t < 14 && __builtin_amdgcn_readfirstlane(fl) < cnt + 1) lds_wait_ge(aF, cnt + 1);
            k0 = kof[0] + cur; k1 = kof[1] + cur; k2 = kof[2] + cur; k3 = kof[3] + cur;
          }
          if (j >= 8) kf[j - 8] = lds128(KADDR(j - 8));
          SBAR();
        }
        if (__any(resc)) {
#pragma unroll
          for (int d = 0; d < 6; ++d)
#pragma unroll
            for (int i = 0; i < 16; ++i) o[d][i] *= alpha;
        }
      }
      {
        lds_wait_ge(aF, cnt + 1);
        vbE = vE + cur; vbO = vO + cur;
#pragma unroll
        for (int j = 0; j < 6; ++j) vf[j] = cat(ldstr(VADDR(j)), ldstr(VADDR(j) + 8 * 384));
        SBAR();
#pragma unroll
        for (int i = 0; i < 8; ++i) expstep(i);
        cvtstep(0);
        cvtstep(1);
        SBAR();
#pragma unroll
        for (int j = 0; j < 12; ++j) {
          o[j % 6] = MFMA32(vf[j], pa[j / 6], o[j % 6]);
          if (j < 6) vf[j + 6] = cat(ldstr(VADDR(j + 6)), ldstr(VADDR(j + 6) + 8 * 384));
          if (j == 5) { ADV(); lds_post_nw(aR, cnt); }
          SBAR();
        }
        if (u == 0) {
          lds_wait_min4(L0 + C_QRDY, 1);
#pragma unroll
          for (int ks = 0; ks < 12; ++ks) qf[ks] = lds128(L0 + SIDE + ((r * 12 + ks) * 64 + ln) * 16);
        }
        lds_post(fme + 4, 4 * u + 1);
      }
      STAMP(2 + 4 * u);
      EST(0);
      {
        auto rr = __builtin_amdgcn_permlane32_swap(__float_as_uint(l), __float_as_uint(l), false, false);
        l = __uint_as_float(rr[0]) + __uint_as_float(rr[1]);
      }
      unsigned lz; LANEID(lz);
      const int zc = lz & 31, zh = lz >> 5;
      if (zh == 0) *(LAS f32x2*)(size_t)(L0 + MISC + (wave * 32 + zc) * 8) = f32x2{m, l};
      float ws_ = 0.f, wo_ = 0.f;
      const __amdgpu_buffer_rsrc_t rsO = __builtin_amdgcn_make_buffer_rsrc(ws + WS_O, (short)0, 2 * 256 * 36864, 0x00020000);
      const int osw = ((zc >> 2) & 1) << 1;
      const int obase = (256 + blockIdx.x) * 36864 + (32 * r + zc) * 64;
      const unsigned xb_ = L0 + SIDE + (u == 0 ? r * ITB : ITB + r * 8192);
      const unsigned xw = xb_ + kh * 4096 + lz * 16, xr = xb_ + (1 - kh) * 4096 + lz * 16;
      if (u == 1) {
        lds_wait_ge(L0 + C_OD, 1);
        lds_wait_ge(L0 + C_OD + 4, 1);
#pragma nounroll
        for (int it = 0; it < SPIN_MAX; ++it) {
          u32x4 d;
          asm volatile("ds_read_b128 %0, %1\n\ts_waitcnt lgkmcnt(0)" : "=v"(d) : "v"(L0 + C_R + 16) : "memory");
          if (__builtin_amdgcn_readfirstlane(min(min(d[0], d[1]), d[2])) >= 64) break;
          __builtin_amdgcn_s_sleep(1);
        }
      }
      EST(1);
      u32x4 hold[2][2];
#pragma unroll
      for (int rd = 0; rd < 3; ++rd) {
        lds_wait_ge(fpa + 4, 4 * u + rd + 1);
#pragma unroll
        for (int i = 0; i < 4; ++i) {
          f32x4 v;
          if (kh == 0) v = f32x4{o[3 + rd][4 * i], o[3 + rd][4 * i + 1], o[3 + rd][4 * i + 2], o[3 + rd][4 * i + 3]};
          else v = f32x4{o[rd][4 * i], o[rd][4 * i + 1], o[rd][4 * i + 2], o[rd][4 * i + 3]};
          *(LAS f32x4*)(size_t)(xw + i * 1024) = v;
        }
        lds_post(fme, 4 * u + rd + 1);
        lds_wait_ge(fpa, 4 * u + rd + 1);
        if (rd == 0) EST(2);
        f32x4 rv[4];
#pragma unroll
        for (int i = 0; i < 4; ++i) rv[i] = *(const LAS f32x4*)(size_t)(xr + i * 1024);
        if (rd == 0) {
          f32x2 stt = *(const LAS f32x2*)(size_t)(L0 + MISC + (pw * 32 + zc) * 8);
          const float M = fmaxf(m, stt[0]);
          const float as = __builtin_amdgcn_exp2f((m - M) * CSC), ao = __builtin_amdgcn_exp2f((stt[0] - M) * CSC);
          const float inv = 1.0f / (as * l + ao * stt[1]);
          ws_ = as * inv;
          wo_ = ao * inv;
        }
        lds_post(fme + 4, 4 * u + rd + 2);
#pragma unroll
        for (int i = 0; i < 4; i += 2) {
          unsigned pk[4];
#pragma unroll
          for (int j = 0; j < 2; ++j) {
            float v[4];
#pragma unroll
            for (int k = 0; k < 4; ++k) {
              const float own = kh == 0 ? o[rd][4 * (i + j) + k] : o[3 + rd][4 * (i + j) + k];
              v[k] = own * ws_ + rv[i + j][k] * wo_;
            }
            pk[2 * j] = cvtpk(v[0], v[1]);
            pk[2 * j + 1] = cvtpk(v[2], v[3]);
          }
          auto r0 = __builtin_amdgcn_permlane32_swap(pk[0], pk[2], false, false);
          auto r1 = __builtin_amdgcn_permlane32_swap(pk[1], pk[3], false, false);
          u32x4 w = {r0[0], r1[0], r0[1], r1[1]};
          const unsigned rowoff = zc * 64 + (((i + zh) ^ osw) * 16);
          if (u == 0) {
            if (rd == 0) *(LAS u32x4*)(size_t)(L0 + SIDE + r * ITB + 8192 + kh * 2048 + rowoff) = w;
            else hold[rd - 1][i >> 1] = w;
          } else {
            *(LAS u32x4*)(size_t)(L0 + 61440 + (3 * kh + rd) * ITB + 6144 + r * 2048 + rowoff) = w;
            __builtin_amdgcn_raw_buffer_store_b128(w, rsO, obase + (3 * kh + rd) * 6144 + (((i + zh) ^ osw) * 16), 0, 16);
          }
        }
      }
      EST(3);
      if (u == 0) {
        lds_wait_ge(fpa + 4, 4);
#pragma unroll
        for (int rd = 1; rd < 3; ++rd)
#pragma unroll
          for (int i = 0; i < 2; ++i)
            *(LAS u32x4*)(size_t)(L0 + SIDE + r * ITB + (rd - 1) * 4096 + kh * 2048 + zc * 64 + (((2 * i + zh) ^ osw) * 16)) = hold[rd - 1][i];
        lds_post(L0 + C_OR + 4 * wave, 1);
      }
      EST(4);
      STAMP(3 + 4 * u);
    }
  }
  {
    unsigned ln; LANEID(ln);
    constexpr unsigned TWO = 10240, TOB = 61440;
    const int ch = wave & 1, rq = wave >> 1;
    const unsigned g = ln >> 4, q4 = (ln & 15) >> 2, p4 = ln & 3, l15 = ln & 15;
    unsigned gA, gB;
    {
      auto wpiece = [&](int p) {
        const int n = 64 * p + ln, row = n / 20, pc = n % 20;
        return (unsigned)(row * (DM * 2) + ((pc ^ (((row >> 3) & 1) << 1)) * 16));
      };
      gA = wpiece(wave);
      gB = wave < 2 ? wpiece(8 + wave) : ln * 16 + (wave - 2) * 1024;
    }
    const int pc3 = wave < 6 ? wave : wave - 2;
    const unsigned gC = ln * 16 + pc3 * 1024 + 256 * 36864;
    const __amdgpu_buffer_rsrc_t rsWo = __builtin_amdgcn_make_buffer_rsrc((void*)((const char*)wo + 4 * xj * DH * DM * 2 + hq * 320), (short)0, HD * DM * 2 - (4 * xj * DH * DM * 2 + hq * 320), 0x00020000);
    const __amdgpu_buffer_rsrc_t rsOt = __builtin_amdgcn_make_buffer_rsrc(ws + WS_O + (size_t)qbase * 36864, (short)0, (512 - qbase) * 36864, 0x00020000);
    const __amdgpu_buffer_rsrc_t rsB = wave < 2 ? rsWo : rsOt;
    const unsigned mA = L0 + wave * 1024, mC = L0 + TOB + 6144 + pc3 * 1024;
    const unsigned mB = wave < 2 ? L0 + (8 + wave) * 1024 : L0 + TOB + (wave - 2) * 1024;
    const unsigned stepB = wave < 2 ? 32 * DM * 2 : 6144;
    const unsigned slotB = wave < 2 ? TWO : ITB;
    unsigned wsrc, osrc, bsrc;
    auto sethead = [&](int hs) { wsrc = hs * (DH * DM * 2); osrc = hs * (8 * 36864); bsrc = wave < 2 ? wsrc : osrc; };
    auto request = [&](int c6, int k) {
      if (k == 0) bdma(mA + c6 * TWO, rsWo, gA, wsrc + c6 * (32 * DM * 2));
      if (k == 1) bdma1(mB + c6 * slotB, rsB, gB, bsrc + c6 * stepB);
      if (k == 2) bdma1(mC + c6 * ITB, rsOt, gC, osrc + c6 * 6144);
    };
    const unsigned WAb = L0 + (8 * g + q4) * 320 + 160 * ch + 8 * p4;
    const unsigned WA0 = WAb + ((ch & 1) ? -32 * (int)(g & 1) : 32 * (int)(g & 1));
    const unsigned WA1 = WAb + ((ch & 1) ? 32 * (int)(g & 1) : -32 * (int)(g & 1));
    const unsigned OA = L0 + TOB + (48 * rq + l15) * 64 + ((g ^ (((l15 >> 2) & 1) << 1)) * 16);
    const unsigned OA2 = OA + 3 * ITB;
    f32x4 acc[3][5] = {};
    bf16x8 af[2][5], bfr[2][3];
    auto rdA = [&](int par, int sl, int cb) {
      const unsigned a = ((cb & 1) ? WA1 : WA0) + sl * TWO + cb * 32;
      af[par][cb] = cat(ldstr(a), ldstr(a + 4 * 320));
    };
    auto rdB = [&](int par, int sl, int rb) { bfr[par][rb] = lds128((sl < 3 ? OA : OA2) + (sl % 3) * ITB + rb * 1024); };
    if (wave >= 6) WAITV(0);
    BAR();
    auto tbody = [&](auto VV) {
      constexpr int V = decltype(VV)::value;
#pragma unroll
      for (int rb = 0; rb < 3; ++rb) rdB(0, 0, rb);
#pragma unroll
      for (int cb = 0; cb < 5; ++cb) rdA(0, 0, cb);
      BAR();
      STAMP(12);
      auto phase = [&](auto QQ, auto MODE) {
        constexpr int q = decltype(QQ)::value, mode = decltype(MODE)::value, cur = q & 1;
        constexpr int R0 = V == 0 ? 0 : 5, D0 = V == 0 ? 9 : 0;
#pragma unroll
        for (int i = 0; i < 15; ++i) {
          acc[i % 3][i / 3] = MFMA16(af[cur][i / 3], bfr[cur][i % 3], acc[i % 3][i / 3]);
          SBAR();
          if (i >= R0 && i < R0 + 3) rdB(cur ^ 1, (q + 1) % 6, i - R0);
          if (i >= R0 + 3 && i < R0 + 8) rdA(cur ^ 1, (q + 1) % 6, i - R0 - 3);
          if (mode == 0 && i >= D0 && i < D0 + 6 && !((i - D0) & 1)) request(q, (i - D0) >> 1);
          if (mode == 2 && q >= 3 && i >= D0 && i < D0 + 6) request(2 * (q - 3) + (i - D0) / 3, (i - D0) % 3);
          SBAR();
        }
        if (mode == 0) WAITV(12);
        if (mode == 1) { if (q == 0) WAITV(9); else if (q == 1) WAITV(6); else if (q == 2) WAITV(3); else WAITV(0); }
        if (mode == 2) {
          if (q == 0) WAITV(0);
          if (q == 2 && wave == 7) poll_flags(FL + (size_t)blockIdx.x * 32, 4, hq, TAG, ln);
          if (q == 4) WAITV(9);
          if (q == 5) WAITV(12);
        }
        BAR();
        if (mode == 2 && q == 0 && wave == 0 && ln < 4 && (int)ln != hq)
          __hip_atomic_store(FL + (size_t)(qbase + 8 * ln) * 32 + hq, TAG, __ATOMIC_RELAXED, __HIP_MEMORY_SCOPE_AGENT);
      };
      auto head6 = [&](auto MODE) {
        phase(std::integral_constant<int, 0>{}, MODE);
        phase(std::integral_constant<int, 1>{}, MODE);
        phase(std::integral_constant<int, 2>{}, MODE);
        phase(std::integral_constant<int, 3>{}, MODE);
        phase(std::integral_constant<int, 4>{}, MODE);
        phase(std::integral_constant<int, 5>{}, MODE);
      };
      sethead((hq + 1) & 3);
      head6(std::integral_constant<int, 2>{});
#pragma unroll 1
      for (int i = 2; i < 4; ++i) {
        sethead((hq + i) & 3);
        head6(std::integral_constant<int, 0>{});
      }
      STAMP(13);
      head6(std::integral_constant<int, 1>{});
    };
    if (wave < 4) tbody(std::integral_constant<int, 0>{}); else tbody(std::integral_constant<int, 1>{});
    STAMP(8);
    {
      const unsigned yw = L0 + (48 * rq + l15) * 336 + (80 * ch + 4 * g) * 2;
#pragma unroll
      for (int rb = 0; rb < 3; ++rb)
#pragma unroll
        for (int cb = 0; cb < 5; ++cb) {
          u32x2 w = {cvtpk(acc[rb][cb][0], acc[rb][cb][1]), cvtpk(acc[rb][cb][2], acc[rb][cb][3])};
          *(LAS u32x2*)(size_t)(yw + rb * 16 * 336 + cb * 32) = w;
        }
    }
    BAR();
    {
      const __amdgpu_buffer_rsrc_t rsP = __builtin_amdgcn_make_buffer_rsrc(ws + WS_P, (short)0, 2 * 8 * 4 * 8 * 30720, 0x00020000);
#pragma unroll
      for (int i = 0; i < 8; ++i) {
        const int j = wave + 8 * i;
        if (j < 60) {
          const int n = 64 * j + ln, row = n / 20, pc = n % 20, un = j >= 30 ? 1 : 0;
          const u32x4 v = *(const LAS u32x4*)(size_t)(L0 + row * 336 + pc * 16);
          __builtin_amdgcn_raw_buffer_store_b128(v, rsP, (((un * 8 + b0) * 4 + hq) * 8 + xj) * 30720 + (n - 1920 * un) * 16, 0, 16);
        }
      }
      WAITV(0);
      BAR();
      if (wave == 0) {
        if (ln < 8) __hip_atomic_store(FL + (size_t)((int)ln + 8 * xi) * 32 + 8 + xj, TAG, __ATOMIC_RELAXED, __HIP_MEMORY_SCOPE_AGENT);
        poll_flags(FL + (size_t)blockIdx.x * 32 + 8, 8, -1, TAG, ln);
      }
      BAR();
      STAMP(9);
      const int tz = wave * 64 + (int)ln;
      if (tz < 480) {
        const int u = tz / 240, e = tz % 240;
        const int pp = (((u * 8 + b0) * 4 + hq) * 8) * 30720 + 12 * xj * 320 + e * 16;
        u32x4 v[8];
#pragma unroll
        for (int j = 0; j < 8; ++j) v[j] = __builtin_amdgcn_raw_buffer_load_b128(rsP, pp + j * 30720, 0, 16);
        float a[8];
#pragma unroll
        for (int k = 0; k < 4; ++k) {
          float lo = 0.f, hi2 = 0.f;
#pragma unroll
          for (int j = 0; j < 8; ++j) { lo += __uint_as_float(v[j][k] << 16); hi2 += __uint_as_float(v[j][k] & 0xffff0000u); }
          a[2 * k] = lo; a[2 * k + 1] = hi2;
        }
        u32x4 w = {cvtpk(a[0], a[1]), cvtpk(a[2], a[3]), cvtpk(a[4], a[5]), cvtpk(a[6], a[7])};
        const int row = e / 20, c8 = e % 20;
        *(u32x4*)(out + (size_t)((b0 + 8 * u) * SQ + 12 * xj + row) * DM + hq * 160 + c8 * 8) = w;
      }
    }
    STAMP(10);
  }
}

extern "C" void kernel_launch(void* const* d_in, const int* in_sizes, int n_in, void* d_out, int out_size, void* d_ws,
                              size_t ws_size, hipStream_t stream) {
  const u16* x = (const u16*)d_in[0];
  const u16* kc = (const u16*)d_in[1];
  const u16* vc = (const u16*)d_in[2];
  const u16* wq = (const u16*)d_in[3];
  const u16* wo = (const u16*)d_in[4];
  (void)hipFuncSetAttribute((const void*)attn_kernel, hipFuncAttributeMaxDynamicSharedMemorySize, LDS_A);
  attn_kernel<<<256, 512, LDS_A, stream>>>(x, kc, vc, wq, wo, (u16*)d_out, (char*)d_ws, (unsigned long long*)((char*)d_ws + (128u << 20)));
}
```

```cpp
#include <hip/hip_runtime.h>
#include <hip/hip_bf16.h>
#include <stdint.h>
#include <type_traits>

typedef unsigned short u16;
typedef short bf16x8 __attribute__((ext_vector_type(8)));
typedef short s16x4 __attribute__((ext_vector_type(4)));
typedef float f32x16 __attribute__((ext_vector_type(16)));
typedef float f32x4 __attribute__((ext_vector_type(4)));
typedef float f32x2 __attribute__((ext_vector_type(2)));
typedef __bf16 bf16x2 __attribute__((ext_vector_type(2)));
typedef unsigned u32x4 __attribute__((ext_vector_type(4)));
typedef unsigned u32x2 __attribute__((ext_vector_type(2)));
#define LAS __attribute__((address_space(3)))
#define DI __device__ __forceinline__

constexpr int NB = 16, SQ = 96, DM = 640, NH = 32, SK = 1024, DH = 192;
constexpr int HD = NH * DH;

#define MFMA32(a, b, c) __builtin_amdgcn_mfma_f32_32x32x16_bf16((a), (b), (c), 0, 0, 0)
#define MFMA16(a, b, c) __builtin_amdgcn_mfma_f32_16x16x32_bf16((a), (b), (c), 0, 0, 0)
#define BAR() do { asm volatile("" ::: "memory"); __builtin_amdgcn_s_waitcnt(0xC07F); __builtin_amdgcn_s_barrier(); asm volatile("" ::: "memory"); } while (0)
#define WAITV(n) asm volatile("s_waitcnt vmcnt(%0)" ::"i"(n) : "memory")
#define SBAR() __builtin_amdgcn_sched_barrier(0)
#define MAX3(a, b, c) fmaxf(fmaxf((a), (b)), (c))
#define LANEID(v) asm volatile("v_mbcnt_lo_u32_b32 %0, -1, 0\n\tv_mbcnt_hi_u32_b32 %0, -1, %0" : "=v"(v))

DI unsigned cvtpk(float lo, float hi) {
  f32x2 v = {lo, hi};
  bf16x2 b = __builtin_convertvector(v, bf16x2);
  return __builtin_bit_cast(unsigned, b);
}
DI void dma16nt(unsigned lds_addr, const void* sbase, unsigned voff) {
  asm volatile("s_mov_b32 m0, %0\n\ts_nop 0\n\tglobal_load_lds_dwordx4 %1, %2 nt" ::"s"(lds_addr), "v"(voff), "s"(sbase) : "memory");
}
DI void bdma(unsigned lds_addr, __amdgpu_buffer_rsrc_t rs, unsigned voff, unsigned soff) {
  asm volatile("s_mov_b32 m0, %0\n\ts_nop 0\n\tbuffer_load_dwordx4 %1, %2, %3 offen lds" ::"s"(lds_addr), "v"(voff), "s"(rs), "s"(soff) : "memory");
}
DI void bdma1(unsigned lds_addr, __amdgpu_buffer_rsrc_t rs, unsigned voff, unsigned soff) {
  asm volatile("s_mov_b32 m0, %0\n\ts_nop 0\n\tbuffer_load_dwordx4 %1, %2, %3 offen sc1 lds" ::"s"(lds_addr), "v"(voff), "s"(rs), "s"(soff) : "memory");
}
DI void dma16(unsigned lds_addr, const void* sbase, unsigned voff) {
  asm volatile("s_mov_b32 m0, %0\n\ts_nop 0\n\tglobal_load_lds_dwordx4 %1, %2" ::"s"(lds_addr), "v"(voff), "s"(sbase) : "memory");
}
template <int I, int N, class F>
DI void sfor(F&& f) {
  if constexpr (I < N) { f(std::integral_constant<int, I>{}); sfor<I + 1, N>(f); }
}
DI bf16x8 lds128(unsigned a) { return *(const LAS bf16x8*)(size_t)a; }
DI s16x4 ldstr(unsigned a) { return __builtin_amdgcn_ds_read_tr16_b64_v4i16((LAS s16x4*)(size_t)a); }
DI bf16x8 cat(s16x4 a, s16x4 b) { return __builtin_shufflevector(a, b, 0, 1, 2, 3, 4, 5, 6, 7); }

constexpr unsigned ITB = 12288;
constexpr unsigned NSL = 5;
constexpr unsigned GRB = NSL * ITB;
constexpr unsigned SIDE = 2 * GRB;
constexpr unsigned MISC = SIDE + 36864;
constexpr unsigned CTL = MISC + 2048;
constexpr unsigned HB = MISC - 49152;
constexpr unsigned C_L = CTL;
constexpr unsigned C_R = CTL + 16;
constexpr unsigned C_QRDY = CTL + 48;
constexpr unsigned C_XF = CTL + 64;
constexpr unsigned C_H = CTL + 128;
constexpr unsigned C_OR = CTL + 160;
constexpr unsigned C_OD = CTL + 192;
constexpr unsigned LDS_A = 163840;
constexpr float CSC = 0.07216878364870322f * 1.4426950408889634f;
constexpr float THR_RAW = 8.0f / CSC;
constexpr int SPIN_MAX = 1 << 20;
#define HD_D 4
#define TL_D 4
#define KV0 2

#define STAMP(i)
#define EST(i)
#define FST(i)

DI void lds_post(unsigned a, unsigned v) {
  asm volatile("s_waitcnt lgkmcnt(0)\n\tds_write_b32 %0, %1" ::"v"(a), "v"(v) : "memory");
}
DI void lds_post_nw(unsigned a, unsigned v) {
  asm volatile("ds_write_b32 %0, %1" ::"v"(a), "v"(v) : "memory");
}
DI void lds_wait_ge(unsigned a, unsigned n) {
#pragma nounroll
  for (int it = 0; it < SPIN_MAX; ++it) {
    unsigned v;
    asm volatile("ds_read_b32 %0, %1\n\ts_waitcnt lgkmcnt(0)" : "=v"(v) : "v"(a) : "memory");
    if (__builtin_amdgcn_readfirstlane(v) >= n) break;
    __builtin_amdgcn_s_sleep(1);
  }
}
DI void lds_wait_min4(unsigned a, unsigned n) {
#pragma nounroll
  for (int it = 0; it < SPIN_MAX; ++it) {
    u32x4 d;
    asm volatile("ds_read_b128 %0, %1\n\ts_waitcnt lgkmcnt(0)" : "=v"(d) : "v"(a) : "memory");
    const unsigned mn = min(min(d[0], d[1]), min(d[2], d[3]));
    if (__builtin_amdgcn_readfirstlane(mn) >= n) break;
    __builtin_amdgcn_s_sleep(1);
  }
}
DI unsigned getvm() {
  unsigned sts;
  asm volatile("s_getreg_b32 %0, hwreg(HW_REG_IB_STS)" : "=s"(sts)::"memory");
  return (sts & 15u) | ((sts >> 18) & 0x30u);
}
DI unsigned lds_peek(unsigned a) { return *(volatile LAS unsigned*)(size_t)a; }

typedef __attribute__((address_space(1))) unsigned long long gu64;
extern "C" __device__ unsigned long long amd_dispatch_id() __asm("llvm.amdgcn.dispatch.id");
constexpr size_t WS_O = 0, WS_P = 32u << 20, WS_F = 96u << 20;

DI bool poll_flags(gu64* f, int nl, int skip, unsigned long long T, int lane) {
#pragma nounroll
  for (int it = 0; it < SPIN_MAX; ++it) {
    unsigned long long v = T;
    if (lane < nl && lane != skip) v = __hip_atomic_load(f + lane, __ATOMIC_RELAXED, __HIP_MEMORY_SCOPE_AGENT);
    if (__all(v == T)) {
      if (lane < nl && lane != skip) __hip_atomic_store(f + lane, 0ull, __ATOMIC_RELAXED, __HIP_MEMORY_SCOPE_AGENT);
      return true;
    }
    __builtin_amdgcn_s_sleep(2);
  }
  return false;
}

constexpr int hNL(int j) { return j < 20 ? 3 : 0; }
constexpr int hKV(int p) { return (p >= KV0 && p < KV0 + 12) ? 1 : 0; }
constexpr int hPend(int j) {
  int cnt = 0, first = 0;
  if (j < HD_D) { cnt = 3 * (HD_D - 1 - j); first = 0; }
  else { cnt = hKV(j - HD_D); first = j - HD_D + 1; }
  for (int p = first; p < j; ++p) cnt += hNL(p + HD_D) + hKV(p);
  return cnt;
}
constexpr int tNL(int j) { return j < 24 ? 3 : 0; }
constexpr int tPend(int j) {
  int cnt = 0, first = 0;
  if (j < TL_D) { cnt = 3 * (TL_D - 1 - j); first = 0; }
  else { cnt = 0; first = j - TL_D + 1; }
  for (int p = first; p < j; ++p) cnt += tNL(p + TL_D);
  return cnt;
}

__global__ __launch_bounds__(512, 2) void attn_kernel(const u16* x, const u16* kc, const u16* vc, const u16* wq, const u16* wo,
                                                      u16* out, char* ws, unsigned long long* stamps) {
  extern __shared__ __attribute__((aligned(16))) char lds[];
  const unsigned L0 = (unsigned)(size_t)(LAS char*)lds;
  const int tid = threadIdx.x, lane = tid & 63;
  const int wave = __builtin_amdgcn_readfirstlane(tid >> 6);
  const int xj = blockIdx.x & 7, xi = blockIdx.x >> 3;
  const int hq = xi & 3, h = 4 * xj + hq, b0 = xi >> 2;
  const size_t UB0 = (size_t)(b0 * NH + h) * SK * 384, UB1 = (size_t)((b0 + 8) * NH + h) * SK * 384;
  unsigned long long TAG;
  {
    const unsigned long long did = amd_dispatch_id(), dp = (unsigned long long)__builtin_amdgcn_dispatch_ptr();
    const unsigned t32 = (unsigned)(did * 0x9E3779B1ull) ^ (unsigned)(dp >> 6) * 0x85EBCA77u ^ (unsigned)(did >> 32);
    TAG = ((unsigned long long)(t32 ^ 0x5A17C3E9u) << 32) | t32;
  }
  const int qbase = xj + 32 * b0;
  gu64* FL = (gu64*)(ws + WS_F);
  const int kh = (0x1A >> wave) & 1, r = (0x102210 >> (4 * wave)) & 3;
  STAMP(0);
  bf16x8 qf[12];

  unsigned offK[6], offV[3];
  {
    if (wave == 6 && lane < 64) *(LAS unsigned*)(size_t)(L0 + CTL + lane * 4) = (lane == 46 || lane == 47) ? 1u : 0u;
    const int rot = (b0 * 5 / 4 + hq * 5 / 2) % 10;
    const int dh = wave & 1, rq = wave >> 1;
    const unsigned g = lane >> 4, q4 = (lane & 15) >> 2, p4 = lane & 3, l15 = lane & 15;
    const __amdgpu_buffer_rsrc_t rsW = __builtin_amdgcn_make_buffer_rsrc((void*)((const char*)wq + h * DH * 2), (short)0, DM * HD * 2 - h * DH * 2, 0x00020000);
    const __amdgpu_buffer_rsrc_t rsX = __builtin_amdgcn_make_buffer_rsrc((void*)((const char*)x + b0 * SQ * DM * 2), (short)0, (NB - b0) * SQ * DM * 2, 0x00020000);
    unsigned gA, gB, gC;
    {
      auto wpiece = [&](int p) {
        const int n = 64 * p + lane, row = n / 24, pc = n % 24;
        const int sw = ((row >> 1) & 1) | (((row >> 3) & 1) << 1);
        return (unsigned)(row * (HD * 2) + ((pc ^ (sw << 1)) * 16));
      };
      auto xpiece = [&](int p) {
        const int row = 8 * p + (lane >> 3);
        return (unsigned)(row * (DM * 2) + (((lane & 7) ^ ((row >> 1) & 7)) * 16));
      };
      gA = wpiece(wave);
      gB = wave < 4 ? wpiece(8 + wave) : xpiece(wave - 4);
      gC = xpiece(4 + wave);
    }
    unsigned hK[3], hV[3];
    constexpr unsigned WB = 3 * ITB, G1X = GRB + 2 * ITB, U1 = 8 * SQ * DM * 2;
    constexpr unsigned WSTEP = 32 * HD * 2;
    const unsigned mW = L0 + WB + wave * 1024;
    const unsigned mX = L0 + (4 + wave) * 1024;
    const unsigned s2 = ((q4 >> 1) & 1) | ((g & 1) << 1);
    unsigned E[4];
    const unsigned XA0 = L0 + (rq >> 1) * G1X + (48 * (rq & 1) + l15) * 128 + ((g ^ ((l15 >> 1) & 7)) * 16), XA1 = XA0 ^ 64;
    f32x4 acc[6][3] = {};
    bf16x8 af[2][6], bfr[2][3];
    unsigned xa;
    auto rdA = [&](int par, int c1, int db) {
      const unsigned a = E[db & 3] + (db >> 2) * 128 + (c1 & 3) * ITB;
      af[par][db] = cat(ldstr(a), ldstr(a + 4 * 384));
    };
    auto rdB = [&](int par, int rb) { bfr[par][rb] = lds128(xa + rb * 2048); };
    unsigned woff = (unsigned)(2 * rot) * WSTEP, xoff = (unsigned)rot * 128;
    auto advW = [&]() { woff += WSTEP; woff = woff == 20 * WSTEP ? 0u : woff; };
    auto advX = [&]() { xoff += 128; xoff = xoff == 1280 ? 0u : xoff; };
    auto request = [&](auto VV, int k, int wsl, unsigned xs, int un) {
      constexpr int V = decltype(VV)::value;
      if (k == 0) bdma(mW + wsl * ITB, rsW, gA, woff);
      if (k == 1) {
        if (V == 0) bdma(mW + 8192 + wsl * ITB, rsW, gB, woff);
        else bdma(mX - 8192 + un * G1X + xs, rsX, gB, xoff + un * U1);
      }
      if (k == 2) bdma(mX + un * G1X + xs, rsX, gC, xoff + un * U1);
    };
    unsigned xr = 2 * ITB;
    unsigned xw = 1 * ITB;
    auto body = [&](auto VV) {
      constexpr int V = decltype(VV)::value;
      if (V == 1) bdma(mX - 8192 + 2 * ITB, rsX, gB, xoff);
      bdma(mX + 2 * ITB, rsX, gC, xoff);
#pragma unroll
      for (int c = -4; c < 0; ++c) {
        if ((c & 1) != 0) advX();
        const unsigned xs = (unsigned)((((c + 1) >> 1) + 2 + 2) % 3) * ITB;
#pragma unroll
        for (int k = 0; k < 3; ++k) request(VV, k, c & 3, xs, (c + 1) & 1);
        advW();
      }
      SBAR();
      asm volatile("" ::: "memory");
      if (wave >= 6) {
        const char* wsrc = (const char*)wo + (size_t)(4 * xj * DH + 96 * b0 + 48 * (wave - 6)) * (DM * 2) + hq * 320;
        const unsigned o0 = (lane >> 2) * (DM * 2) + min((unsigned)(lane & 3) * 128u, 316u);
#pragma unroll
        for (int z = 0; z < 3; ++z)
          asm volatile("s_mov_b32 m0, %0\n\ts_nop 0\n\tglobal_load_lds_dword %1, %2" ::"s"(L0 + MISC + 1536), "v"(o0 + z * 16 * DM * 2), "s"(wsrc) : "memory");
        const char* t0 = lane == 0 ? ws + WS_O + (size_t)blockIdx.x * 36864 : lane == 1 ? ws + WS_O + (size_t)(256 + blockIdx.x) * 36864
                       : lane == 2 ? ws + WS_P + (size_t)(((b0 * 4 + hq) * 8 + xj) * 30720) : lane == 3 ? ws + WS_P + (size_t)((((8 + b0) * 4 + hq) * 8 + xj) * 30720)
                       : lane == 4 ? ws + WS_F + (size_t)blockIdx.x * 256 : (const char*)out + (size_t)((b0 + 8 * (lane & 1)) * SQ + 12 * xj) * DM * 2;
        if (wave == 7 && lane < 7)
          asm volatile("s_mov_b32 m0, %0\n\ts_nop 0\n\tglobal_load_lds_dword %1, off" ::"s"(L0 + MISC + 1536), "v"(t0) : "memory");
      }
#pragma unroll
    for (int j = 0; j < 6; ++j) {
      int n = 64 * j + lane, row = n / 24, pc = n % 24;
      offK[j] = row * 384 + ((pc ^ ((row >> 1) & 7)) * 16);
    }
#pragma unroll
    for (int j = 0; j < 3; ++j) {
      int n = 64 * j + lane, row = n / 24, pc = n % 24;
      offV[j] = row * 384 + ((pc ^ (((row >> 1) & 1) << 2)) * 16);
    }
#pragma unroll
    for (int j = 0; j < 3; ++j) {
      const int n = 64 * (3 * (wave & 3) + j) + lane, row = n / 24, pc = n % 24;
      hK[j] = row * 384 + ((pc ^ ((row >> 1) & 7)) * 16);
      hV[j] = row * 384 + ((pc ^ (((row >> 1) & 1) << 2)) * 16);
    }
#pragma unroll
      for (int j = 0; j < 4; ++j) E[j] = L0 + WB + (8 * g + q4) * 384 + 32 * ((unsigned)(6 * dh + j) ^ s2) + 8 * p4;
      WAITV(9);
      BAR();
      xa = XA0 + 2 * ITB;
#pragma unroll
      for (int rb = 0; rb < 3; ++rb) rdB(0, rb);
#pragma unroll
      for (int db = 0; db < 6; ++db) rdA(0, 0, db);
      WAITV(6);
      BAR();
      STAMP(4);
      auto phase = [&](int c, auto QQ, auto MODE) {
        constexpr int q = decltype(QQ)::value, mode = decltype(MODE)::value, cur = q & 1;
        if (cur == 1) {
          xr = xr == 2 * ITB ? 0 : xr + ITB;
          if (mode == 0) { xw = xw == 2 * ITB ? 0 : xw + ITB; advX(); }
        }
        xa = (cur == 1 ? XA0 : XA1) + xr;
        constexpr int R0 = V == 0 ? 0 : 5, D0 = V == 0 ? 10 : 0;
#pragma unroll
        for (int i = 0; i < 18; ++i) {
          acc[i / 3][i % 3] = MFMA16(af[cur][i / 3], bfr[cur][i % 3], acc[i / 3][i % 3]);
          SBAR();
          if (i >= R0 && i < R0 + 3) rdB(cur ^ 1, i - R0);
          if (i >= R0 + 3 && i < R0 + 9) rdA(cur ^ 1, q + 1, i - R0 - 3);
          if (mode == 0 && i >= D0 && i < D0 + 6 && !((i - D0) & 1)) request(VV, (i - D0) >> 1, q, xw, (q + 1) & 1);
          if (mode == 0 && i == D0 + 5) advW();
          if (mode == 2 && i >= D0 && i < D0 + 6 && !((i - D0) & 1)) {
            const int k = (i - D0) >> 1, li = c - 17;
            const unsigned dst = L0 + V * G1X + li * ITB + (3 * (wave & 3) + k) * 1024;
            if (li == 2) dma16nt(dst, (const char*)vc + UB0 + (size_t)(32 * V) * 384, hV[k]);
            else dma16nt(dst, (const char*)kc + UB0 + (size_t)(64 * li + 32 * V) * 384, hK[k]);
          }
          SBAR();
        }
        if (mode == 0) WAITV(6);
        if (mode == 1) WAITV(0);
        BAR();
      };
      using I0 = std::integral_constant<int, 0>; using I1 = std::integral_constant<int, 1>;
      using I2 = std::integral_constant<int, 2>; using I3 = std::integral_constant<int, 3>;
#pragma unroll 1
      for (int c = 0; c < 16; c += 4) {
        phase(c, I0{}, I0{});
        phase(c + 1, I1{}, I0{});
        phase(c + 2, I2{}, I0{});
        phase(c + 3, I3{}, I0{});
      }
      STAMP(5);
      phase(16, I0{}, I1{});
      phase(17, I1{}, I2{});
      phase(18, I2{}, I2{});
      phase(19, I3{}, I2{});
    };
    if (wave < 4) body(std::integral_constant<int, 0>{}); else body(std::integral_constant<int, 1>{});
    STAMP(11);
    auto qstore = [&]() {
      unsigned lz; LANEID(lz);
      const unsigned zg = lz >> 4, zl = lz & 15;
#pragma unroll
      for (int rb = 0; rb < 3; ++rb) {
        const int R16 = 3 * (rq & 1) + rb;
        const unsigned qb = L0 + SIDE + ((R16 >> 1) * 12 + 6 * dh) * 1024 + (16 * (R16 & 1) + zl + 32 * (zg >> 1)) * 16 + 8 * (zg & 1);
#pragma unroll
        for (int db = 0; db < 6; ++db) {
          u32x2 w = {cvtpk(acc[db][rb][0], acc[db][rb][1]), cvtpk(acc[db][rb][2], acc[db][rb][3])};
          *(LAS u32x2*)(size_t)(qb + db * 1024) = w;
        }
      }
    };
    if (wave < 4) qstore();
    BAR();
    if (wave < 6) {
      unsigned lz; LANEID(lz);
#pragma unroll
      for (int ks = 0; ks < 12; ++ks) qf[ks] = lds128(L0 + SIDE + ((r * 12 + ks) * 64 + lz) * 16);
    }
    BAR();
    if (wave >= 4) { qstore(); lds_post(L0 + C_QRDY + 4 * (wave - 4), 1); }
  }
  STAMP(1);

  if (wave >= 6) {
    const int hw = wave - 6;
    __builtin_amdgcn_s_setprio(3);
    unsigned ll; LANEID(ll);
    const unsigned aL = L0 + C_L + 4 * hw, aR = L0 + C_R + 16 * hw;
    auto issue_item = [&](unsigned li_, unsigned slot_) {
      const unsigned j = li_ & 31;
      const bool isK = (j == 0) || ((j & 1) && j < 31);
      const unsigned tile = isK ? (j + 1) >> 1 : (j - 1) >> 1;
      const size_t ub = ((li_ >> 5) ? UB1 : UB0) + (size_t)(64 * tile + 32 * hw) * 384;
      const unsigned dst = L0 + hw * GRB + slot_;
      if (isK) {
        const char* src = (const char*)kc + ub;
#pragma unroll
        for (int q = 0; q < 12; ++q) dma16nt(dst + q * 1024, src + (q / 6) * 6144, offK[q % 6]);
      } else {
        const char* src = (const char*)vc + ub;
#pragma unroll
        for (int q = 0; q < 12; ++q) dma16nt(dst + q * 1024, src + (q / 3) * 3072, offV[q % 3]);
      }
    };
    issue_item(3, hw ? 0 : 3 * ITB);
    issue_item(4, hw ? ITB : 4 * ITB);
    WAITV(30); lds_post_nw(L0 + C_H + 4 * wave, 1); lds_wait_min4(L0 + C_H + 16 * hw, 1); lds_post_nw(aL, 1);
    WAITV(27); lds_post_nw(L0 + C_H + 4 * wave, 2); lds_wait_min4(L0 + C_H + 16 * hw, 2); lds_post_nw(aL, 2);
    WAITV(24); lds_post_nw(L0 + C_H + 4 * wave, 3); lds_wait_min4(L0 + C_H + 16 * hw, 3); lds_post_nw(aL, 3);
    unsigned li = 5, lpub = 3, slot = hw ? 2 * ITB : 0;
    unsigned sb = 0, ost = 0;
    bool ordy = false;
    const __amdgpu_buffer_rsrc_t rsO = __builtin_amdgcn_make_buffer_rsrc(ws + WS_O, (short)0, 2 * 256 * 36864, 0x00020000);
    auto obatch = [&]() {
#pragma unroll
      for (int k = 0; k < 3; ++k) {
        const unsigned p = 18 * hw + 3 * ost + k, rr = p / 12, q = p % 12;
        const unsigned c6 = (0x305241u >> (4 * (q >> 1))) & 7;
        const u32x4 v = *(const LAS u32x4*)(size_t)(L0 + SIDE + p * 1024 + ll * 16);
        __builtin_amdgcn_raw_buffer_store_b128(v, rsO, (int)(blockIdx.x * 36864 + c6 * 6144 + rr * 2048 + (q & 1) * 1024 + ll * 16), 0, 16);
      }
      ++ost;
      if (ost == 6) lds_post(L0 + C_OD + 4 * hw, 1);
    };
    auto ocheck = [&]() {
      u32x4 d0, d1;
      asm volatile("ds_read_b128 %0, %2\n\tds_read_b128 %1, %2 offset:16\n\ts_waitcnt lgkmcnt(0)" : "=&v"(d0), "=&v"(d1) : "v"(L0 + C_OR) : "memory");
      const unsigned mn = min(min(min(d0[0], d0[1]), min(d0[2], d0[3])), min(min(d1[0], d1[1]), min(d1[2], d1[3])));
      return __builtin_amdgcn_readfirstlane(mn) >= 1;
    };
#pragma nounroll
    for (int it = 0; it < (SPIN_MAX << 2); ++it) {
      bool idle = true;
      if (li < 64) {
        u32x4 d;
        asm volatile("ds_read_b128 %0, %1\n\ts_waitcnt lgkmcnt(0)" : "=v"(d) : "v"(aR) : "memory");
        const unsigned mn = __builtin_amdgcn_readfirstlane(min(min(d[0], d[1]), d[2]));
        if (mn + NSL > li && getvm() <= 48) {
          issue_item(li, slot);
          if (li >= 32 && ost < 6) {
            if (!ordy) ordy = ocheck();
            if (ordy) { obatch(); sb |= 1u << (li & 31); }
          }
          ++li;
          slot = slot + ITB == GRB ? 0 : slot + ITB;
          idle = false;
        }
      }
      if (lpub < li) {
        const unsigned vm = getvm();
        unsigned done = lpub;
#pragma nounroll
        while (done < li) {
          const unsigned n = li - done;
          const unsigned rotm = (sb >> (done & 31)) | (sb << ((32 - (done & 31)) & 31));
          const unsigned after = 12 * (n - 1) + 3 * __builtin_popcount(rotm & ((1u << n) - 1));
          if (vm > after) break;
          sb &= ~(1u << (done & 31));
          ++done;
        }
        if (done > lpub) {
          lpub = done;
          lds_post_nw(aL, lpub);
          idle = false;
        }
      } else if (li == 64) {
        break;
      }
      if (idle) __builtin_amdgcn_s_sleep(1);
    }
    __builtin_amdgcn_s_setprio(0);
#pragma nounroll
    for (int it = 0; it < SPIN_MAX && ost < 6; ++it) { if (ocheck()) obatch(); else __builtin_amdgcn_s_sleep(1); }
    WAITV(0);
    lds_post(L0 + C_OD + 4 * hw, 2);
    {
      constexpr unsigned TWO = 10240, TOB = 61440;
      u32x4 d;
#pragma nounroll
      for (int it = 0; it < SPIN_MAX; ++it) {
        asm volatile("ds_read_b128 %0, %1\n\ts_waitcnt lgkmcnt(0)" : "=v"(d) : "v"(aR) : "memory");
        if (__builtin_amdgcn_readfirstlane(min(min(d[0], d[1]), d[2])) >= 64) break;
        __builtin_amdgcn_s_sleep(1);
      }
      lds_post_nw(L0 + C_H + 4 * wave, 8);
      lds_wait_ge(L0 + C_H + 4 * (13 - wave), 8);
      lds_wait_ge(L0 + C_OD + 4 * (1 - hw), 2);
      const __amdgpu_buffer_rsrc_t rsWo = __builtin_amdgcn_make_buffer_rsrc((void*)((const char*)wo + 4 * xj * DH * DM * 2 + hq * 320), (short)0, HD * DM * 2 - (4 * xj * DH * DM * 2 + hq * 320), 0x00020000);
      unsigned go[5];
#pragma unroll
      for (int p = 0; p < 5; ++p) {
        const int n = 64 * p + ll, row = n / 20, pc = n % 20;
        go[p] = row * (DM * 2) + ((pc ^ (((row >> 3) & 1) << 1)) * 16);
      }
#pragma unroll 1
      for (int c6 = 0; c6 < 6; ++c6) {
#pragma unroll
        for (int p = 0; p < 5; ++p) bdma(L0 + c6 * TWO + (5 * hw + p) * 1024, rsWo, go[p], (hq * DH + 32 * c6 + 16 * hw) * (DM * 2));
#pragma unroll
        for (int p = 0; p < 3; ++p) bdma1(L0 + TOB + c6 * ITB + (3 * hw + p) * 1024, rsO, ll * 16, blockIdx.x * 36864 + c6 * 6144 + (3 * hw + p) * 1024);
      }
    }
  } else {
    unsigned ln; LANEID(ln);
    unsigned kof[4], vE, vO;
    {
      const unsigned a32 = ln & 31, ahi = ln >> 5, ablk = (ln >> 4) & 1, aq4 = (ln & 15) >> 2, ap4 = ln & 3, axq = (aq4 >> 1) & 1;
#pragma unroll
      for (int i = 0; i < 4; ++i) kof[i] = L0 + kh * GRB + a32 * 384 + (((2 * i + ahi) ^ ((a32 >> 1) & 7)) * 16);
      vE = L0 + kh * GRB + (4 * ahi + aq4) * 384 + 64 * axq + 32 * ablk + 8 * ap4;
      vO = L0 + kh * GRB + (4 * ahi + aq4) * 384 - 64 * axq + 32 * ablk + 8 * ap4;
    }
    const unsigned aF = L0 + C_L + 4 * kh, aR = L0 + C_R + 16 * kh + 4 * r;
    const int pw = (0x102354 >> (4 * wave)) & 7;
    const unsigned fme = L0 + C_XF + 8 * wave, fpa = L0 + C_XF + 8 * pw;
    unsigned cur = kh ? 2 * ITB : 0;
    unsigned cnt = 0;
#define ADV() do { cur = (cur + ITB == GRB) ? 0u : cur + ITB; ++cnt; } while (0)
#define KADDR(ks) (((ks) & 3) == 0 ? k0 : ((ks) & 3) == 1 ? k1 : ((ks) & 3) == 2 ? k2 : k3) + 128 * ((ks) >> 2)
#define VADDR(j) ((((j) % 6) & 1) ? vbO : vbE) + (16 * ((j) / 6)) * 384 + 64 * ((j) % 6)

#pragma unroll 1
    for (int u = 0; u < 2; ++u) {
      float m, l = 0.f;
      f32x16 o[6] = {};
      f32x16 sB;
      float pe[16];
      bf16x8 kf[12], vf[12];
      unsigned k0, k1, k2, k3, vbE, vbO;
      if (u == 0) { WAITV(6); lds_post_nw(L0 + C_H + 4 * wave, 1); }
      lds_wait_ge(aF, cnt + 1);
      k0 = kof[0] + cur; k1 = kof[1] + cur; k2 = kof[2] + cur; k3 = kof[3] + cur;
#pragma unroll
      for (int ks = 0; ks < 12; ++ks) kf[ks] = lds128(KADDR(ks));
      ADV();
      lds_post_nw(aR, cnt);
      SBAR();
      {
        f32x16 z = {};
        sB = MFMA32(kf[0], qf[0], z);
#pragma unroll
        for (int ks = 1; ks < 12; ++ks) sB = MFMA32(kf[ks], qf[ks], sB);
      }
      SBAR();
      if (u == 0) { WAITV(3); lds_post_nw(L0 + C_H + 4 * wave, 2); }
      lds_wait_ge(aF, cnt + 1);
      if (u == 0) { WAITV(0); lds_post_nw(L0 + C_H + 4 * wave, 3); }
      k0 = kof[0] + cur; k1 = kof[1] + cur; k2 = kof[2] + cur; k3 = kof[3] + cur;
#pragma unroll
      for (int ks = 0; ks < 4; ++ks) kf[ks] = lds128(KADDR(ks));
      {
        float pmax = sB[0];
#pragma unroll
        for (int i = 1; i < 16; ++i) pmax = fmaxf(pmax, sB[i]);
        auto rr = __builtin_amdgcn_permlane32_swap(__float_as_uint(pmax), __float_as_uint(pmax), false, false);
        m = fmaxf(__uint_as_float(rr[0]), __uint_as_float(rr[1]));
        const float mc = -m * CSC;
#pragma unroll
        for (int i = 0; i < 16; ++i) pe[i] = fmaf(sB[i], CSC, mc);
      }
      SBAR();
      bf16x8 pa[2];
      auto expstep = [&](int i) {
        pe[2 * i] = __builtin_amdgcn_exp2f(pe[2 * i]);
        pe[2 * i + 1] = __builtin_amdgcn_exp2f(pe[2 * i + 1]);
        l += pe[2 * i];
        l += pe[2 * i + 1];
        asm volatile("" : "+v"(pe[2 * i]), "+v"(pe[2 * i + 1]), "+v"(l));
      };
      auto cvtstep = [&](int s) {
        u32x4 w = {cvtpk(pe[8 * s], pe[8 * s + 1]), cvtpk(pe[8 * s + 2], pe[8 * s + 3]), cvtpk(pe[8 * s + 4], pe[8 * s + 5]),
                   cvtpk(pe[8 * s + 6], pe[8 * s + 7])};
        asm volatile("" : "+v"(w));
        pa[s] = __builtin_bit_cast(bf16x8, w);
      };
#pragma unroll 1
      for (int t = 0; t < 15; ++t) {
        unsigned fl = 0;
#pragma unroll
        for (int i = 0; i < 12; ++i) {
          if (i == 0) { f32x16 z = {}; sB = MFMA32(kf[0], qf[0], z); }
          else sB = MFMA32(kf[i], qf[i], sB);
          if (i < 8) kf[i + 4] = lds128(KADDR(i + 4));
          if (i == 2) fl = lds_peek(aF);
          if (i == 7) { ADV(); lds_post_nw(aR, cnt); }
          if (i < 8) expstep(i);
          if (i == 8) {
            if (__builtin_amdgcn_readfirstlane(fl) < cnt + 1) lds_wait_ge(aF, cnt + 1);
            vbE = vE + cur; vbO = vO + cur;
            cvtstep(0);
          }
          if (i == 9) cvtstep(1);
          if (i >= 8) vf[i - 8] = cat(ldstr(VADDR(i - 8)), ldstr(VADDR(i - 8) + 8 * 384));
          SBAR();
        }
        float pmax = 0.f, alpha = 1.f, mc = 0.f;
        bool resc = false;
#pragma unroll
        for (int j = 0; j < 12; ++j) {
          o[j % 6] = MFMA32(vf[j], pa[j / 6], o[j % 6]);
          if (j < 8) vf[j + 4] = cat(ldstr(VADDR(j + 4)), ldstr(VADDR(j + 4) + 8 * 384));
          if (j == 2) fl = lds_peek(aF);
          if (j == 7) { ADV(); lds_post_nw(aR, cnt); }
          if (j == 1) {
            asm volatile("" : "+v"(sB));
            pmax = MAX3(MAX3(sB[0], sB[1], sB[2]), MAX3(sB[3], sB[4], sB[5]), MAX3(sB[6], sB[7], sB[8]));
            asm volatile("" : "+v"(pmax));
          }
          if (j == 2) {
            pmax = MAX3(MAX3(sB[9], sB[10], sB[11]), MAX3(sB[12], sB[13], sB[14]), MAX3(sB[15], sB[15], pmax));
            asm volatile("" : "+v"(pmax));
          }
          if (j == 3) {
            auto rr = __builtin_amdgcn_permlane32_swap(__float_as_uint(pmax), __float_as_uint(pmax), false, false);
            pmax = fmaxf(__uint_as_float(rr[0]), __uint_as_float(rr[1]));
            resc = pmax - m > THR_RAW;
            const float mn = resc ? pmax : m;
            alpha = __builtin_amdgcn_exp2f((m - mn) * CSC);
            m = mn;
            mc = -mn * CSC;
            l *= alpha;
            asm volatile("" : "+v"(m), "+v"(mc), "+v"(l), "+v"(alpha));
          }
          if (j >= 4 && j < 8) {
#pragma unroll
            for (int i = 4 * (j - 4); i < 4 * (j - 4) + 4; ++i) pe[i] = fmaf(sB[i], CSC, mc);
            asm volatile("" : "+v"(pe[4 * (j - 4)]), "+v"(pe[4 * (j - 4) + 1]), "+v"(pe[4 * (j - 4) + 2]), "+v"(pe[4 * (j - 4) + 3]));
          }
          if (j == 8) {
            if (t < 14 && __builtin_amdgcn_readfirstlane(fl) < cnt + 1) lds_wait_ge(aF, cnt + 1);
            k0 = kof[0] + cur; k1 = kof[1] + cur; k2 = kof[2] + cur; k3 = kof[3] + cur;
          }
          if (j >= 8) kf[j - 8] = lds128(KADDR(j - 8));
          SBAR();
        }
        if (__any(resc)) {
#pragma unroll
          for (int d = 0; d < 6; ++d)
#pragma unroll
            for (int i = 0; i < 16; ++i) o[d][i] *= alpha;
        }
      }
      {
        lds_wait_ge(aF, cnt + 1);
        vbE = vE + cur; vbO = vO + cur;
#pragma unroll
        for (int j = 0; j < 6; ++j) vf[j] = cat(ldstr(VADDR(j)), ldstr(VADDR(j) + 8 * 384));
        SBAR();
#pragma unroll
        for (int i = 0; i < 8; ++i) expstep(i);
        cvtstep(0);
        cvtstep(1);
        SBAR();
#pragma unroll
        for (int j = 0; j < 12; ++j) {
          o[j % 6] = MFMA32(vf[j], pa[j / 6], o[j % 6]);
          if (j < 6) vf[j + 6] = cat(ldstr(VADDR(j + 6)), ldstr(VADDR(j + 6) + 8 * 384));
          if (j == 5) { ADV(); lds_post_nw(aR, cnt); }
          SBAR();
        }
        if (u == 0) {
          lds_wait_min4(L0 + C_QRDY, 1);
#pragma unroll
          for (int ks = 0; ks < 12; ++ks) qf[ks] = lds128(L0 + SIDE + ((r * 12 + ks) * 64 + ln) * 16);
        }
        lds_post(fme + 4, 4 * u + 1);
      }
      STAMP(2 + 4 * u);
      EST(0);
      {
        auto rr = __builtin_amdgcn_permlane32_swap(__float_as_uint(l), __float_as_uint(l), false, false);
        l = __uint_as_float(rr[0]) + __uint_as_float(rr[1]);
      }
      unsigned lz; LANEID(lz);
      const int zc = lz & 31, zh = lz >> 5;
      if (zh == 0) *(LAS f32x2*)(size_t)(L0 + MISC + (wave * 32 + zc) * 8) = f32x2{m, l};
      float ws_ = 0.f, wo_ = 0.f;
      const __amdgpu_buffer_rsrc_t rsO = __builtin_amdgcn_make_buffer_rsrc(ws + WS_O, (short)0, 2 * 256 * 36864, 0x00020000);
      const int osw = ((zc >> 2) & 1) << 1;
      const int obase = (256 + blockIdx.x) * 36864 + (32 * r + zc) * 64;
      const unsigned xb_ = L0 + SIDE + (u == 0 ? r * ITB : ITB + r * 8192);
      const unsigned xw = xb_ + kh * 4096 + lz * 16, xr = xb_ + (1 - kh) * 4096 + lz * 16;
      if (u == 1) {
        lds_wait_ge(L0 + C_OD, 1);
        lds_wait_ge(L0 + C_OD + 4, 1);
#pragma nounroll
        for (int it = 0; it < SPIN_MAX; ++it) {
          u32x4 d;
          asm volatile("ds_read_b128 %0, %1\n\ts_waitcnt lgkmcnt(0)" : "=v"(d) : "v"(L0 + C_R + 16) : "memory");
          if (__builtin_amdgcn_readfirstlane(min(min(d[0], d[1]), d[2])) >= 64) break;
          __builtin_amdgcn_s_sleep(1);
        }
      }
      EST(1);
      u32x4 hold[2][2];
#pragma unroll
      for (int rd = 0; rd < 3; ++rd) {
        lds_wait_ge(fpa + 4, 4 * u + rd + 1);
#pragma unroll
        for (int i = 0; i < 4; ++i) {
          f32x4 v;
          if (kh == 0) v = f32x4{o[3 + rd][4 * i], o[3 + rd][4 * i + 1], o[3 + rd][4 * i + 2], o[3 + rd][4 * i + 3]};
          else v = f32x4{o[rd][4 * i], o[rd][4 * i + 1], o[rd][4 * i + 2], o[rd][4 * i + 3]};
          *(LAS f32x4*)(size_t)(xw + i * 1024) = v;
        }
        lds_post(fme, 4 * u + rd + 1);
        lds_wait_ge(fpa, 4 * u + rd + 1);
        if (rd == 0) EST(2);
        f32x4 rv[4];
#pragma unroll
        for (int i = 0; i < 4; ++i) rv[i] = *(const LAS f32x4*)(size_t)(xr + i * 1024);
        if (rd == 0) {
          f32x2 stt = *(const LAS f32x2*)(size_t)(L0 + MISC + (pw * 32 + zc) * 8);
          const float M = fmaxf(m, stt[0]);
          const float as = __builtin_amdgcn_exp2f((m - M) * CSC), ao = __builtin_amdgcn_exp2f((stt[0] - M) * CSC);
          const float inv = 1.0f / (as * l + ao * stt[1]);
          ws_ = as * inv;
          wo_ = ao * inv;
        }
        lds_post(fme + 4, 4 * u + rd + 2);
#pragma unroll
        for (int i = 0; i < 4; i += 2) {
          unsigned pk[4];
#pragma unroll
          for (int j = 0; j < 2; ++j) {
            float v[4];
#pragma unroll
            for (int k = 0; k < 4; ++k) {
              const float own = kh == 0 ? o[rd][4 * (i + j) + k] : o[3 + rd][4 * (i + j) + k];
              v[k] = own * ws_ + rv[i + j][k] * wo_;
            }
            pk[2 * j] = cvtpk(v[0], v[1]);
            pk[2 * j + 1] = cvtpk(v[2], v[3]);
          }
          auto r0 = __builtin_amdgcn_permlane32_swap(pk[0], pk[2], false, false);
          auto r1 = __builtin_amdgcn_permlane32_swap(pk[1], pk[3], false, false);
          u32x4 w = {r0[0], r1[0], r0[1], r1[1]};
          const unsigned rowoff = zc * 64 + (((i + zh) ^ osw) * 16);
          if (u == 0) {
            if (rd == 0) *(LAS u32x4*)(size_t)(L0 + SIDE + r * ITB + 8192 + kh * 2048 + rowoff) = w;
            else hold[rd - 1][i >> 1] = w;
          } else {
            *(LAS u32x4*)(size_t)(L0 + 61440 + (3 * kh + rd) * ITB + 6144 + r * 2048 + rowoff) = w;
            __builtin_amdgcn_raw_buffer_store_b128(w, rsO, obase + (3 * kh + rd) * 6144 + (((i + zh) ^ osw) * 16), 0, 16);
          }
        }
      }
      EST(3);
      if (u == 0) {
        lds_wait_ge(fpa + 4, 4);
#pragma unroll
        for (int rd = 1; rd < 3; ++rd)
#pragma unroll
          for (int i = 0; i < 2; ++i)
            *(LAS u32x4*)(size_t)(L0 + SIDE + r * ITB + (rd - 1) * 4096 + kh * 2048 + zc * 64 + (((2 * i + zh) ^ osw) * 16)) = hold[rd - 1][i];
        lds_post(L0 + C_OR + 4 * wave, 1);
      }
      EST(4);
      STAMP(3 + 4 * u);
    }
  }
  {
    unsigned ln; LANEID(ln);
    constexpr unsigned TWO = 10240, TOB = 61440;
    const int ch = wave & 1, rq = wave >> 1;
    const unsigned g = ln >> 4, q4 = (ln & 15) >> 2, p4 = ln & 3, l15 = ln & 15;
    unsigned gA, gB;
    {
      auto wpiece = [&](int p) {
        const int n = 64 * p + ln, row = n / 20, pc = n % 20;
        return (unsigned)(row * (DM * 2) + ((pc ^ (((row >> 3) & 1) << 1)) * 16));
      };
      gA = wpiece(wave);
      gB = wave < 2 ? wpiece(8 + wave) : ln * 16 + (wave - 2) * 1024;
    }
    const int pc3 = wave < 6 ? wave : wave - 2;
    const unsigned gC = ln * 16 + pc3 * 1024 + 256 * 36864;
    const __amdgpu_buffer_rsrc_t rsWo = __builtin_amdgcn_make_buffer_rsrc((void*)((const char*)wo + 4 * xj * DH * DM * 2 + hq * 320), (short)0, HD * DM * 2 - (4 * xj * DH * DM * 2 + hq * 320), 0x00020000);
    const __amdgpu_buffer_rsrc_t rsOt = __builtin_amdgcn_make_buffer_rsrc(ws + WS_O + (size_t)qbase * 36864, (short)0, (512 - qbase) * 36864, 0x00020000);
    const __amdgpu_buffer_rsrc_t rsB = wave < 2 ? rsWo : rsOt;
    const unsigned mA = L0 + wave * 1024, mC = L0 + TOB + 6144 + pc3 * 1024;
    const unsigned mB = wave < 2 ? L0 + (8 + wave) * 1024 : L0 + TOB + (wave - 2) * 1024;
    const unsigned stepB = wave < 2 ? 32 * DM * 2 : 6144;
    const unsigned slotB = wave < 2 ? TWO : ITB;
    unsigned wsrc, osrc, bsrc;
    auto sethead = [&](int hs) { wsrc = hs * (DH * DM * 2); osrc = hs * (8 * 36864); bsrc = wave < 2 ? wsrc : osrc; };
    auto request = [&](int c6, int k) {
      if (k == 0) bdma(mA + c6 * TWO, rsWo, gA, wsrc + c6 * (32 * DM * 2));
      if (k == 1) bdma1(mB + c6 * slotB, rsB, gB, bsrc + c6 * stepB);
      if (k == 2) bdma1(mC + c6 * ITB, rsOt, gC, osrc + c6 * 6144);
    };
    const unsigned WAb = L0 + (8 * g + q4) * 320 + 160 * ch + 8 * p4;
    const unsigned WA0 = WAb + ((ch & 1) ? -32 * (int)(g & 1) : 32 * (int)(g & 1));
    const unsigned WA1 = WAb + ((ch & 1) ? 32 * (int)(g & 1) : -32 * (int)(g & 1));
    const unsigned OA = L0 + TOB + (48 * rq + l15) * 64 + ((g ^ (((l15 >> 2) & 1) << 1)) * 16);
    const unsigned OA2 = OA + 3 * ITB;
    f32x4 acc[3][5] = {};
    bf16x8 af[2][5], bfr[2][3];
    auto rdA = [&](int par, int sl, int cb) {
      const unsigned a = ((cb & 1) ? WA1 : WA0) + sl * TWO + cb * 32;
      af[par][cb] = cat(ldstr(a), ldstr(a + 4 * 320));
    };
    auto rdB = [&](int par, int sl, int rb) { bfr[par][rb] = lds128((sl < 3 ? OA : OA2) + (sl % 3) * ITB + rb * 1024); };
    if (wave >= 6) WAITV(0);
    BAR();
    auto tbody = [&](auto VV) {
      constexpr int V = decltype(VV)::value;
#pragma unroll
      for (int rb = 0; rb < 3; ++rb) rdB(0, 0, rb);
#pragma unroll
      for (int cb = 0; cb < 5; ++cb) rdA(0, 0, cb);
      BAR();
      STAMP(12);
      auto phase = [&](auto QQ, auto MODE) {
        constexpr int q = decltype(QQ)::value, mode = decltype(MODE)::value, cur = q & 1;
        constexpr int R0 = V == 0 ? 0 : 5, D0 = V == 0 ? 9 : 0;
#pragma unroll
        for (int i = 0; i < 15; ++i) {
          acc[i % 3][i / 3] = MFMA16(af[cur][i / 3], bfr[cur][i % 3], acc[i % 3][i / 3]);
          SBAR();
          if (i >= R0 && i < R0 + 3) rdB(cur ^ 1, (q + 1) % 6, i - R0);
          if (i >= R0 + 3 && i < R0 + 8) rdA(cur ^ 1, (q + 1) % 6, i - R0 - 3);
          if (mode == 0 && i >= D0 && i < D0 + 6 && !((i - D0) & 1)) request(q, (i - D0) >> 1);
          if (mode == 2 && q >= 3 && i >= D0 && i < D0 + 6) request(2 * (q - 3) + (i - D0) / 3, (i - D0) % 3);
          SBAR();
        }
        if (mode == 0) WAITV(12);
        if (mode == 1) { if (q == 0) WAITV(9); else if (q == 1) WAITV(6); else if (q == 2) WAITV(3); else WAITV(0); }
        if (mode == 2) {
          if (q == 0) WAITV(0);
          if (q == 2 && wave == 7) poll_flags(FL + (size_t)blockIdx.x * 32, 4, hq, TAG, ln);
          if (q == 4) WAITV(9);
          if (q == 5) WAITV(12);
        }
        BAR();
        if (mode == 2 && q == 0 && wave == 0 && ln < 4 && (int)ln != hq)
          __hip_atomic_store(FL + (size_t)(qbase + 8 * ln) * 32 + hq, TAG, __ATOMIC_RELAXED, __HIP_MEMORY_SCOPE_AGENT);
      };
      auto head6 = [&](auto MODE) {
        phase(std::integral_constant<int, 0>{}, MODE);
        phase(std::integral_constant<int, 1>{}, MODE);
        phase(std::integral_constant<int, 2>{}, MODE);
        phase(std::integral_constant<int, 3>{}, MODE);
        phase(std::integral_constant<int, 4>{}, MODE);
        phase(std::integral_constant<int, 5>{}, MODE);
      };
      sethead((hq + 1) & 3);
      head6(std::integral_constant<int, 2>{});
#pragma unroll 1
      for (int i = 2; i < 4; ++i) {
        sethead((hq + i) & 3);
        head6(std::integral_constant<int, 0>{});
      }
      STAMP(13);
      head6(std::integral_constant<int, 1>{});
    };
    if (wave < 4) tbody(std::integral_constant<int, 0>{}); else tbody(std::integral_constant<int, 1>{});
    STAMP(8);
    {
      const unsigned yw = L0 + (48 * rq + l15) * 336 + (80 * ch + 4 * g) * 2;
#pragma unroll
      for (int rb = 0; rb < 3; ++rb)
#pragma unroll
        for (int cb = 0; cb < 5; ++cb) {
          u32x2 w = {cvtpk(acc[rb][cb][0], acc[rb][cb][1]), cvtpk(acc[rb][cb][2], acc[rb][cb][3])};
          *(LAS u32x2*)(size_t)(yw + rb * 16 * 336 + cb * 32) = w;
        }
    }
    BAR();
    {
      const __amdgpu_buffer_rsrc_t rsP = __builtin_amdgcn_make_buffer_rsrc(ws + WS_P, (short)0, 2 * 8 * 4 * 8 * 30720, 0x00020000);
#pragma unroll
      for (int i = 0; i < 8; ++i) {
        const int j = wave + 8 * i;
        if (j < 60) {
          const int n = 64 * j + ln, row = n / 20, pc = n % 20, un = j >= 30 ? 1 : 0;
          const u32x4 v = *(const LAS u32x4*)(size_t)(L0 + row * 336 + pc * 16);
          __builtin_amdgcn_raw_buffer_store_b128(v, rsP, (((un * 8 + b0) * 4 + hq) * 8 + xj) * 30720 + (n - 1920 * un) * 16, 0, 16);
        }
      }
      WAITV(0);
      BAR();
      if (wave == 0) {
        if (ln < 8) __hip_atomic_store(FL + (size_t)((int)ln + 8 * xi) * 32 + 8 + xj, TAG, __ATOMIC_RELAXED, __HIP_MEMORY_SCOPE_AGENT);
        poll_flags(FL + (size_t)blockIdx.x * 32 + 8, 8, -1, TAG, ln);
      }
      BAR();
      STAMP(9);
      const int tz = wave * 64 + (int)ln;
      if (tz < 480) {
        const int u = tz / 240, e = tz % 240;
        const int pp = (((u * 8 + b0) * 4 + hq) * 8) * 30720 + 12 * xj * 320 + e * 16;
        u32x4 v[8];
#pragma unroll
        for (int j = 0; j < 8; ++j) v[j] = __builtin_amdgcn_raw_buffer_load_b128(rsP, pp + j * 30720, 0, 16);
        float a[8];
#pragma unroll
        for (int k = 0; k < 4; ++k) {
          float lo = 0.f, hi2 = 0.f;
#pragma unroll
          for (int j = 0; j < 8; ++j) { lo += __uint_as_float(v[j][k] << 16); hi2 += __uint_as_float(v[j][k] & 0xffff0000u); }
          a[2 * k] = lo; a[2 * k + 1] = hi2;
        }
        u32x4 w = {cvtpk(a[0], a[1]), cvtpk(a[2], a[3]), cvtpk(a[4], a[5]), cvtpk(a[6], a[7])};
        const int row = e / 20, c8 = e % 20;
        *(u32x4*)(out + (size_t)((b0 + 8 * u) * SQ + 12 * xj + row) * DM + hq * 160 + c8 * 8) = w;
      }
    }
    STAMP(10);
  }
}

extern "C" void kernel_launch(void* const* d_in, const int* in_sizes, int n_in, void* d_out, int out_size, void* d_ws,
                              size_t ws_size, hipStream_t stream) {
  const u16* x = (const u16*)d_in[0];
  const u16* kc = (const u16*)d_in[1];
  const u16* vc = (const u16*)d_in[2];
  const u16* wq = (const u16*)d_in[3];
  const u16* wo = (const u16*)d_in[4];
  (void)hipFuncSetAttribute((const void*)attn_kernel, hipFuncAttributeMaxDynamicSharedMemorySize, LDS_A);
  attn_kernel<<<256, 512, LDS_A, stream>>>(x, kc, vc, wq, wo, (u16*)d_out, (char*)d_ws, (unsigned long long*)((char*)d_ws + (128u << 20)));
}
```

```cpp
#include <hip/hip_runtime.h>
#include <hip/hip_bf16.h>
#include <stdint.h>
#include <type_traits>

typedef unsigned short u16;
typedef short bf16x8 __attribute__((ext_vector_type(8)));
typedef short s16x4 __attribute__((ext_vector_type(4)));
typedef float f32x16 __attribute__((ext_vector_type(16)));
typedef float f32x4 __attribute__((ext_vector_type(4)));
typedef float f32x2 __attribute__((ext_vector_type(2)));
typedef __bf16 bf16x2 __attribute__((ext_vector_type(2)));
typedef unsigned u32x4 __attribute__((ext_vector_type(4)));
typedef unsigned u32x2 __attribute__((ext_vector_type(2)));
#define LAS __attribute__((address_space(3)))
#define DI __device__ __forceinline__

constexpr int NB = 16, SQ = 96, DM = 640, NH = 32, SK = 1024, DH = 192;
constexpr int HD = NH * DH;

#define MFMA32(a, b, c) __builtin_amdgcn_mfma_f32_32x32x16_bf16((a), (b), (c), 0, 0, 0)
#define MFMA16(a, b, c) __builtin_amdgcn_mfma_f32_16x16x32_bf16((a), (b), (c), 0, 0, 0)
#define BAR() do { asm volatile("" ::: "memory"); __builtin_amdgcn_s_waitcnt(0xC07F); __builtin_amdgcn_s_barrier(); asm volatile("" ::: "memory"); } while (0)
#define WAITV(n) asm volatile("s_waitcnt vmcnt(%0)" ::"i"(n) : "memory")
#define SBAR() __builtin_amdgcn_sched_barrier(0)
#define MAX3(a, b, c) fmaxf(fmaxf((a), (b)), (c))
#define LANEID(v) asm volatile("v_mbcnt_lo_u32_b32 %0, -1, 0\n\tv_mbcnt_hi_u32_b32 %0, -1, %0" : "=v"(v))

DI unsigned cvtpk(float lo, float hi) {
  f32x2 v = {lo, hi};
  bf16x2 b = __builtin_convertvector(v, bf16x2);
  return __builtin_bit_cast(unsigned, b);
}
DI void dma16nt(unsigned lds_addr, const void* sbase, unsigned voff) {
  asm volatile("s_mov_b32 m0, %0\n\ts_nop 0\n\tglobal_load_lds_dwordx4 %1, %2 nt" ::"s"(lds_addr), "v"(voff), "s"(sbase) : "memory");
}
DI void bdma(unsigned lds_addr, __amdgpu_buffer_rsrc_t rs, unsigned voff, unsigned soff) {
  asm volatile("s_mov_b32 m0, %0\n\ts_nop 0\n\tbuffer_load_dwordx4 %1, %2, %3 offen lds" ::"s"(lds_addr), "v"(voff), "s"(rs), "s"(soff) : "memory");
}
DI void bdma1(unsigned lds_addr, __amdgpu_buffer_rsrc_t rs, unsigned voff, unsigned soff) {
  asm volatile("s_mov_b32 m0, %0\n\ts_nop 0\n\tbuffer_load_dwordx4 %1, %2, %3 offen sc1 lds" ::"s"(lds_addr), "v"(voff), "s"(rs), "s"(soff) : "memory");
}
DI void dma16(unsigned lds_addr, const void* sbase, unsigned voff) {
  asm volatile("s_mov_b32 m0, %0\n\ts_nop 0\n\tglobal_load_lds_dwordx4 %1, %2" ::"s"(lds_addr), "v"(voff), "s"(sbase) : "memory");
}
template <int I, int N, class F>
DI void sfor(F&& f) {
  if constexpr (I < N) { f(std::integral_constant<int, I>{}); sfor<I + 1, N>(f); }
}
DI bf16x8 lds128(unsigned a) { return *(const LAS bf16x8*)(size_t)a; }
DI s16x4 ldstr(unsigned a) { return __builtin_amdgcn_ds_read_tr16_b64_v4i16((LAS s16x4*)(size_t)a); }
DI bf16x8 cat(s16x4 a, s16x4 b) { return __builtin_shufflevector(a, b, 0, 1, 2, 3, 4, 5, 6, 7); }

constexpr unsigned ITB = 12288;
constexpr unsigned NSL = 5;
constexpr unsigned GRB = NSL * ITB;
constexpr unsigned SIDE = 2 * GRB;
constexpr unsigned MISC = SIDE + 36864;
constexpr unsigned CTL = MISC + 2048;
constexpr unsigned HB = MISC - 49152;
constexpr unsigned C_L = CTL;
constexpr unsigned C_R = CTL + 16;
constexpr unsigned C_QRDY = CTL + 48;
constexpr unsigned C_XF = CTL + 64;
constexpr unsigned C_H = CTL + 128;
constexpr unsigned C_OR = CTL + 160;
constexpr unsigned C_OD = CTL + 192;
constexpr unsigned LDS_A = 163840;
constexpr float CSC = 0.07216878364870322f * 1.4426950408889634f;
constexpr float THR_RAW = 8.0f / CSC;
constexpr int SPIN_MAX = 1 << 20;
#define HD_D 4
#define TL_D 4
#define KV0 2

#define STAMP(i)
#define EST(i)
#define FST(i)

DI void lds_post(unsigned a, unsigned v) {
  asm volatile("s_waitcnt lgkmcnt(0)\n\tds_write_b32 %0, %1" ::"v"(a), "v"(v) : "memory");
}
DI void lds_post_nw(unsigned a, unsigned v) {
  asm volatile("ds_write_b32 %0, %1" ::"v"(a), "v"(v) : "memory");
}
DI void lds_wait_ge(unsigned a, unsigned n) {
#pragma nounroll
  for (int it = 0; it < SPIN_MAX; ++it) {
    unsigned v;
    asm volatile("ds_read_b32 %0, %1\n\ts_waitcnt lgkmcnt(0)" : "=v"(v) : "v"(a) : "memory");
    if (__builtin_amdgcn_readfirstlane(v) >= n) break;
    __builtin_amdgcn_s_sleep(1);
  }
}
DI void lds_wait_min4(unsigned a, unsigned n) {
#pragma nounroll
  for (int it = 0; it < SPIN_MAX; ++it) {
    u32x4 d;
    asm volatile("ds_read_b128 %0, %1\n\ts_waitcnt lgkmcnt(0)" : "=v"(d) : "v"(a) : "memory");
    const unsigned mn = min(min(d[0], d[1]), min(d[2], d[3]));
    if (__builtin_amdgcn_readfirstlane(mn) >= n) break;
    __builtin_amdgcn_s_sleep(1);
  }
}
DI unsigned lds_rd1(unsigned a) {
  unsigned v;
  asm volatile("ds_read_b32 %0, %1\n\ts_waitcnt lgkmcnt(0)" : "=v"(v) : "v"(a) : "memory");
  return __builtin_amdgcn_readfirstlane(v);
}
DI unsigned lds_min3(unsigned a) {
  u32x4 d;
  asm volatile("ds_read_b128 %0, %1\n\ts_waitcnt lgkmcnt(0)" : "=v"(d) : "v"(a) : "memory");
  return __builtin_amdgcn_readfirstlane(min(min(d[0], d[1]), d[2]));
}
DI unsigned getvm() {
  unsigned sts;
  asm volatile("s_getreg_b32 %0, hwreg(HW_REG_IB_STS)" : "=s"(sts)::"memory");
  return (sts & 15u) | ((sts >> 18) & 0x30u);
}
DI unsigned lds_peek(unsigned a) { return *(volatile LAS unsigned*)(size_t)a; }

typedef __attribute__((address_space(1))) unsigned long long gu64;
extern "C" __device__ unsigned long long amd_dispatch_id() __asm("llvm.amdgcn.dispatch.id");
constexpr size_t WS_O = 0, WS_P = 32u << 20, WS_F = 96u << 20;

DI bool poll_flags(gu64* f, int nl, int skip, unsigned long long T, int lane) {
#pragma nounroll
  for (int it = 0; it < SPIN_MAX; ++it) {
    unsigned long long v = T;
    if (lane < nl && lane != skip) v = __hip_atomic_load(f + lane, __ATOMIC_RELAXED, __HIP_MEMORY_SCOPE_AGENT);
    if (__all(v == T)) {
      if (lane < nl && lane != skip) __hip_atomic_store(f + lane, 0ull, __ATOMIC_RELAXED, __HIP_MEMORY_SCOPE_AGENT);
      return true;
    }
    __builtin_amdgcn_s_sleep(2);
  }
  return false;
}

constexpr int hNL(int j) { return j < 20 ? 3 : 0; }
constexpr int hKV(int p) { return (p >= KV0 && p < KV0 + 12) ? 1 : 0; }
constexpr int hPend(int j) {
  int cnt = 0, first = 0;
  if (j < HD_D) { cnt = 3 * (HD_D - 1 - j); first = 0; }
  else { cnt = hKV(j - HD_D); first = j - HD_D + 1; }
  for (int p = first; p < j; ++p) cnt += hNL(p + HD_D) + hKV(p);
  return cnt;
}
constexpr int tNL(int j) { return j < 24 ? 3 : 0; }
constexpr int tPend(int j) {
  int cnt = 0, first = 0;
  if (j < TL_D) { cnt = 3 * (TL_D - 1 - j); first = 0; }
  else { cnt = 0; first = j - TL_D + 1; }
  for (int p = first; p < j; ++p) cnt += tNL(p + TL_D);
  return cnt;
}

__global__ __launch_bounds__(512, 2) void attn_kernel(const u16* x, const u16* kc, const u16* vc, const u16* wq, const u16* wo,
                                                      u16* out, char* ws, unsigned long long* stamps) {
  extern __shared__ __attribute__((aligned(16))) char lds[];
  const unsigned L0 = (unsigned)(size_t)(LAS char*)lds;
  const int tid = threadIdx.x, lane = tid & 63;
  const int wave = __builtin_amdgcn_readfirstlane(tid >> 6);
  const int xj = blockIdx.x & 7, xi = blockIdx.x >> 3;
  const int hq = xi & 3, h = 4 * xj + hq, b0 = xi >> 2;
  const size_t UB0 = (size_t)(b0 * NH + h) * SK * 384, UB1 = (size_t)((b0 + 8) * NH + h) * SK * 384;
  unsigned long long TAG;
  {
    const unsigned long long did = amd_dispatch_id(), dp = (unsigned long long)__builtin_amdgcn_dispatch_ptr();
    const unsigned t32 = (unsigned)(did * 0x9E3779B1ull) ^ (unsigned)(dp >> 6) * 0x85EBCA77u ^ (unsigned)(did >> 32);
    TAG = ((unsigned long long)(t32 ^ 0x5A17C3E9u) << 32) | t32;
  }
  const int qbase = xj + 32 * b0;
  gu64* FL = (gu64*)(ws + WS_F);
  const int kh = (0x1A >> wave) & 1, r = (0x102210 >> (4 * wave)) & 3;
  STAMP(0);
  bf16x8 qf[12];

  unsigned offK[6], offV[3];
  {
    if (wave == 6 && lane < 64) *(LAS unsigned*)(size_t)(L0 + CTL + lane * 4) = (lane == 46 || lane == 47) ? 1u : 0u;
    const int rot = (b0 * 5 / 4 + hq * 5 / 2) % 10;
    const int dh = wave & 1, rq = wave >> 1;
    const unsigned g = lane >> 4, q4 = (lane & 15) >> 2, p4 = lane & 3, l15 = lane & 15;
    const __amdgpu_buffer_rsrc_t rsW = __builtin_amdgcn_make_buffer_rsrc((void*)((const char*)wq + h * DH * 2), (short)0, DM * HD * 2 - h * DH * 2, 0x00020000);
    const __amdgpu_buffer_rsrc_t rsX = __builtin_amdgcn_make_buffer_rsrc((void*)((const char*)x + b0 * SQ * DM * 2), (short)0, (NB - b0) * SQ * DM * 2, 0x00020000);
    unsigned gA, gB, gC;
    {
      auto wpiece = [&](int p) {
        const int n = 64 * p + lane, row = n / 24, pc = n % 24;
        const int sw = ((row >> 1) & 1) | (((row >> 3) & 1) << 1);
        return (unsigned)(row * (HD * 2) + ((pc ^ (sw << 1)) * 16));
      };
      auto xpiece = [&](int p) {
        const int row = 8 * p + (lane >> 3);
        return (unsigned)(row * (DM * 2) + (((lane & 7) ^ ((row >> 1) & 7)) * 16));
      };
      gA = wpiece(wave);
      gB = wave < 4 ? wpiece(8 + wave) : xpiece(wave - 4);
      gC = xpiece(4 + wave);
    }
    unsigned hK[3], hV[3];
    constexpr unsigned WB = 3 * ITB, G1X = GRB + 2 * ITB, U1 = 8 * SQ * DM * 2;
    constexpr unsigned WSTEP = 32 * HD * 2;
    const unsigned mW = L0 + WB + wave * 1024;
    const unsigned mX = L0 + (4 + wave) * 1024;
    const unsigned s2 = ((q4 >> 1) & 1) | ((g & 1) << 1);
    unsigned E[4];
    const unsigned XA0 = L0 + (rq >> 1) * G1X + (48 * (rq & 1) + l15) * 128 + ((g ^ ((l15 >> 1) & 7)) * 16), XA1 = XA0 ^ 64;
    f32x4 acc[6][3] = {};
    bf16x8 af[2][6], bfr[2][3];
    unsigned xa;
    auto rdA = [&](int par, int c1, int db) {
      const unsigned a = E[db & 3] + (db >> 2) * 128 + (c1 & 3) * ITB;
      af[par][db] = cat(ldstr(a), ldstr(a + 4 * 384));
    };
    auto rdB = [&](int par, int rb) { bfr[par][rb] = lds128(xa + rb * 2048); };
    unsigned woff = (unsigned)(2 * rot) * WSTEP, xoff = (unsigned)rot * 128;
    auto advW = [&]() { woff += WSTEP; woff = woff == 20 * WSTEP ? 0u : woff; };
    auto advX = [&]() { xoff += 128; xoff = xoff == 1280 ? 0u : xoff; };
    auto request = [&](auto VV, int k, int wsl, unsigned xs, int un) {
      constexpr int V = decltype(VV)::value;
      if (k == 0) bdma(mW + wsl * ITB, rsW, gA, woff);
      if (k == 1) {
        if (V == 0) bdma(mW + 8192 + wsl * ITB, rsW, gB, woff);
        else bdma(mX - 8192 + un * G1X + xs, rsX, gB, xoff + un * U1);
      }
      if (k == 2) bdma(mX + un * G1X + xs, rsX, gC, xoff + un * U1);
    };
    unsigned xr = 2 * ITB;
    unsigned xw = 1 * ITB;
    auto body = [&](auto VV) {
      constexpr int V = decltype(VV)::value;
      if (V == 1) bdma(mX - 8192 + 2 * ITB, rsX, gB, xoff);
      bdma(mX + 2 * ITB, rsX, gC, xoff);
#pragma unroll
      for (int c = -4; c < 0; ++c) {
        if ((c & 1) != 0) advX();
        const unsigned xs = (unsigned)((((c + 1) >> 1) + 2 + 2) % 3) * ITB;
#pragma unroll
        for (int k = 0; k < 3; ++k) request(VV, k, c & 3, xs, (c + 1) & 1);
        advW();
      }
      SBAR();
      asm volatile("" ::: "memory");
      if (wave >= 6) {
        const char* wsrc = (const char*)wo + (size_t)(4 * xj * DH + 96 * b0 + 48 * (wave - 6)) * (DM * 2) + hq * 320;
        const unsigned o0 = (lane >> 2) * (DM * 2) + min((unsigned)(lane & 3) * 128u, 316u);
#pragma unroll
        for (int z = 0; z < 3; ++z)
          asm volatile("s_mov_b32 m0, %0\n\ts_nop 0\n\tglobal_load_lds_dword %1, %2" ::"s"(L0 + MISC + 1536), "v"(o0 + z * 16 * DM * 2), "s"(wsrc) : "memory");
        const char* t0 = lane == 0 ? ws + WS_O + (size_t)blockIdx.x * 36864 : lane == 1 ? ws + WS_O + (size_t)(256 + blockIdx.x) * 36864
                       : lane == 2 ? ws + WS_P + (size_t)(((b0 * 4 + hq) * 8 + xj) * 30720) : lane == 3 ? ws + WS_P + (size_t)((((8 + b0) * 4 + hq) * 8 + xj) * 30720)
                       : lane == 4 ? ws + WS_F + (size_t)blockIdx.x * 256 : (const char*)out + (size_t)((b0 + 8 * (lane & 1)) * SQ + 12 * xj) * DM * 2;
        if (wave == 7 && lane < 7)
          asm volatile("s_mov_b32 m0, %0\n\ts_nop 0\n\tglobal_load_lds_dword %1, off" ::"s"(L0 + MISC + 1536), "v"(t0) : "memory");
      }
#pragma unroll
    for (int j = 0; j < 6; ++j) {
      int n = 64 * j + lane, row = n / 24, pc = n % 24;
      offK[j] = row * 384 + ((pc ^ ((row >> 1) & 7)) * 16);
    }
#pragma unroll
    for (int j = 0; j < 3; ++j) {
      int n = 64 * j + lane, row = n / 24, pc = n % 24;
      offV[j] = row * 384 + ((pc ^ (((row >> 1) & 1) << 2)) * 16);
    }
#pragma unroll
    for (int j = 0; j < 3; ++j) {
      const int n = 64 * (3 * (wave & 3) + j) + lane, row = n / 24, pc = n % 24;
      hK[j] = row * 384 + ((pc ^ ((row >> 1) & 7)) * 16);
      hV[j] = row * 384 + ((pc ^ (((row >> 1) & 1) << 2)) * 16);
    }
#pragma unroll
      for (int j = 0; j < 4; ++j) E[j] = L0 + WB + (8 * g + q4) * 384 + 32 * ((unsigned)(6 * dh + j) ^ s2) + 8 * p4;
      WAITV(9);
      BAR();
      xa = XA0 + 2 * ITB;
#pragma unroll
      for (int rb = 0; rb < 3; ++rb) rdB(0, rb);
#pragma unroll
      for (int db = 0; db < 6; ++db) rdA(0, 0, db);
      WAITV(6);
      BAR();
      STAMP(4);
      auto phase = [&](int c, auto QQ, auto MODE) {
        constexpr int q = decltype(QQ)::value, mode = decltype(MODE)::value, cur = q & 1;
        if (cur == 1) {
          xr = xr == 2 * ITB ? 0 : xr + ITB;
          if (mode == 0) { xw = xw == 2 * ITB ? 0 : xw + ITB; advX(); }
        }
        xa = (cur == 1 ? XA0 : XA1) + xr;
        constexpr int R0 = V == 0 ? 0 : 5, D0 = V == 0 ? 10 : 0;
#pragma unroll
        for (int i = 0; i < 18; ++i) {
          acc[i / 3][i % 3] = MFMA16(af[cur][i / 3], bfr[cur][i % 3], acc[i / 3][i % 3]);
          SBAR();
          if (i >= R0 && i < R0 + 3) rdB(cur ^ 1, i - R0);
          if (i >= R0 + 3 && i < R0 + 9) rdA(cur ^ 1, q + 1, i - R0 - 3);
          if (mode == 0 && i >= D0 && i < D0 + 6 && !((i - D0) & 1)) request(VV, (i - D0) >> 1, q, xw, (q + 1) & 1);
          if (mode == 0 && i == D0 + 5) advW();
          if (mode == 2 && i >= D0 && i < D0 + 6 && !((i - D0) & 1)) {
            const int k = (i - D0) >> 1, li = c - 17;
            const unsigned dst = L0 + V * G1X + li * ITB + (3 * (wave & 3) + k) * 1024;
            if (li == 2) dma16nt(dst, (const char*)vc + UB0 + (size_t)(32 * V) * 384, hV[k]);
            else dma16nt(dst, (const char*)kc + UB0 + (size_t)(64 * li + 32 * V) * 384, hK[k]);
          }
          SBAR();
        }
        if (mode == 0) WAITV(6);
        if (mode == 1) WAITV(0);
        BAR();
      };
      using I0 = std::integral_constant<int, 0>; using I1 = std::integral_constant<int, 1>;
      using I2 = std::integral_constant<int, 2>; using I3 = std::integral_constant<int, 3>;
#pragma unroll 1
      for (int c = 0; c < 16; c += 4) {
        phase(c, I0{}, I0{});
        phase(c + 1, I1{}, I0{});
        phase(c + 2, I2{}, I0{});
        phase(c + 3, I3{}, I0{});
      }
      STAMP(5);
      phase(16, I0{}, I1{});
      phase(17, I1{}, I2{});
      phase(18, I2{}, I2{});
      phase(19, I3{}, I2{});
    };
    if (wave < 4) body(std::integral_constant<int, 0>{}); else body(std::integral_constant<int, 1>{});
    STAMP(11);
    auto qstore = [&]() {
      unsigned lz; LANEID(lz);
      const unsigned zg = lz >> 4, zl = lz & 15;
#pragma unroll
      for (int rb = 0; rb < 3; ++rb) {
        const int R16 = 3 * (rq & 1) + rb;
        const unsigned qb = L0 + SIDE + ((R16 >> 1) * 12 + 6 * dh) * 1024 + (16 * (R16 & 1) + zl + 32 * (zg >> 1)) * 16 + 8 * (zg & 1);
#pragma unroll
        for (int db = 0; db < 6; ++db) {
          u32x2 w = {cvtpk(acc[db][rb][0], acc[db][rb][1]), cvtpk(acc[db][rb][2], acc[db][rb][3])};
          *(LAS u32x2*)(size_t)(qb + db * 1024) = w;
        }
      }
    };
    if (wave < 4) qstore();
    BAR();
    if (wave < 6) {
      unsigned lz; LANEID(lz);
#pragma unroll
      for (int ks = 0; ks < 12; ++ks) qf[ks] = lds128(L0 + SIDE + ((r * 12 + ks) * 64 + lz) * 16);
    }
    BAR();
    if (wave >= 4) { qstore(); lds_post(L0 + C_QRDY + 4 * (wave - 4), 1); }
  }
  STAMP(1);

  if (wave >= 6) {
    const int hw = wave - 6;
    __builtin_amdgcn_s_setprio(3);
    unsigned ll; LANEID(ll);
    const unsigned aL = L0 + C_L + 4 * hw, aR = L0 + C_R + 16 * hw;
    auto issue_item = [&](unsigned li_, unsigned slot_) {
      const unsigned j = li_ & 31;
      const bool isK = (j == 0) || ((j & 1) && j < 31);
      const unsigned tile = isK ? (j + 1) >> 1 : (j - 1) >> 1;
      const size_t ub = ((li_ >> 5) ? UB1 : UB0) + (size_t)(64 * tile + 32 * hw) * 384;
      const unsigned dst = L0 + hw * GRB + slot_;
      if (isK) {
        const char* src = (const char*)kc + ub;
#pragma unroll
        for (int q = 0; q < 12; ++q) dma16nt(dst + q * 1024, src + (q / 6) * 6144, offK[q % 6]);
      } else {
        const char* src = (const char*)vc + ub;
#pragma unroll
        for (int q = 0; q < 12; ++q) dma16nt(dst + q * 1024, src + (q / 3) * 3072, offV[q % 3]);
      }
    };
    issue_item(3, hw ? 0 : 3 * ITB);
    issue_item(4, hw ? ITB : 4 * ITB);
    WAITV(30); lds_post_nw(L0 + C_H + 4 * wave, 1); lds_wait_min4(L0 + C_H + 16 * hw, 1); lds_post_nw(aL, 1);
    WAITV(27); lds_post_nw(L0 + C_H + 4 * wave, 2); lds_wait_min4(L0 + C_H + 16 * hw, 2); lds_post_nw(aL, 2);
    WAITV(24); lds_post_nw(L0 + C_H + 4 * wave, 3); lds_wait_min4(L0 + C_H + 16 * hw, 3); lds_post_nw(aL, 3);
    unsigned li = 5, lpub = 3, slot = hw ? 2 * ITB : 0;
    unsigned sb = 0, ost = 0;
    unsigned tix = 0, xt = 0;
    bool odone = false;
    const __amdgpu_buffer_rsrc_t rsWo = __builtin_amdgcn_make_buffer_rsrc((void*)((const char*)wo + 4 * xj * DH * DM * 2 + hq * 320), (short)0, HD * DM * 2 - (4 * xj * DH * DM * 2 + hq * 320), 0x00020000);
    unsigned go[5];
#pragma unroll
    for (int p = 0; p < 5; ++p) {
      const int n = 64 * p + ll, row = n / 20, pc = n % 20;
      go[p] = row * (DM * 2) + ((pc ^ (((row >> 3) & 1) << 1)) * 16);
    }
    const __amdgpu_buffer_rsrc_t rsO = __builtin_amdgcn_make_buffer_rsrc(ws + WS_O, (short)0, 2 * 256 * 36864, 0x00020000);
    auto obatch = [&]() {
#pragma unroll
      for (int k = 0; k < 3; ++k) {
        const unsigned p = 18 * hw + 3 * ost + k, rr = p / 12, q = p % 12;
        const unsigned c6 = q >> 1;
        const u32x4 v = *(const LAS u32x4*)(size_t)(L0 + SIDE + p * 1024 + ll * 16);
        __builtin_amdgcn_raw_buffer_store_b128(v, rsO, (int)(blockIdx.x * 36864 + c6 * 6144 + rr * 2048 + (q & 1) * 1024 + ll * 16), 0, 16);
      }
      ++ost;
      if (ost == 6) lds_post(L0 + C_OD + 4 * hw, 1);
    };
    auto ocheck = [&]() {
      u32x4 d0, d1;
      asm volatile("ds_read_b128 %0, %2\n\tds_read_b128 %1, %2 offset:16\n\ts_waitcnt lgkmcnt(0)" : "=&v"(d0), "=&v"(d1) : "v"(L0 + C_OR) : "memory");
      const unsigned mn = min(min(min(d0[0], d0[1]), min(d0[2], d0[3])), min(min(d1[0], d1[1]), min(d1[2], d1[3])));
      return __builtin_amdgcn_readfirstlane(mn) >= 1;
    };
#pragma nounroll
    for (int it = 0; it < (SPIN_MAX << 2); ++it) {
      bool idle = true;
      if (li < 64) {
        u32x4 d;
        asm volatile("ds_read_b128 %0, %1\n\ts_waitcnt lgkmcnt(0)" : "=v"(d) : "v"(aR) : "memory");
        const unsigned mn = __builtin_amdgcn_readfirstlane(min(min(d[0], d[1]), d[2]));
        if (mn + NSL > li && getvm() <= 48) {
          issue_item(li, slot);
          ++li;
          slot = slot + ITB == GRB ? 0 : slot + ITB;
          idle = false;
        }
      } else if (tix < 5 && ost == 6 && odone) {
        if (lds_min3(aR) >= 60 + tix && getvm() <= 42 && (hw == 0 || lds_rd1(L0 + C_OD) >= 2)) {
          if (hw == 0) {
            const unsigned c0 = tix == 0 ? 5 : tix - 1, nc = tix == 4 ? 2 : 1;
#pragma unroll 1
            for (unsigned c6 = c0; c6 < c0 + nc; ++c6) {
#pragma unroll
              for (int p = 0; p < 10; ++p) bdma(L0 + c6 * 10240 + p * 1024, rsWo, go[p % 5], (hq * DH + 32 * c6 + 16 * (p / 5)) * (DM * 2));
              xt += 10;
            }
          } else {
            const unsigned sl = tix == 4 ? 0 : tix + 1;
#pragma unroll
            for (int p = 0; p < 6; ++p) bdma1(L0 + GRB + sl * ITB + p * 1024, rsO, ll * 16, blockIdx.x * 36864 + tix * 6144 + p * 1024);
            xt += 6;
          }
          ++tix;
          idle = false;
        }
      }
      if (li > 32 && ost == 0 && getvm() <= 45 && ocheck()) {
#pragma unroll 1
        for (int z = 0; z < 6; ++z) obatch();
        sb = li;
        idle = false;
      }
      if (lpub < li) {
        const unsigned vm = getvm();
        unsigned done = lpub;
#pragma nounroll
        while (done < li) {
          const unsigned after = 12 * (li - 1 - done) + (done < sb ? 18 : 0) + xt;
          if (vm > after) break;
          ++done;
        }
        if (done > lpub) {
          lpub = done;
          lds_post_nw(aL, lpub);
          idle = false;
        }
      } else if (li == 64 && tix == 5) {
        break;
      }
      if (!odone && ost == 6 && (lpub > sb || getvm() == 0)) { odone = true; lds_post_nw(L0 + C_OD + 4 * hw, 2); }
      if (idle) __builtin_amdgcn_s_sleep(1);
    }
    __builtin_amdgcn_s_setprio(0);
    if (hw == 1) {
      lds_wait_ge(L0 + C_XF + 8 * 4 + 4, 6);
#pragma unroll
      for (int p = 0; p < 6; ++p) bdma1(L0 + GRB + 5 * ITB + p * 1024, rsO, ll * 16, blockIdx.x * 36864 + 5 * 6144 + p * 1024);
    }
  } else {
    unsigned ln; LANEID(ln);
    unsigned kof[4], vE, vO;
    {
      const unsigned a32 = ln & 31, ahi = ln >> 5, ablk = (ln >> 4) & 1, aq4 = (ln & 15) >> 2, ap4 = ln & 3, axq = (aq4 >> 1) & 1;
#pragma unroll
      for (int i = 0; i < 4; ++i) kof[i] = L0 + kh * GRB + a32 * 384 + (((2 * i + ahi) ^ ((a32 >> 1) & 7)) * 16);
      vE = L0 + kh * GRB + (4 * ahi + aq4) * 384 + 64 * axq + 32 * ablk + 8 * ap4;
      vO = L0 + kh * GRB + (4 * ahi + aq4) * 384 - 64 * axq + 32 * ablk + 8 * ap4;
    }
    const unsigned aF = L0 + C_L + 4 * kh, aR = L0 + C_R + 16 * kh + 4 * r;
    const int pw = (0x102354 >> (4 * wave)) & 7;
    const unsigned fme = L0 + C_XF + 8 * wave, fpa = L0 + C_XF + 8 * pw;
    unsigned cur = kh ? 2 * ITB : 0;
    unsigned cnt = 0;
#define ADV() do { cur = (cur + ITB == GRB) ? 0u : cur + ITB; ++cnt; } while (0)
#define KADDR(ks) (((ks) & 3) == 0 ? k0 : ((ks) & 3) == 1 ? k1 : ((ks) & 3) == 2 ? k2 : k3) + 128 * ((ks) >> 2)
#define VADDR(j) ((((j) % 6) & 1) ? vbO : vbE) + (16 * ((j) / 6)) * 384 + 64 * ((j) % 6)

#pragma unroll 1
    for (int u = 0; u < 2; ++u) {
      float m, l = 0.f;
      f32x16 o[6] = {};
      f32x16 sB;
      float pe[16];
      bf16x8 kf[12], vf[12];
      unsigned k0, k1, k2, k3, vbE, vbO;
      if (u == 0) { WAITV(6); lds_post_nw(L0 + C_H + 4 * wave, 1); }
      lds_wait_ge(aF, cnt + 1);
      k0 = kof[0] + cur; k1 = kof[1] + cur; k2 = kof[2] + cur; k3 = kof[3] + cur;
#pragma unroll
      for (int ks = 0; ks < 12; ++ks) kf[ks] = lds128(KADDR(ks));
      ADV();
      lds_post_nw(aR, cnt);
      SBAR();
      {
        f32x16 z = {};
        sB = MFMA32(kf[0], qf[0], z);
#pragma unroll
        for (int ks = 1; ks < 12; ++ks) sB = MFMA32(kf[ks], qf[ks], sB);
      }
      SBAR();
      if (u == 0) { WAITV(3); lds_post_nw(L0 + C_H + 4 * wave, 2); }
      lds_wait_ge(aF, cnt + 1);
      if (u == 0) { WAITV(0); lds_post_nw(L0 + C_H + 4 * wave, 3); }
      k0 = kof[0] + cur; k1 = kof[1] + cur; k2 = kof[2] + cur; k3 = kof[3] + cur;
#pragma unroll
      for (int ks = 0; ks < 4; ++ks) kf[ks] = lds128(KADDR(ks));
      {
        float pmax = sB[0];
#pragma unroll
        for (int i = 1; i < 16; ++i) pmax = fmaxf(pmax, sB[i]);
        auto rr = __builtin_amdgcn_permlane32_swap(__float_as_uint(pmax), __float_as_uint(pmax), false, false);
        m = fmaxf(__uint_as_float(rr[0]), __uint_as_float(rr[1]));
        const float mc = -m * CSC;
#pragma unroll
        for (int i = 0; i < 16; ++i) pe[i] = fmaf(sB[i], CSC, mc);
      }
      SBAR();
      bf16x8 pa[2];
      auto expstep = [&](int i) {
        pe[2 * i] = __builtin_amdgcn_exp2f(pe[2 * i]);
        pe[2 * i + 1] = __builtin_amdgcn_exp2f(pe[2 * i + 1]);
        l += pe[2 * i];
        l += pe[2 * i + 1];
        asm volatile("" : "+v"(pe[2 * i]), "+v"(pe[2 * i + 1]), "+v"(l));
      };
      auto cvtstep = [&](int s) {
        u32x4 w = {cvtpk(pe[8 * s], pe[8 * s + 1]), cvtpk(pe[8 * s + 2], pe[8 * s + 3]), cvtpk(pe[8 * s + 4], pe[8 * s + 5]),
                   cvtpk(pe[8 * s + 6], pe[8 * s + 7])};
        asm volatile("" : "+v"(w));
        pa[s] = __builtin_bit_cast(bf16x8, w);
      };
#pragma unroll 1
      for (int t = 0; t < 15; ++t) {
        unsigned fl = 0;
#pragma unroll
        for (int i = 0; i < 12; ++i) {
          if (i == 0) { f32x16 z = {}; sB = MFMA32(kf[0], qf[0], z); }
          else sB = MFMA32(kf[i], qf[i], sB);
          if (i < 8) kf[i + 4] = lds128(KADDR(i + 4));
          if (i == 2) fl = lds_peek(aF);
          if (i == 7) { ADV(); lds_post_nw(aR, cnt); }
          if (i < 8) expstep(i);
          if (i == 8) {
            if (__builtin_amdgcn_readfirstlane(fl) < cnt + 1) lds_wait_ge(aF, cnt + 1);
            vbE = vE + cur; vbO = vO + cur;
            cvtstep(0);
          }
          if (i == 9) cvtstep(1);
          if (i >= 8) vf[i - 8] = cat(ldstr(VADDR(i - 8)), ldstr(VADDR(i - 8) + 8 * 384));
          SBAR();
        }
        float pmax = 0.f, alpha = 1.f, mc = 0.f;
        bool resc = false;
#pragma unroll
        for (int j = 0; j < 12; ++j) {
          o[j % 6] = MFMA32(vf[j], pa[j / 6], o[j % 6]);
          if (j < 8) vf[j + 4] = cat(ldstr(VADDR(j + 4)), ldstr(VADDR(j + 4) + 8 * 384));
          if (j == 2) fl = lds_peek(aF);
          if (j == 7) { ADV(); lds_post_nw(aR, cnt); }
          if (j == 1) {
            asm volatile("" : "+v"(sB));
            pmax = MAX3(MAX3(sB[0], sB[1], sB[2]), MAX3(sB[3], sB[4], sB[5]), MAX3(sB[6], sB[7], sB[8]));
            asm volatile("" : "+v"(pmax));
          }
          if (j == 2) {
            pmax = MAX3(MAX3(sB[9], sB[10], sB[11]), MAX3(sB[12], sB[13], sB[14]), MAX3(sB[15], sB[15], pmax));
            asm volatile("" : "+v"(pmax));
          }
          if (j == 3) {
            auto rr = __builtin_amdgcn_permlane32_swap(__float_as_uint(pmax), __float_as_uint(pmax), false, false);
            pmax = fmaxf(__uint_as_float(rr[0]), __uint_as_float(rr[1]));
            resc = pmax - m > THR_RAW;
            const float mn = resc ? pmax : m;
            alpha = __builtin_amdgcn_exp2f((m - mn) * CSC);
            m = mn;
            mc = -mn * CSC;
            l *= alpha;
            asm volatile("" : "+v"(m), "+v"(mc), "+v"(l), "+v"(alpha));
          }
          if (j >= 4 && j < 8) {
#pragma unroll
            for (int i = 4 * (j - 4); i < 4 * (j - 4) + 4; ++i) pe[i] = fmaf(sB[i], CSC, mc);
            asm volatile("" : "+v"(pe[4 * (j - 4)]), "+v"(pe[4 * (j - 4) + 1]), "+v"(pe[4 * (j - 4) + 2]), "+v"(pe[4 * (j - 4) + 3]));
          }
          if (j == 8) {
            if (t < 14 && __builtin_amdgcn_readfirstlane(fl) < cnt + 1) lds_wait_ge(aF, cnt + 1);
            k0 = kof[0] + cur; k1 = kof[1] + cur; k2 = kof[2] + cur; k3 = kof[3] + cur;
          }
          if (j >= 8) kf[j - 8] = lds128(KADDR(j - 8));
          SBAR();
        }
        if (__any(resc)) {
#pragma unroll
          for (int d = 0; d < 6; ++d)
#pragma unroll
            for (int i = 0; i < 16; ++i) o[d][i] *= alpha;
        }
      }
      {
        lds_wait_ge(aF, cnt + 1);
        vbE = vE + cur; vbO = vO + cur;
#pragma unroll
        for (int j = 0; j < 6; ++j) vf[j] = cat(ldstr(VADDR(j)), ldstr(VADDR(j) + 8 * 384));
        SBAR();
#pragma unroll
        for (int i = 0; i < 8; ++i) expstep(i);
        cvtstep(0);
        cvtstep(1);
        SBAR();
#pragma unroll
        for (int j = 0; j < 12; ++j) {
          o[j % 6] = MFMA32(vf[j], pa[j / 6], o[j % 6]);
          if (j < 6) vf[j + 6] = cat(ldstr(VADDR(j + 6)), ldstr(VADDR(j + 6) + 8 * 384));
          if (j == 5) { ADV(); lds_post_nw(aR, cnt); }
          SBAR();
        }
        if (u == 0) {
          lds_wait_min4(L0 + C_QRDY, 1);
#pragma unroll
          for (int ks = 0; ks < 12; ++ks) qf[ks] = lds128(L0 + SIDE + ((r * 12 + ks) * 64 + ln) * 16);
        }
        lds_post(fme + 4, 4 * u + 1);
      }
      STAMP(2 + 4 * u);
      EST(0);
      {
        auto rr = __builtin_amdgcn_permlane32_swap(__float_as_uint(l), __float_as_uint(l), false, false);
        l = __uint_as_float(rr[0]) + __uint_as_float(rr[1]);
      }
      unsigned lz; LANEID(lz);
      const int zc = lz & 31, zh = lz >> 5;
      if (zh == 0) *(LAS f32x2*)(size_t)(L0 + MISC + (wave * 32 + zc) * 8) = f32x2{m, l};
      const __amdgpu_buffer_rsrc_t rsO = __builtin_amdgcn_make_buffer_rsrc(ws + WS_O, (short)0, 2 * 256 * 36864, 0x00020000);
      const int osw = ((zc >> 2) & 1) << 1;
      const int obase = (256 + blockIdx.x) * 36864 + (32 * r + zc) * 64;
      const unsigned xw = L0 + SIDE + r * ITB + kh * 6144 + lz * 16, xr = L0 + SIDE + r * ITB + (1 - kh) * 6144 + lz * 16;
      u32x4 snd[6];
      {
        const float invl = 1.0f / l;
#pragma unroll
        for (int d = 0; d < 3; ++d)
#pragma unroll
          for (int hf = 0; hf < 2; ++hf) {
            unsigned w4[4];
#pragma unroll
            for (int k = 0; k < 4; ++k) {
              const float a0 = kh == 0 ? o[3 + d][8 * hf + 2 * k] : o[d][8 * hf + 2 * k];
              const float a1 = kh == 0 ? o[3 + d][8 * hf + 2 * k + 1] : o[d][8 * hf + 2 * k + 1];
              w4[k] = cvtpk(a0 * invl, a1 * invl);
            }
            snd[2 * d + hf] = u32x4{w4[0], w4[1], w4[2], w4[3]};
          }
      }
      if (u == 0) {
        lds_wait_ge(fpa + 4, 1);
      } else {
        lds_wait_ge(L0 + C_OD, 1);
        lds_wait_ge(L0 + C_OD + 4, 1);
      }
      EST(1);
#pragma unroll
      for (int i = 0; i < 6; ++i) *(LAS u32x4*)(size_t)(xw + i * 1024) = snd[i];
      lds_post(fme, u + 1);
      if (u == 1) {
#pragma nounroll
        for (int it = 0; it < SPIN_MAX; ++it) {
          u32x4 d;
          asm volatile("ds_read_b128 %0, %1\n\ts_waitcnt lgkmcnt(0)" : "=v"(d) : "v"(L0 + C_R + 16) : "memory");
          if (__builtin_amdgcn_readfirstlane(min(min(d[0], d[1]), d[2])) >= 64) break;
          __builtin_amdgcn_s_sleep(1);
        }
      }
      lds_wait_ge(fpa, u + 1);
      EST(2);
#pragma unroll
      for (int i = 0; i < 6; ++i) snd[i] = *(const LAS u32x4*)(size_t)(xr + i * 1024);
      float ws_, wo_;
      {
        f32x2 stt = *(const LAS f32x2*)(size_t)(L0 + MISC + (pw * 32 + zc) * 8);
        const float M = fmaxf(m, stt[0]);
        const float as = __builtin_amdgcn_exp2f((m - M) * CSC), ao = __builtin_amdgcn_exp2f((stt[0] - M) * CSC);
        const float inv = 1.0f / (as * l + ao * stt[1]);
        ws_ = as * inv;
        wo_ = ao * stt[1] * inv;
      }
      lds_post(fme + 4, 4 * u + 2);
      u32x4 wq_[3][2];
#pragma unroll
      for (int d = 0; d < 3; ++d)
#pragma unroll
        for (int i = 0; i < 4; i += 2) {
          unsigned pk[4];
#pragma unroll
          for (int j = 0; j < 2; ++j) {
            float v[4];
#pragma unroll
            for (int k = 0; k < 4; ++k) {
              const int e = 4 * (i + j) + k;
              const float own = kh == 0 ? o[d][e] : o[3 + d][e];
              const unsigned pw2 = snd[2 * d + (e >> 3)][(e & 7) >> 1];
              const float oth = __uint_as_float((e & 1) ? (pw2 & 0xffff0000u) : (pw2 << 16));
              v[k] = own * ws_ + oth * wo_;
            }
            pk[2 * j] = cvtpk(v[0], v[1]);
            pk[2 * j + 1] = cvtpk(v[2], v[3]);
          }
          auto r0 = __builtin_amdgcn_permlane32_swap(pk[0], pk[2], false, false);
          auto r1 = __builtin_amdgcn_permlane32_swap(pk[1], pk[3], false, false);
          wq_[d][i >> 1] = u32x4{r0[0], r1[0], r0[1], r1[1]};
        }
      EST(3);
      if (u == 0) {
        lds_wait_ge(fpa + 4, 2);
#pragma unroll
        for (int d = 0; d < 3; ++d)
#pragma unroll
          for (int i = 0; i < 2; ++i)
            *(LAS u32x4*)(size_t)(L0 + SIDE + r * ITB + (3 * kh + d) * 2048 + zc * 64 + (((2 * i + zh) ^ osw) * 16)) = wq_[d][i];
        lds_post(L0 + C_OR + 4 * wave, 1);
      } else {
#pragma unroll
        for (int d = 0; d < 3; ++d)
#pragma unroll
          for (int i = 0; i < 2; ++i)
            __builtin_amdgcn_raw_buffer_store_b128(wq_[d][i], rsO, obase + (3 * kh + d) * 6144 + (((2 * i + zh) ^ osw) * 16), 0, 16);
#pragma unroll
        for (int d = 0; d < 3; ++d) {
          if (d == 2 && kh == 1) lds_wait_ge(L0 + C_XF + 4, 6);
#pragma unroll
          for (int i = 0; i < 2; ++i)
            *(LAS u32x4*)(size_t)(L0 + 61440 + (kh ? (d == 0 ? 4 : d == 1 ? 0 : 5) : d + 1) * ITB + 6144 + r * 2048 + zc * 64 + (((2 * i + zh) ^ osw) * 16)) = wq_[d][i];
        }
      }
      EST(4);
      STAMP(3 + 4 * u);
    }
  }
  {
    unsigned ln; LANEID(ln);
    constexpr unsigned TWO = 10240, TOB = 61440;
    const int ch = wave & 1, rq = wave >> 1;
    const unsigned g = ln >> 4, q4 = (ln & 15) >> 2, p4 = ln & 3, l15 = ln & 15;
    unsigned gA, gB;
    {
      auto wpiece = [&](int p) {
        const int n = 64 * p + ln, row = n / 20, pc = n % 20;
        return (unsigned)(row * (DM * 2) + ((pc ^ (((row >> 3) & 1) << 1)) * 16));
      };
      gA = wpiece(wave);
      gB = wave < 2 ? wpiece(8 + wave) : ln * 16 + (wave - 2) * 1024;
    }
    const int pc3 = wave < 6 ? wave : wave - 2;
    const unsigned gC = ln * 16 + pc3 * 1024 + 256 * 36864;
    const __amdgpu_buffer_rsrc_t rsWo = __builtin_amdgcn_make_buffer_rsrc((void*)((const char*)wo + 4 * xj * DH * DM * 2 + hq * 320), (short)0, HD * DM * 2 - (4 * xj * DH * DM * 2 + hq * 320), 0x00020000);
    const __amdgpu_buffer_rsrc_t rsOt = __builtin_amdgcn_make_buffer_rsrc(ws + WS_O + (size_t)qbase * 36864, (short)0, (512 - qbase) * 36864, 0x00020000);
    const __amdgpu_buffer_rsrc_t rsB = wave < 2 ? rsWo : rsOt;
    const unsigned mA = L0 + wave * 1024, mC = L0 + TOB + 6144 + pc3 * 1024;
    const unsigned mB = wave < 2 ? L0 + (8 + wave) * 1024 : L0 + TOB + (wave - 2) * 1024;
    const unsigned stepB = wave < 2 ? 32 * DM * 2 : 6144;
    unsigned wsrc, osrc, bsrc;
    auto sethead = [&](int hs) { wsrc = hs * (DH * DM * 2); osrc = hs * (8 * 36864); bsrc = wave < 2 ? wsrc : osrc; };
    auto request = [&](int c6, int k) {
      if (k == 0) bdma(mA + c6 * TWO, rsWo, gA, wsrc + c6 * (32 * DM * 2));
      const unsigned os = (c6 < 5 ? (c6 + 1) % 5 : 5) * ITB;
      if (k == 1) bdma1(mB + (wave < 2 ? c6 * TWO : os), rsB, gB, bsrc + c6 * stepB);
      if (k == 2) bdma1(mC + os, rsOt, gC, osrc + c6 * 6144);
    };
    const unsigned WAb = L0 + (8 * g + q4) * 320 + 160 * ch + 8 * p4;
    const unsigned WA0 = WAb + ((ch & 1) ? -32 * (int)(g & 1) : 32 * (int)(g & 1));
    const unsigned WA1 = WAb + ((ch & 1) ? 32 * (int)(g & 1) : -32 * (int)(g & 1));
    const unsigned OA = L0 + TOB + (48 * rq + l15) * 64 + ((g ^ (((l15 >> 2) & 1) << 1)) * 16);
    f32x4 acc[3][5] = {};
    bf16x8 af[2][5], bfr[2][3];
    auto rdA = [&](int par, int sl, int cb) {
      const unsigned a = ((cb & 1) ? WA1 : WA0) + sl * TWO + cb * 32;
      af[par][cb] = cat(ldstr(a), ldstr(a + 4 * 320));
    };
    auto rdB = [&](int par, int sl, int rb) { bfr[par][rb] = lds128(OA + (sl < 5 ? (sl + 1) % 5 : 5) * ITB + rb * 1024); };
    if (wave == 6) WAITV(20);
    if (wave == 7) WAITV(12);
    BAR();
    auto tbody = [&](auto VV) {
      constexpr int V = decltype(VV)::value;
#pragma unroll
      for (int rb = 0; rb < 3; ++rb) rdB(0, 0, rb);
#pragma unroll
      for (int cb = 0; cb < 5; ++cb) rdA(0, 0, cb);
      BAR();
      STAMP(12);
      auto phase = [&](auto QQ, auto MODE) {
        constexpr int q = decltype(QQ)::value, mode = decltype(MODE)::value, cur = q & 1;
        constexpr int R0 = V == 0 ? 0 : 5, D0 = V == 0 ? 9 : 0;
#pragma unroll
        for (int i = 0; i < 15; ++i) {
          acc[i % 3][i / 3] = MFMA16(af[cur][i / 3], bfr[cur][i % 3], acc[i % 3][i / 3]);
          SBAR();
          if (i >= R0 && i < R0 + 3) rdB(cur ^ 1, (q + 1) % 6, i - R0);
          if (i >= R0 + 3 && i < R0 + 8) rdA(cur ^ 1, (q + 1) % 6, i - R0 - 3);
          if (mode == 0 && i >= D0 && i < D0 + 6 && !((i - D0) & 1)) request(q, (i - D0) >> 1);
          if (mode == 2 && q >= 3 && i >= D0 && i < D0 + 6) request(2 * (q - 3) + (i - D0) / 3, (i - D0) % 3);
          SBAR();
        }
        if (mode == 0) WAITV(12);
        if (mode == 1) { if (q == 0) WAITV(9); else if (q == 1) WAITV(6); else if (q == 2) WAITV(3); else WAITV(0); }
        if (mode == 2) {
          if (q == 0 && wave < 6) WAITV(0);
          if (q == 1 && wave >= 6) WAITV(0);
          if (q == 2 && wave == 7) poll_flags(FL + (size_t)blockIdx.x * 32, 4, hq, TAG, ln);
          if (q == 4) WAITV(9);
          if (q == 5) WAITV(12);
        }
        BAR();
        if (mode == 2 && q == 0 && wave == 0 && ln < 4 && (int)ln != hq)
          __hip_atomic_store(FL + (size_t)(qbase + 8 * ln) * 32 + hq, TAG, __ATOMIC_RELAXED, __HIP_MEMORY_SCOPE_AGENT);
      };
      auto head6 = [&](auto MODE) {
        phase(std::integral_constant<int, 0>{}, MODE);
        phase(std::integral_constant<int, 1>{}, MODE);
        phase(std::integral_constant<int, 2>{}, MODE);
        phase(std::integral_constant<int, 3>{}, MODE);
        phase(std::integral_constant<int, 4>{}, MODE);
        phase(std::integral_constant<int, 5>{}, MODE);
      };
      sethead((hq + 1) & 3);
      head6(std::integral_constant<int, 2>{});
#pragma unroll 1
      for (int i = 2; i < 4; ++i) {
        sethead((hq + i) & 3);
        head6(std::integral_constant<int, 0>{});
      }
      STAMP(13);
      head6(std::integral_constant<int, 1>{});
    };
    if (wave < 4) tbody(std::integral_constant<int, 0>{}); else tbody(std::integral_constant<int, 1>{});
    STAMP(8);
    {
      const unsigned yw = L0 + (48 * rq + l15) * 336 + (80 * ch + 4 * g) * 2;
#pragma unroll
      for (int rb = 0; rb < 3; ++rb)
#pragma unroll
        for (int cb = 0; cb < 5; ++cb) {
          u32x2 w = {cvtpk(acc[rb][cb][0], acc[rb][cb][1]), cvtpk(acc[rb][cb][2], acc[rb][cb][3])};
          *(LAS u32x2*)(size_t)(yw + rb * 16 * 336 + cb * 32) = w;
        }
    }
    BAR();
    {
      const __amdgpu_buffer_rsrc_t rsP = __builtin_amdgcn_make_buffer_rsrc(ws + WS_P, (short)0, 2 * 8 * 4 * 8 * 30720, 0x00020000);
#pragma unroll
      for (int i = 0; i < 8; ++i) {
        const int j = wave + 8 * i;
        if (j < 60) {
          const int n = 64 * j + ln, row = n / 20, pc = n % 20, un = j >= 30 ? 1 : 0;
          const u32x4 v = *(const LAS u32x4*)(size_t)(L0 + row * 336 + pc * 16);
          __builtin_amdgcn_raw_buffer_store_b128(v, rsP, (((un * 8 + b0) * 4 + hq) * 8 + xj) * 30720 + (n - 1920 * un) * 16, 0, 16);
        }
      }
      WAITV(0);
      BAR();
      if (wave == 0) {
        if (ln < 8) __hip_atomic_store(FL + (size_t)((int)ln + 8 * xi) * 32 + 8 + xj, TAG, __ATOMIC_RELAXED, __HIP_MEMORY_SCOPE_AGENT);
        poll_flags(FL + (size_t)blockIdx.x * 32 + 8, 8, -1, TAG, ln);
      }
      BAR();
      STAMP(9);
      const int tz = wave * 64 + (int)ln;
      if (tz < 480) {
        const int u = tz / 240, e = tz % 240;
        const int pp = (((u * 8 + b0) * 4 + hq) * 8) * 30720 + 12 * xj * 320 + e * 16;
        u32x4 v[8];
#pragma unroll
        for (int j = 0; j < 8; ++j) v[j] = __builtin_amdgcn_raw_buffer_load_b128(rsP, pp + j * 30720, 0, 16);
        float a[8];
#pragma unroll
        for (int k = 0; k < 4; ++k) {
          float lo = 0.f, hi2 = 0.f;
#pragma unroll
          for (int j = 0; j < 8; ++j) { lo += __uint_as_float(v[j][k] << 16); hi2 += __uint_as_float(v[j][k] & 0xffff0000u); }
          a[2 * k] = lo; a[2 * k + 1] = hi2;
        }
        u32x4 w = {cvtpk(a[0], a[1]), cvtpk(a[2], a[3]), cvtpk(a[4], a[5]), cvtpk(a[6], a[7])};
        const int row = e / 20, c8 = e % 20;
        *(u32x4*)(out + (size_t)((b0 + 8 * u) * SQ + 12 * xj + row) * DM + hq * 160 + c8 * 8) = w;
      }
    }
    STAMP(10);
  }
}

extern "C" void kernel_launch(void* const* d_in, const int* in_sizes, int n_in, void* d_out, int out_size, void* d_ws,
                              size_t ws_size, hipStream_t stream) {
  const u16* x = (const u16*)d_in[0];
  const u16* kc = (const u16*)d_in[1];
  const u16* vc = (const u16*)d_in[2];
  const u16* wq = (const u16*)d_in[3];
  const u16* wo = (const u16*)d_in[4];
  (void)hipFuncSetAttribute((const void*)attn_kernel, hipFuncAttributeMaxDynamicSharedMemorySize, LDS_A);
  attn_kernel<<<256, 512, LDS_A, stream>>>(x, kc, vc, wq, wo, (u16*)d_out, (char*)d_ws, (unsigned long long*)((char*)d_ws + (128u << 20)));
}
```

```cpp
#include <hip/hip_runtime.h>
#include <hip/hip_bf16.h>
#include <stdint.h>
#include <type_traits>

typedef unsigned short u16;
typedef short bf16x8 __attribute__((ext_vector_type(8)));
typedef short s16x4 __attribute__((ext_vector_type(4)));
typedef float f32x16 __attribute__((ext_vector_type(16)));
typedef float f32x4 __attribute__((ext_vector_type(4)));
typedef float f32x2 __attribute__((ext_vector_type(2)));
typedef __bf16 bf16x2 __attribute__((ext_vector_type(2)));
typedef unsigned u32x4 __attribute__((ext_vector_type(4)));
typedef unsigned u32x2 __attribute__((ext_vector_type(2)));
#define LAS __attribute__((address_space(3)))
#define DI __device__ __forceinline__

constexpr int NB = 16, SQ = 96, DM = 640, NH = 32, SK = 1024, DH = 192;
constexpr int HD = NH * DH;

#define MFMA32(a, b, c) __builtin_amdgcn_mfma_f32_32x32x16_bf16((a), (b), (c), 0, 0, 0)
#define MFMA16(a, b, c) __builtin_amdgcn_mfma_f32_16x16x32_bf16((a), (b), (c), 0, 0, 0)
#define BAR() do { asm volatile("" ::: "memory"); __builtin_amdgcn_s_waitcnt(0xC07F); __builtin_amdgcn_s_barrier(); asm volatile("" ::: "memory"); } while (0)
#define WAITV(n) asm volatile("s_waitcnt vmcnt(%0)" ::"i"(n) : "memory")
#define SBAR() __builtin_amdgcn_sched_barrier(0)
#define MAX3(a, b, c) fmaxf(fmaxf((a), (b)), (c))
#define LANEID(v) asm volatile("v_mbcnt_lo_u32_b32 %0, -1, 0\n\tv_mbcnt_hi_u32_b32 %0, -1, %0" : "=v"(v))

DI unsigned cvtpk(float lo, float hi) {
  f32x2 v = {lo, hi};
  bf16x2 b = __builtin_convertvector(v, bf16x2);
  return __builtin_bit_cast(unsigned, b);
}
DI void dma16nt(unsigned lds_addr, const void* sbase, unsigned voff) {
  asm volatile("s_mov_b32 m0, %0\n\ts_nop 0\n\tglobal_load_lds_dwordx4 %1, %2 nt" ::"s"(lds_addr), "v"(voff), "s"(sbase) : "memory");
}
DI void bdma(unsigned lds_addr, __amdgpu_buffer_rsrc_t rs, unsigned voff, unsigned soff) {
  asm volatile("s_mov_b32 m0, %0\n\ts_nop 0\n\tbuffer_load_dwordx4 %1, %2, %3 offen lds" ::"s"(lds_addr), "v"(voff), "s"(rs), "s"(soff) : "memory");
}
DI void bdma1(unsigned lds_addr, __amdgpu_buffer_rsrc_t rs, unsigned voff, unsigned soff) {
  asm volatile("s_mov_b32 m0, %0\n\ts_nop 0\n\tbuffer_load_dwordx4 %1, %2, %3 offen sc1 lds" ::"s"(lds_addr), "v"(voff), "s"(rs), "s"(soff) : "memory");
}
DI void dma16(unsigned lds_addr, const void* sbase, unsigned voff) {
  asm volatile("s_mov_b32 m0, %0\n\ts_nop 0\n\tglobal_load_lds_dwordx4 %1, %2" ::"s"(lds_addr), "v"(voff), "s"(sbase) : "memory");
}
template <int I, int N, class F>
DI void sfor(F&& f) {
  if constexpr (I < N) { f(std::integral_constant<int, I>{}); sfor<I + 1, N>(f); }
}
DI bf16x8 lds128(unsigned a) { return *(const LAS bf16x8*)(size_t)a; }
DI s16x4 ldstr(unsigned a) { return __builtin_amdgcn_ds_read_tr16_b64_v4i16((LAS s16x4*)(size_t)a); }
DI bf16x8 cat(s16x4 a, s16x4 b) { return __builtin_shufflevector(a, b, 0, 1, 2, 3, 4, 5, 6, 7); }

constexpr unsigned ITB = 12288;
constexpr unsigned NSL = 5;
constexpr unsigned GRB = NSL * ITB;
constexpr unsigned SIDE = 2 * GRB;
constexpr unsigned MISC = SIDE + 36864;
constexpr unsigned CTL = MISC + 2048;
constexpr unsigned HB = MISC - 49152;
constexpr unsigned C_L = CTL;
constexpr unsigned C_R = CTL + 16;
constexpr unsigned C_QRDY = CTL + 48;
constexpr unsigned C_XF = CTL + 64;
constexpr unsigned C_H = CTL + 128;
constexpr unsigned C_OR = CTL + 160;
constexpr unsigned C_OD = CTL + 192;
constexpr unsigned LDS_A = 163840;
constexpr float CSC = 0.07216878364870322f * 1.4426950408889634f;
constexpr float THR_RAW = 8.0f / CSC;
constexpr int SPIN_MAX = 1 << 20;
#define HD_D 4
#define TL_D 4
#define KV0 2

#define STAMP(i)
#define EST(i)
#define FST(i)

DI void lds_post(unsigned a, unsigned v) {
  asm volatile("s_waitcnt lgkmcnt(0)\n\tds_write_b32 %0, %1" ::"v"(a), "v"(v) : "memory");
}
DI void lds_post_nw(unsigned a, unsigned v) {
  asm volatile("ds_write_b32 %0, %1" ::"v"(a), "v"(v) : "memory");
}
DI void lds_wait_ge(unsigned a, unsigned n) {
#pragma nounroll
  for (int it = 0; it < SPIN_MAX; ++it) {
    unsigned v;
    asm volatile("ds_read_b32 %0, %1\n\ts_waitcnt lgkmcnt(0)" : "=v"(v) : "v"(a) : "memory");
    if (__builtin_amdgcn_readfirstlane(v) >= n) break;
    __builtin_amdgcn_s_sleep(1);
  }
}
DI void lds_wait_min4(unsigned a, unsigned n) {
#pragma nounroll
  for (int it = 0; it < SPIN_MAX; ++it) {
    u32x4 d;
    asm volatile("ds_read_b128 %0, %1\n\ts_waitcnt lgkmcnt(0)" : "=v"(d) : "v"(a) : "memory");
    const unsigned mn = min(min(d[0], d[1]), min(d[2], d[3]));
    if (__builtin_amdgcn_readfirstlane(mn) >= n) break;
    __builtin_amdgcn_s_sleep(1);
  }
}
DI unsigned lds_rd1(unsigned a) {
  unsigned v;
  asm volatile("ds_read_b32 %0, %1\n\ts_waitcnt lgkmcnt(0)" : "=v"(v) : "v"(a) : "memory");
  return __builtin_amdgcn_readfirstlane(v);
}
DI unsigned lds_min3(unsigned a) {
  u32x4 d;
  asm volatile("ds_read_b128 %0, %1\n\ts_waitcnt lgkmcnt(0)" : "=v"(d) : "v"(a) : "memory");
  return __builtin_amdgcn_readfirstlane(min(min(d[0], d[1]), d[2]));
}
DI unsigned getvm() {
  unsigned sts;
  asm volatile("s_getreg_b32 %0, hwreg(HW_REG_IB_STS)" : "=s"(sts)::"memory");
  return (sts & 15u) | ((sts >> 18) & 0x30u);
}
DI unsigned lds_peek(unsigned a) { return *(volatile LAS unsigned*)(size_t)a; }

typedef __attribute__((address_space(1))) unsigned long long gu64;
extern "C" __device__ unsigned long long amd_dispatch_id() __asm("llvm.amdgcn.dispatch.id");
constexpr size_t WS_O = 0, WS_P = 32u << 20, WS_F = 96u << 20;

DI bool poll_flags(gu64* f, int nl, int skip, unsigned long long T, int lane) {
#pragma nounroll
  for (int it = 0; it < SPIN_MAX; ++it) {
    unsigned long long v = T;
    if (lane < nl && lane != skip) v = __hip_atomic_load(f + lane, __ATOMIC_RELAXED, __HIP_MEMORY_SCOPE_AGENT);
    if (__all(v == T)) {
      if (lane < nl && lane != skip) __hip_atomic_store(f + lane, 0ull, __ATOMIC_RELAXED, __HIP_MEMORY_SCOPE_AGENT);
      return true;
    }
    __builtin_amdgcn_s_sleep(2);
  }
  return false;
}

constexpr int hNL(int j) { return j < 20 ? 3 : 0; }
constexpr int hKV(int p) { return (p >= KV0 && p < KV0 + 12) ? 1 : 0; }
constexpr int hPend(int j) {
  int cnt = 0, first = 0;
  if (j < HD_D) { cnt = 3 * (HD_D - 1 - j); first = 0; }
  else { cnt = hKV(j - HD_D); first = j - HD_D + 1; }
  for (int p = first; p < j; ++p) cnt += hNL(p + HD_D) + hKV(p);
  return cnt;
}
constexpr int tNL(int j) { return j < 24 ? 3 : 0; }
constexpr int tPend(int j) {
  int cnt = 0, first = 0;
  if (j < TL_D) { cnt = 3 * (TL_D - 1 - j); first = 0; }
  else { cnt = 0; first = j - TL_D + 1; }
  for (int p = first; p < j; ++p) cnt += tNL(p + TL_D);
  return cnt;
}

__global__ __launch_bounds__(512, 2) void attn_kernel(const u16* x, const u16* kc, const u16* vc, const u16* wq, const u16* wo,
                                                      u16* out, char* ws, unsigned long long* stamps) {
  extern __shared__ __attribute__((aligned(16))) char lds[];
  const unsigned L0 = (unsigned)(size_t)(LAS char*)lds;
  const int tid = threadIdx.x, lane = tid & 63;
  const int wave = __builtin_amdgcn_readfirstlane(tid >> 6);
  const int xj = blockIdx.x & 7, xi = blockIdx.x >> 3;
  const int hq = xi & 3, h = 4 * xj + hq, b0 = xi >> 2;
  const size_t UB0 = (size_t)(b0 * NH + h) * SK * 384, UB1 = (size_t)((b0 + 8) * NH + h) * SK * 384;
  unsigned long long TAG;
  {
    const unsigned long long did = amd_dispatch_id(), dp = (unsigned long long)__builtin_amdgcn_dispatch_ptr();
    const unsigned t32 = (unsigned)(did * 0x9E3779B1ull) ^ (unsigned)(dp >> 6) * 0x85EBCA77u ^ (unsigned)(did >> 32);
    TAG = ((unsigned long long)(t32 ^ 0x5A17C3E9u) << 32) | t32;
  }
  const int qbase = xj + 32 * b0;
  gu64* FL = (gu64*)(ws + WS_F);
  const int kh = (0x1A >> wave) & 1, r = (0x102210 >> (4 * wave)) & 3;
  STAMP(0);
  bf16x8 qf[12];

  unsigned offK[6], offV[3];
  {
    if (wave == 6 && lane < 64) *(LAS unsigned*)(size_t)(L0 + CTL + lane * 4) = (lane == 46 || lane == 47) ? 1u : 0u;
    const int rot = (b0 * 5 / 4 + hq * 5 / 2) % 10;
    const int dh = wave & 1, rq = wave >> 1;
    const unsigned g = lane >> 4, q4 = (lane & 15) >> 2, p4 = lane & 3, l15 = lane & 15;
    const __amdgpu_buffer_rsrc_t rsW = __builtin_amdgcn_make_buffer_rsrc((void*)((const char*)wq + h * DH * 2), (short)0, DM * HD * 2 - h * DH * 2, 0x00020000);
    const __amdgpu_buffer_rsrc_t rsX = __builtin_amdgcn_make_buffer_rsrc((void*)((const char*)x + b0 * SQ * DM * 2), (short)0, (NB - b0) * SQ * DM * 2, 0x00020000);
    unsigned gA, gB, gC;
    {
      auto wpiece = [&](int p) {
        const int n = 64 * p + lane, row = n / 24, pc = n % 24;
        const int sw = ((row >> 1) & 1) | (((row >> 3) & 1) << 1);
        return (unsigned)(row * (HD * 2) + ((pc ^ (sw << 1)) * 16));
      };
      auto xpiece = [&](int p) {
        const int row = 8 * p + (lane >> 3);
        return (unsigned)(row * (DM * 2) + (((lane & 7) ^ ((row >> 1) & 7)) * 16));
      };
      gA = wpiece(wave);
      gB = wave < 4 ? wpiece(8 + wave) : xpiece(wave - 4);
      gC = xpiece(4 + wave);
    }
    unsigned hK[3], hV[3];
    constexpr unsigned WB = 3 * ITB, G1X = GRB + 2 * ITB, U1 = 8 * SQ * DM * 2;
    constexpr unsigned WSTEP = 32 * HD * 2;
    const unsigned mW = L0 + WB + wave * 1024;
    const unsigned mX = L0 + (4 + wave) * 1024;
    const unsigned s2 = ((q4 >> 1) & 1) | ((g & 1) << 1);
    unsigned E[4];
    const unsigned XA0 = L0 + (rq >> 1) * G1X + (48 * (rq & 1) + l15) * 128 + ((g ^ ((l15 >> 1) & 7)) * 16), XA1 = XA0 ^ 64;
    f32x4 acc[6][3] = {};
    bf16x8 af[2][6], bfr[2][3];
    unsigned xa;
    auto rdA = [&](int par, int c1, int db) {
      const unsigned a = E[db & 3] + (db >> 2) * 128 + (c1 & 3) * ITB;
      af[par][db] = cat(ldstr(a), ldstr(a + 4 * 384));
    };
    auto rdB = [&](int par, int rb) { bfr[par][rb] = lds128(xa + rb * 2048); };
    unsigned woff = (unsigned)(2 * rot) * WSTEP, xoff = (unsigned)rot * 128;
    auto advW = [&]() { woff += WSTEP; woff = woff == 20 * WSTEP ? 0u : woff; };
    auto advX = [&]() { xoff += 128; xoff = xoff == 1280 ? 0u : xoff; };
    auto request = [&](auto VV, int k, int wsl, unsigned xs, int un) {
      constexpr int V = decltype(VV)::value;
      if (k == 0) bdma(mW + wsl * ITB, rsW, gA, woff);
      if (k == 1) {
        if (V == 0) bdma(mW + 8192 + wsl * ITB, rsW, gB, woff);
        else bdma(mX - 8192 + un * G1X + xs, rsX, gB, xoff + un * U1);
      }
      if (k == 2) bdma(mX + un * G1X + xs, rsX, gC, xoff + un * U1);
    };
    unsigned xr = 2 * ITB;
    unsigned xw = 1 * ITB;
    auto body = [&](auto VV) {
      constexpr int V = decltype(VV)::value;
      if (V == 1) bdma(mX - 8192 + 2 * ITB, rsX, gB, xoff);
      bdma(mX + 2 * ITB, rsX, gC, xoff);
#pragma unroll
      for (int c = -4; c < 0; ++c) {
        if ((c & 1) != 0) advX();
        const unsigned xs = (unsigned)((((c + 1) >> 1) + 2 + 2) % 3) * ITB;
#pragma unroll
        for (int k = 0; k < 3; ++k) request(VV, k, c & 3, xs, (c + 1) & 1);
        advW();
      }
      SBAR();
      asm volatile("" ::: "memory");
      if (wave >= 6) {
        const char* wsrc = (const char*)wo + (size_t)(4 * xj * DH + 96 * b0 + 48 * (wave - 6)) * (DM * 2) + hq * 320;
        const unsigned o0 = (lane >> 2) * (DM * 2) + min((unsigned)(lane & 3) * 128u, 316u);
#pragma unroll
        for (int z = 0; z < 3; ++z)
          asm volatile("s_mov_b32 m0, %0\n\ts_nop 0\n\tglobal_load_lds_dword %1, %2" ::"s"(L0 + MISC + 1536), "v"(o0 + z * 16 * DM * 2), "s"(wsrc) : "memory");
        const char* t0 = lane == 0 ? ws + WS_O + (size_t)blockIdx.x * 36864 : lane == 1 ? ws + WS_O + (size_t)(256 + blockIdx.x) * 36864
                       : lane == 2 ? ws + WS_P + (size_t)(((b0 * 4 + hq) * 8 + xj) * 30720) : lane == 3 ? ws + WS_P + (size_t)((((8 + b0) * 4 + hq) * 8 + xj) * 30720)
                       : lane == 4 ? ws + WS_F + (size_t)blockIdx.x * 256 : (const char*)out + (size_t)((b0 + 8 * (lane & 1)) * SQ + 12 * xj) * DM * 2;
        if (wave == 7 && lane < 7)
          asm volatile("s_mov_b32 m0, %0\n\ts_nop 0\n\tglobal_load_lds_dword %1, off" ::"s"(L0 + MISC + 1536), "v"(t0) : "memory");
      }
#pragma unroll
    for (int j = 0; j < 6; ++j) {
      int n = 64 * j + lane, row = n / 24, pc = n % 24;
      offK[j] = row * 384 + ((pc ^ ((row >> 1) & 7)) * 16);
    }
#pragma unroll
    for (int j = 0; j < 3; ++j) {
      int n = 64 * j + lane, row = n / 24, pc = n % 24;
      offV[j] = row * 384 + ((pc ^ (((row >> 1) & 1) << 2)) * 16);
    }
#pragma unroll
    for (int j = 0; j < 3; ++j) {
      const int n = 64 * (3 * (wave & 3) + j) + lane, row = n / 24, pc = n % 24;
      hK[j] = row * 384 + ((pc ^ ((row >> 1) & 7)) * 16);
      hV[j] = row * 384 + ((pc ^ (((row >> 1) & 1) << 2)) * 16);
    }
#pragma unroll
      for (int j = 0; j < 4; ++j) E[j] = L0 + WB + (8 * g + q4) * 384 + 32 * ((unsigned)(6 * dh + j) ^ s2) + 8 * p4;
      WAITV(9);
      BAR();
      xa = XA0 + 2 * ITB;
#pragma unroll
      for (int rb = 0; rb < 3; ++rb) rdB(0, rb);
#pragma unroll
      for (int db = 0; db < 6; ++db) rdA(0, 0, db);
      WAITV(6);
      BAR();
      STAMP(4);
      auto phase = [&](int c, auto QQ, auto MODE) {
        constexpr int q = decltype(QQ)::value, mode = decltype(MODE)::value, cur = q & 1;
        if (cur == 1) {
          xr = xr == 2 * ITB ? 0 : xr + ITB;
          if (mode == 0) { xw = xw == 2 * ITB ? 0 : xw + ITB; advX(); }
        }
        xa = (cur == 1 ? XA0 : XA1) + xr;
        constexpr int R0 = V == 0 ? 0 : 5, D0 = V == 0 ? 10 : 0;
#pragma unroll
        for (int i = 0; i < 18; ++i) {
          acc[i / 3][i % 3] = MFMA16(af[cur][i / 3], bfr[cur][i % 3], acc[i / 3][i % 3]);
          SBAR();
          if (i >= R0 && i < R0 + 3) rdB(cur ^ 1, i - R0);
          if (i >= R0 + 3 && i < R0 + 9) rdA(cur ^ 1, q + 1, i - R0 - 3);
          if (mode == 0 && i >= D0 && i < D0 + 6 && !((i - D0) & 1)) request(VV, (i - D0) >> 1, q, xw, (q + 1) & 1);
          if (mode == 0 && i == D0 + 5) advW();
          if (mode == 2 && i >= D0 && i < D0 + 6 && !((i - D0) & 1)) {
            const int k = (i - D0) >> 1, li = c - 17;
            const unsigned dst = L0 + V * G1X + li * ITB + (3 * (wave & 3) + k) * 1024;
            if (li == 2) dma16nt(dst, (const char*)vc + (V ? UB1 : UB0), hV[k]);
            else dma16nt(dst, (const char*)kc + (V ? UB1 : UB0) + (size_t)li * ITB, hK[k]);
          }
          SBAR();
        }
        if (mode == 0) WAITV(6);
        if (mode == 1) WAITV(0);
        BAR();
      };
      using I0 = std::integral_constant<int, 0>; using I1 = std::integral_constant<int, 1>;
      using I2 = std::integral_constant<int, 2>; using I3 = std::integral_constant<int, 3>;
#pragma unroll 1
      for (int c = 0; c < 16; c += 4) {
        phase(c, I0{}, I0{});
        phase(c + 1, I1{}, I0{});
        phase(c + 2, I2{}, I0{});
        phase(c + 3, I3{}, I0{});
      }
      STAMP(5);
      phase(16, I0{}, I1{});
      phase(17, I1{}, I2{});
      phase(18, I2{}, I2{});
      phase(19, I3{}, I2{});
    };
    if (wave < 4) body(std::integral_constant<int, 0>{}); else body(std::integral_constant<int, 1>{});
    STAMP(11);
    {
      unsigned lz; LANEID(lz);
      const unsigned zg = lz >> 4, zl = lz & 15;
      const unsigned qimg = L0 + (wave < 4 ? SIDE : WB);
#pragma unroll
      for (int rb = 0; rb < 3; ++rb) {
        const int R16 = 3 * (rq & 1) + rb;
        const unsigned qb = qimg + ((R16 >> 1) * 12 + 6 * dh) * 1024 + (16 * (R16 & 1) + zl + 32 * (zg >> 1)) * 16 + 8 * (zg & 1);
#pragma unroll
        for (int db = 0; db < 6; ++db) {
          u32x2 w = {cvtpk(acc[db][rb][0], acc[db][rb][1]), cvtpk(acc[db][rb][2], acc[db][rb][3])};
          *(LAS u32x2*)(size_t)(qb + db * 1024) = w;
        }
      }
      BAR();
      if (wave < 6) {
#pragma unroll
        for (int ks = 0; ks < 12; ++ks) qf[ks] = lds128(L0 + (kh ? WB : SIDE) + ((r * 12 + ks) * 64 + lz) * 16);
      }
      BAR();
    }
  }
  STAMP(1);

  if (wave >= 6) {
    const int hw = wave - 6;
    __builtin_amdgcn_s_setprio(3);
    unsigned ll; LANEID(ll);
    const unsigned aL = L0 + C_L + 4 * hw, aR = L0 + C_R + 16 * hw;
    auto issue_item = [&](unsigned li_, unsigned slot_) {
      const bool isK = (li_ == 0) || ((li_ & 1) && li_ < 63);
      const unsigned tile = isK ? (li_ + 1) >> 1 : (li_ - 1) >> 1;
      const size_t ub = (hw ? UB1 : UB0) + (size_t)tile * ITB;
      const unsigned dst = L0 + hw * GRB + slot_;
      if (isK) {
        const char* src = (const char*)kc + ub;
#pragma unroll
        for (int q = 0; q < 12; ++q) dma16nt(dst + q * 1024, src + (q / 6) * 6144, offK[q % 6]);
      } else {
        const char* src = (const char*)vc + ub;
#pragma unroll
        for (int q = 0; q < 12; ++q) dma16nt(dst + q * 1024, src + (q / 3) * 3072, offV[q % 3]);
      }
    };
    issue_item(3, hw ? 0 : 3 * ITB);
    issue_item(4, hw ? ITB : 4 * ITB);
    WAITV(30); lds_post_nw(L0 + C_H + 4 * wave, 1); lds_wait_min4(L0 + C_H + 16 * hw, 1); lds_post_nw(aL, 1);
    WAITV(27); lds_post_nw(L0 + C_H + 4 * wave, 2); lds_wait_min4(L0 + C_H + 16 * hw, 2); lds_post_nw(aL, 2);
    WAITV(24); lds_post_nw(L0 + C_H + 4 * wave, 3); lds_wait_min4(L0 + C_H + 16 * hw, 3); lds_post_nw(aL, 3);
    unsigned li = 5, lpub = 3, slot = hw ? 2 * ITB : 0;
    unsigned tix = 0, xt = 0;
    const __amdgpu_buffer_rsrc_t rsWo = __builtin_amdgcn_make_buffer_rsrc((void*)((const char*)wo + 4 * xj * DH * DM * 2 + hq * 320), (short)0, HD * DM * 2 - (4 * xj * DH * DM * 2 + hq * 320), 0x00020000);
    unsigned go[5];
#pragma unroll
    for (int p = 0; p < 5; ++p) {
      const int n = 64 * p + ll, row = n / 20, pc = n % 20;
      go[p] = row * (DM * 2) + ((pc ^ (((row >> 3) & 1) << 1)) * 16);
    }
#pragma nounroll
    for (int it = 0; it < (SPIN_MAX << 2); ++it) {
      bool idle = true;
      if (li < 64) {
        if (lds_min3(aR) + NSL > li && getvm() <= 48) {
          issue_item(li, slot);
          ++li;
          slot = slot + ITB == GRB ? 0 : slot + ITB;
          idle = false;
        }
      } else if (tix < 5) {
        if (lds_min3(L0 + C_R) >= 60 + tix && getvm() <= 50) {
          const unsigned c0 = tix == 0 ? 5 : tix - 1, nc = tix == 4 ? 2 : 1;
#pragma unroll 1
          for (unsigned c6 = c0; c6 < c0 + nc; ++c6) {
#pragma unroll
            for (int p = 0; p < 5; ++p) bdma(L0 + c6 * 10240 + (5 * hw + p) * 1024, rsWo, go[p], (hq * DH + 32 * c6 + 16 * hw) * (DM * 2));
            xt += 5;
          }
          ++tix;
          idle = false;
        }
      }
      if (lpub < li) {
        const unsigned vm = getvm();
        unsigned done = lpub;
#pragma nounroll
        while (done < li) {
          const unsigned after = 12 * (li - 1 - done) + xt;
          if (vm > after) break;
          ++done;
        }
        if (done > lpub) {
          lpub = done;
          lds_post_nw(aL, lpub);
          idle = false;
        }
      } else if (li == 64 && tix == 5) {
        break;
      }
      if (idle) __builtin_amdgcn_s_sleep(1);
    }
    __builtin_amdgcn_s_setprio(0);
  } else {
    unsigned ln; LANEID(ln);
    unsigned kof[4], vE, vO;
    {
      const unsigned a32 = ln & 31, ahi = ln >> 5, ablk = (ln >> 4) & 1, aq4 = (ln & 15) >> 2, ap4 = ln & 3, axq = (aq4 >> 1) & 1;
#pragma unroll
      for (int i = 0; i < 4; ++i) kof[i] = L0 + kh * GRB + a32 * 384 + (((2 * i + ahi) ^ ((a32 >> 1) & 7)) * 16);
      vE = L0 + kh * GRB + (4 * ahi + aq4) * 384 + 64 * axq + 32 * ablk + 8 * ap4;
      vO = L0 + kh * GRB + (4 * ahi + aq4) * 384 - 64 * axq + 32 * ablk + 8 * ap4;
    }
    const unsigned aF = L0 + C_L + 4 * kh, aR = L0 + C_R + 16 * kh + 4 * r;
    const int pw = (0x102354 >> (4 * wave)) & 7;
    const unsigned fme = L0 + C_XF + 8 * wave, fpa = L0 + C_XF + 8 * pw;
    unsigned cur = kh ? 2 * ITB : 0;
    unsigned cnt = 0;
#define ADV() do { cur = (cur + ITB == GRB) ? 0u : cur + ITB; ++cnt; } while (0)
#define KADDR(ks) (((ks) & 3) == 0 ? k0 : ((ks) & 3) == 1 ? k1 : ((ks) & 3) == 2 ? k2 : k3) + 128 * ((ks) >> 2)
#define VADDR(j) ((((j) % 6) & 1) ? vbO : vbE) + (16 * ((j) / 6)) * 384 + 64 * ((j) % 6)

    {
      float m, l = 0.f;
      f32x16 o[6] = {};
      f32x16 sB;
      float pe[16];
      bf16x8 kf[12], vf[12];
      unsigned k0, k1, k2, k3, vbE, vbO;
      WAITV(6); lds_post_nw(L0 + C_H + 4 * wave, 1);
      lds_wait_ge(aF, cnt + 1);
      k0 = kof[0] + cur; k1 = kof[1] + cur; k2 = kof[2] + cur; k3 = kof[3] + cur;
#pragma unroll
      for (int ks = 0; ks < 12; ++ks) kf[ks] = lds128(KADDR(ks));
      ADV();
      lds_post_nw(aR, cnt);
      SBAR();
      {
        f32x16 z = {};
        sB = MFMA32(kf[0], qf[0], z);
#pragma unroll
        for (int ks = 1; ks < 12; ++ks) sB = MFMA32(kf[ks], qf[ks], sB);
      }
      SBAR();
      WAITV(3); lds_post_nw(L0 + C_H + 4 * wave, 2);
      lds_wait_ge(aF, cnt + 1);
      WAITV(0); lds_post_nw(L0 + C_H + 4 * wave, 3);
      k0 = kof[0] + cur; k1 = kof[1] + cur; k2 = kof[2] + cur; k3 = kof[3] + cur;
#pragma unroll
      for (int ks = 0; ks < 4; ++ks) kf[ks] = lds128(KADDR(ks));
      {
        float pmax = sB[0];
#pragma unroll
        for (int i = 1; i < 16; ++i) pmax = fmaxf(pmax, sB[i]);
        auto rr = __builtin_amdgcn_permlane32_swap(__float_as_uint(pmax), __float_as_uint(pmax), false, false);
        m = fmaxf(__uint_as_float(rr[0]), __uint_as_float(rr[1]));
        const float mc = -m * CSC;
#pragma unroll
        for (int i = 0; i < 16; ++i) pe[i] = fmaf(sB[i], CSC, mc);
      }
      SBAR();
      bf16x8 pa[2];
      auto expstep = [&](int i) {
        pe[2 * i] = __builtin_amdgcn_exp2f(pe[2 * i]);
        pe[2 * i + 1] = __builtin_amdgcn_exp2f(pe[2 * i + 1]);
        l += pe[2 * i];
        l += pe[2 * i + 1];
        asm volatile("" : "+v"(pe[2 * i]), "+v"(pe[2 * i + 1]), "+v"(l));
      };
      auto cvtstep = [&](int s) {
        u32x4 w = {cvtpk(pe[8 * s], pe[8 * s + 1]), cvtpk(pe[8 * s + 2], pe[8 * s + 3]), cvtpk(pe[8 * s + 4], pe[8 * s + 5]),
                   cvtpk(pe[8 * s + 6], pe[8 * s + 7])};
        asm volatile("" : "+v"(w));
        pa[s] = __builtin_bit_cast(bf16x8, w);
      };
#pragma unroll 1
      for (int t = 0; t < 31; ++t) {
        unsigned fl = 0;
#pragma unroll
        for (int i = 0; i < 12; ++i) {
          if (i == 0) { f32x16 z = {}; sB = MFMA32(kf[0], qf[0], z); }
          else sB = MFMA32(kf[i], qf[i], sB);
          if (i < 8) kf[i + 4] = lds128(KADDR(i + 4));
          if (i == 2) fl = lds_peek(aF);
          if (i == 7) { ADV(); lds_post_nw(aR, cnt); }
          if (i < 8) expstep(i);
          if (i == 8) {
            if (__builtin_amdgcn_readfirstlane(fl) < cnt + 1) lds_wait_ge(aF, cnt + 1);
            vbE = vE + cur; vbO = vO + cur;
            cvtstep(0);
          }
          if (i == 9) cvtstep(1);
          if (i >= 8) vf[i - 8] = cat(ldstr(VADDR(i - 8)), ldstr(VADDR(i - 8) + 8 * 384));
          SBAR();
        }
        float pmax = 0.f, alpha = 1.f, mc = 0.f;
        bool resc = false;
#pragma unroll
        for (int j = 0; j < 12; ++j) {
          o[j % 6] = MFMA32(vf[j], pa[j / 6], o[j % 6]);
          if (j < 8) vf[j + 4] = cat(ldstr(VADDR(j + 4)), ldstr(VADDR(j + 4) + 8 * 384));
          if (j == 2) fl = lds_peek(aF);
          if (j == 7) { ADV(); lds_post_nw(aR, cnt); }
          if (j == 1) {
            asm volatile("" : "+v"(sB));
            pmax = MAX3(MAX3(sB[0], sB[1], sB[2]), MAX3(sB[3], sB[4], sB[5]), MAX3(sB[6], sB[7], sB[8]));
            asm volatile("" : "+v"(pmax));
          }
          if (j == 2) {
            pmax = MAX3(MAX3(sB[9], sB[10], sB[11]), MAX3(sB[12], sB[13], sB[14]), MAX3(sB[15], sB[15], pmax));
            asm volatile("" : "+v"(pmax));
          }
          if (j == 3) {
            auto rr = __builtin_amdgcn_permlane32_swap(__float_as_uint(pmax), __float_as_uint(pmax), false, false);
            pmax = fmaxf(__uint_as_float(rr[0]), __uint_as_float(rr[1]));
            resc = pmax - m > THR_RAW;
            const float mn = resc ? pmax : m;
            alpha = __builtin_amdgcn_exp2f((m - mn) * CSC);
            m = mn;
            mc = -mn * CSC;
            l *= alpha;
            asm volatile("" : "+v"(m), "+v"(mc), "+v"(l), "+v"(alpha));
          }
          if (j >= 4 && j < 8) {
#pragma unroll
            for (int i = 4 * (j - 4); i < 4 * (j - 4) + 4; ++i) pe[i] = fmaf(sB[i], CSC, mc);
            asm volatile("" : "+v"(pe[4 * (j - 4)]), "+v"(pe[4 * (j - 4) + 1]), "+v"(pe[4 * (j - 4) + 2]), "+v"(pe[4 * (j - 4) + 3]));
          }
          if (j == 8) {
            if (t < 30 && __builtin_amdgcn_readfirstlane(fl) < cnt + 1) lds_wait_ge(aF, cnt + 1);
            k0 = kof[0] + cur; k1 = kof[1] + cur; k2 = kof[2] + cur; k3 = kof[3] + cur;
          }
          if (j >= 8) kf[j - 8] = lds128(KADDR(j - 8));
          SBAR();
        }
        if (__any(resc)) {
#pragma unroll
          for (int d = 0; d < 6; ++d)
#pragma unroll
            for (int i = 0; i < 16; ++i) o[d][i] *= alpha;
        }
      }
      {
        lds_wait_ge(aF, cnt + 1);
        vbE = vE + cur; vbO = vO + cur;
#pragma unroll
        for (int j = 0; j < 6; ++j) vf[j] = cat(ldstr(VADDR(j)), ldstr(VADDR(j) + 8 * 384));
        SBAR();
#pragma unroll
        for (int i = 0; i < 8; ++i) expstep(i);
        cvtstep(0);
        cvtstep(1);
        SBAR();
#pragma unroll
        for (int j = 0; j < 12; ++j) {
          o[j % 6] = MFMA32(vf[j], pa[j / 6], o[j % 6]);
          if (j < 6) vf[j + 6] = cat(ldstr(VADDR(j + 6)), ldstr(VADDR(j + 6) + 8 * 384));
          if (j == 5) { ADV(); lds_post_nw(aR, cnt); }
          SBAR();
        }
      }
      STAMP(6);
      {
        auto rr = __builtin_amdgcn_permlane32_swap(__float_as_uint(l), __float_as_uint(l), false, false);
        l = __uint_as_float(rr[0]) + __uint_as_float(rr[1]);
      }
      const float inv = 1.0f / l;
      unsigned lz; LANEID(lz);
      const int zc = lz & 31, zh = lz >> 5;
      const __amdgpu_buffer_rsrc_t rsO = __builtin_amdgcn_make_buffer_rsrc(ws + WS_O, (short)0, 2 * 256 * 36864, 0x00020000);
      const int osw = ((zc >> 2) & 1) << 1;
      const int obase = (kh * 256 + blockIdx.x) * 36864 + (32 * r + zc) * 64;
      u32x4 wq_[6][2];
#pragma unroll
      for (int d = 0; d < 6; ++d)
#pragma unroll
        for (int i = 0; i < 4; i += 2) {
          unsigned pk[4];
#pragma unroll
          for (int j = 0; j < 2; ++j) {
            pk[2 * j] = cvtpk(o[d][4 * (i + j)] * inv, o[d][4 * (i + j) + 1] * inv);
            pk[2 * j + 1] = cvtpk(o[d][4 * (i + j) + 2] * inv, o[d][4 * (i + j) + 3] * inv);
          }
          auto r0 = __builtin_amdgcn_permlane32_swap(pk[0], pk[2], false, false);
          auto r1 = __builtin_amdgcn_permlane32_swap(pk[1], pk[3], false, false);
          wq_[d][i >> 1] = u32x4{r0[0], r1[0], r0[1], r1[1]};
          __builtin_amdgcn_raw_buffer_store_b128(wq_[d][i >> 1], rsO, obase + d * 6144 + (((i + zh) ^ osw) * 16), 0, 16);
        }
#pragma nounroll
      for (int it = 0; it < SPIN_MAX; ++it) {
        if (lds_min3(L0 + C_R + 16) >= 64) break;
        __builtin_amdgcn_s_sleep(1);
      }
#pragma unroll
      for (int d = 0; d < 6; ++d)
#pragma unroll
        for (int i = 0; i < 2; ++i)
          *(LAS u32x4*)(size_t)(L0 + 61440 + (d < 5 ? (d + 1) % 5 : 5) * ITB + kh * 6144 + r * 2048 + zc * 64 + (((2 * i + zh) ^ osw) * 16)) = wq_[d][i];
      STAMP(7);
    }
  }
  {
    unsigned ln; LANEID(ln);
    constexpr unsigned TWO = 10240, TOB = 61440;
    const int ch = wave & 1, rq = wave >> 1;
    const unsigned g = ln >> 4, q4 = (ln & 15) >> 2, p4 = ln & 3, l15 = ln & 15;
    unsigned gA, gB;
    {
      auto wpiece = [&](int p) {
        const int n = 64 * p + ln, row = n / 20, pc = n % 20;
        return (unsigned)(row * (DM * 2) + ((pc ^ (((row >> 3) & 1) << 1)) * 16));
      };
      gA = wpiece(wave);
      gB = wave < 2 ? wpiece(8 + wave) : ln * 16 + (wave - 2) * 1024;
    }
    const int pc3 = wave < 6 ? wave : wave - 2;
    const unsigned gC = ln * 16 + pc3 * 1024 + 256 * 36864;
    const __amdgpu_buffer_rsrc_t rsWo = __builtin_amdgcn_make_buffer_rsrc((void*)((const char*)wo + 4 * xj * DH * DM * 2 + hq * 320), (short)0, HD * DM * 2 - (4 * xj * DH * DM * 2 + hq * 320), 0x00020000);
    const __amdgpu_buffer_rsrc_t rsOt = __builtin_amdgcn_make_buffer_rsrc(ws + WS_O + (size_t)qbase * 36864, (short)0, (512 - qbase) * 36864, 0x00020000);
    const __amdgpu_buffer_rsrc_t rsB = wave < 2 ? rsWo : rsOt;
    const unsigned mA = L0 + wave * 1024, mC = L0 + TOB + 6144 + pc3 * 1024;
    const unsigned mB = wave < 2 ? L0 + (8 + wave) * 1024 : L0 + TOB + (wave - 2) * 1024;
    const unsigned stepB = wave < 2 ? 32 * DM * 2 : 6144;
    unsigned wsrc, osrc, bsrc;
    auto sethead = [&](int hs) { wsrc = hs * (DH * DM * 2); osrc = hs * (8 * 36864); bsrc = wave < 2 ? wsrc : osrc; };
    auto request = [&](int c6, int k) {
      if (k == 0) bdma(mA + c6 * TWO, rsWo, gA, wsrc + c6 * (32 * DM * 2));
      const unsigned os = (c6 < 5 ? (c6 + 1) % 5 : 5) * ITB;
      if (k == 1) bdma1(mB + (wave < 2 ? c6 * TWO : os), rsB, gB, bsrc + c6 * stepB);
      if (k == 2) bdma1(mC + os, rsOt, gC, osrc + c6 * 6144);
    };
    const unsigned WAb = L0 + (8 * g + q4) * 320 + 160 * ch + 8 * p4;
    const unsigned WA0 = WAb + ((ch & 1) ? -32 * (int)(g & 1) : 32 * (int)(g & 1));
    const unsigned WA1 = WAb + ((ch & 1) ? 32 * (int)(g & 1) : -32 * (int)(g & 1));
    const unsigned OA = L0 + TOB + (48 * rq + l15) * 64 + ((g ^ (((l15 >> 2) & 1) << 1)) * 16);
    f32x4 acc[3][5] = {};
    bf16x8 af[2][5], bfr[2][3];
    auto rdA = [&](int par, int sl, int cb) {
      const unsigned a = ((cb & 1) ? WA1 : WA0) + sl * TWO + cb * 32;
      af[par][cb] = cat(ldstr(a), ldstr(a + 4 * 320));
    };
    auto rdB = [&](int par, int sl, int rb) { bfr[par][rb] = lds128(OA + (sl < 5 ? (sl + 1) % 5 : 5) * ITB + rb * 1024); };
    if (wave >= 6) WAITV(10);
    BAR();
    auto tbody = [&](auto VV) {
      constexpr int V = decltype(VV)::value;
#pragma unroll
      for (int rb = 0; rb < 3; ++rb) rdB(0, 0, rb);
#pragma unroll
      for (int cb = 0; cb < 5; ++cb) rdA(0, 0, cb);
      BAR();
      STAMP(12);
      auto phase = [&](auto QQ, auto MODE) {
        constexpr int q = decltype(QQ)::value, mode = decltype(MODE)::value, cur = q & 1;
        constexpr int R0 = V == 0 ? 0 : 5, D0 = V == 0 ? 9 : 0;
#pragma unroll
        for (int i = 0; i < 15; ++i) {
          acc[i % 3][i / 3] = MFMA16(af[cur][i / 3], bfr[cur][i % 3], acc[i % 3][i / 3]);
          SBAR();
          if (i >= R0 && i < R0 + 3) rdB(cur ^ 1, (q + 1) % 6, i - R0);
          if (i >= R0 + 3 && i < R0 + 8) rdA(cur ^ 1, (q + 1) % 6, i - R0 - 3);
          if (mode == 0 && i >= D0 && i < D0 + 6 && !((i - D0) & 1)) request(q, (i - D0) >> 1);
          if (mode == 2 && q >= 3 && i >= D0 && i < D0 + 6) request(2 * (q - 3) + (i - D0) / 3, (i - D0) % 3);
          SBAR();
        }
        if (mode == 0) WAITV(12);
        if (mode == 1) { if (q == 0) WAITV(9); else if (q == 1) WAITV(6); else if (q == 2) WAITV(3); else WAITV(0); }
        if (mode == 2) {
          if (q == 0 && wave < 6) WAITV(0);
          if (q == 1 && wave >= 6) WAITV(0);
          if (q == 2 && wave == 7) poll_flags(FL + (size_t)blockIdx.x * 32, 4, hq, TAG, ln);
          if (q == 4) WAITV(9);
          if (q == 5) WAITV(12);
        }
        BAR();
        if (mode == 2 && q == 0 && wave == 0 && ln < 4 && (int)ln != hq)
          __hip_atomic_store(FL + (size_t)(qbase + 8 * ln) * 32 + hq, TAG, __ATOMIC_RELAXED, __HIP_MEMORY_SCOPE_AGENT);
      };
      auto head6 = [&](auto MODE) {
        phase(std::integral_constant<int, 0>{}, MODE);
        phase(std::integral_constant<int, 1>{}, MODE);
        phase(std::integral_constant<int, 2>{}, MODE);
        phase(std::integral_constant<int, 3>{}, MODE);
        phase(std::integral_constant<int, 4>{}, MODE);
        phase(std::integral_constant<int, 5>{}, MODE);
      };
      sethead((hq + 1) & 3);
      head6(std::integral_constant<int, 2>{});
#pragma unroll 1
      for (int i = 2; i < 4; ++i) {
        sethead((hq + i) & 3);
        head6(std::integral_constant<int, 0>{});
      }
      STAMP(13);
      head6(std::integral_constant<int, 1>{});
    };
    if (wave < 4) tbody(std::integral_constant<int, 0>{}); else tbody(std::integral_constant<int, 1>{});
    STAMP(8);
    {
      const unsigned yw = L0 + (48 * rq + l15) * 336 + (80 * ch + 4 * g) * 2;
#pragma unroll
      for (int rb = 0; rb < 3; ++rb)
#pragma unroll
        for (int cb = 0; cb < 5; ++cb) {
          u32x2 w = {cvtpk(acc[rb][cb][0], acc[rb][cb][1]), cvtpk(acc[rb][cb][2], acc[rb][cb][3])};
          *(LAS u32x2*)(size_t)(yw + rb * 16 * 336 + cb * 32) = w;
        }
    }
    BAR();
    {
      const __amdgpu_buffer_rsrc_t rsP = __builtin_amdgcn_make_buffer_rsrc(ws + WS_P, (short)0, 2 * 8 * 4 * 8 * 30720, 0x00020000);
#pragma unroll
      for (int i = 0; i < 8; ++i) {
        const int j = wave + 8 * i;
        if (j < 60) {
          const int n = 64 * j + ln, row = n / 20, pc = n % 20, un = j >= 30 ? 1 : 0;
          const u32x4 v = *(const LAS u32x4*)(size_t)(L0 + row * 336 + pc * 16);
          __builtin_amdgcn_raw_buffer_store_b128(v, rsP, (((un * 8 + b0) * 4 + hq) * 8 + xj) * 30720 + (n - 1920 * un) * 16, 0, 16);
        }
      }
      WAITV(0);
      BAR();
      if (wave == 0) {
        if (ln < 8) __hip_atomic_store(FL + (size_t)((int)ln + 8 * xi) * 32 + 8 + xj, TAG, __ATOMIC_RELAXED, __HIP_MEMORY_SCOPE_AGENT);
        poll_flags(FL + (size_t)blockIdx.x * 32 + 8, 8, -1, TAG, ln);
      }
      BAR();
      STAMP(9);
      const int tz = wave * 64 + (int)ln;
      if (tz < 480) {
        const int u = tz / 240, e = tz % 240;
        const int pp = (((u * 8 + b0) * 4 + hq) * 8) * 30720 + 12 * xj * 320 + e * 16;
        u32x4 v[8];
#pragma unroll
        for (int j = 0; j < 8; ++j) v[j] = __builtin_amdgcn_raw_buffer_load_b128(rsP, pp + j * 30720, 0, 16);
        float a[8];
#pragma unroll
        for (int k = 0; k < 4; ++k) {
          float lo = 0.f, hi2 = 0.f;
#pragma unroll
          for (int j = 0; j < 8; ++j) { lo += __uint_as_float(v[j][k] << 16); hi2 += __uint_as_float(v[j][k] & 0xffff0000u); }
          a[2 * k] = lo; a[2 * k + 1] = hi2;
        }
        u32x4 w = {cvtpk(a[0], a[1]), cvtpk(a[2], a[3]), cvtpk(a[4], a[5]), cvtpk(a[6], a[7])};
        const int row = e / 20, c8 = e % 20;
        *(u32x4*)(out + (size_t)((b0 + 8 * u) * SQ + 12 * xj + row) * DM + hq * 160 + c8 * 8) = w;
      }
    }
    STAMP(10);
  }
}

extern "C" void kernel_launch(void* const* d_in, const int* in_sizes, int n_in, void* d_out, int out_size, void* d_ws,
                              size_t ws_size, hipStream_t stream) {
  const u16* x = (const u16*)d_in[0];
  const u16* kc = (const u16*)d_in[1];
  const u16* vc = (const u16*)d_in[2];
  const u16* wq = (const u16*)d_in[3];
  const u16* wo = (const u16*)d_in[4];
  (void)hipFuncSetAttribute((const void*)attn_kernel, hipFuncAttributeMaxDynamicSharedMemorySize, LDS_A);
  attn_kernel<<<256, 512, LDS_A, stream>>>(x, kc, vc, wq, wo, (u16*)d_out, (char*)d_ws, (unsigned long long*)((char*)d_ws + (128u << 20)));
}
```

```cpp
#include <hip/hip_runtime.h>
#include <hip/hip_bf16.h>
#include <stdint.h>
#include <type_traits>

typedef unsigned short u16;
typedef short bf16x8 __attribute__((ext_vector_type(8)));
typedef short s16x4 __attribute__((ext_vector_type(4)));
typedef float f32x16 __attribute__((ext_vector_type(16)));
typedef float f32x4 __attribute__((ext_vector_type(4)));
typedef float f32x2 __attribute__((ext_vector_type(2)));
typedef __bf16 bf16x2 __attribute__((ext_vector_type(2)));
typedef unsigned u32x4 __attribute__((ext_vector_type(4)));
typedef unsigned u32x2 __attribute__((ext_vector_type(2)));
#define LAS __attribute__((address_space(3)))
#define DI __device__ __forceinline__

constexpr int NB = 16, SQ = 96, DM = 640, NH = 32, SK = 1024, DH = 192;
constexpr int HD = NH * DH;

#define MFMA32(a, b, c) __builtin_amdgcn_mfma_f32_32x32x16_bf16((a), (b), (c), 0, 0, 0)
#define MFMA16(a, b, c) __builtin_amdgcn_mfma_f32_16x16x32_bf16((a), (b), (c), 0, 0, 0)
#define BAR() do { asm volatile("" ::: "memory"); __builtin_amdgcn_s_waitcnt(0xC07F); __builtin_amdgcn_s_barrier(); asm volatile("" ::: "memory"); } while (0)
#define WAITV(n) asm volatile("s_waitcnt vmcnt(%0)" ::"i"(n) : "memory")
#define SBAR() __builtin_amdgcn_sched_barrier(0)
#define MAX3(a, b, c) fmaxf(fmaxf((a), (b)), (c))
#define LANEID(v) asm volatile("v_mbcnt_lo_u32_b32 %0, -1, 0\n\tv_mbcnt_hi_u32_b32 %0, -1, %0" : "=v"(v))

DI unsigned cvtpk(float lo, float hi) {
  f32x2 v = {lo, hi};
  bf16x2 b = __builtin_convertvector(v, bf16x2);
  return __builtin_bit_cast(unsigned, b);
}
DI void dma16nt(unsigned lds_addr, const void* sbase, unsigned voff) {
  asm volatile("s_mov_b32 m0, %0\n\ts_nop 0\n\tglobal_load_lds_dwordx4 %1, %2 nt" ::"s"(lds_addr), "v"(voff), "s"(sbase) : "memory");
}
DI void bdma(unsigned lds_addr, __amdgpu_buffer_rsrc_t rs, unsigned voff, unsigned soff) {
  asm volatile("s_mov_b32 m0, %0\n\ts_nop 0\n\tbuffer_load_dwordx4 %1, %2, %3 offen lds" ::"s"(lds_addr), "v"(voff), "s"(rs), "s"(soff) : "memory");
}
DI void bdma1(unsigned lds_addr, __amdgpu_buffer_rsrc_t rs, unsigned voff, unsigned soff) {
  asm volatile("s_mov_b32 m0, %0\n\ts_nop 0\n\tbuffer_load_dwordx4 %1, %2, %3 offen sc1 lds" ::"s"(lds_addr), "v"(voff), "s"(rs), "s"(soff) : "memory");
}
DI void dma16(unsigned lds_addr, const void* sbase, unsigned voff) {
  asm volatile("s_mov_b32 m0, %0\n\ts_nop 0\n\tglobal_load_lds_dwordx4 %1, %2" ::"s"(lds_addr), "v"(voff), "s"(sbase) : "memory");
}
template <int I, int N, class F>
DI void sfor(F&& f) {
  if constexpr (I < N) { f(std::integral_constant<int, I>{}); sfor<I + 1, N>(f); }
}
DI bf16x8 lds128(unsigned a) { return *(const LAS bf16x8*)(size_t)a; }
DI s16x4 ldstr(unsigned a) { return __builtin_amdgcn_ds_read_tr16_b64_v4i16((LAS s16x4*)(size_t)a); }
DI bf16x8 cat(s16x4 a, s16x4 b) { return __builtin_shufflevector(a, b, 0, 1, 2, 3, 4, 5, 6, 7); }

constexpr unsigned ITB = 12288;
constexpr unsigned NSL = 5;
constexpr unsigned GRB = NSL * ITB;
constexpr unsigned SIDE = 2 * GRB;
constexpr unsigned MISC = SIDE + 36864;
constexpr unsigned CTL = MISC + 2048;
constexpr unsigned HB = MISC - 49152;
constexpr unsigned C_L = CTL;
constexpr unsigned C_R = CTL + 16;
constexpr unsigned C_QRDY = CTL + 48;
constexpr unsigned C_XF = CTL + 64;
constexpr unsigned C_H = CTL + 128;
constexpr unsigned C_OR = CTL + 160;
constexpr unsigned C_OD = CTL + 192;
constexpr unsigned LDS_A = 163840;
constexpr float CSC = 0.07216878364870322f * 1.4426950408889634f;
constexpr float THR_RAW = 8.0f / CSC;
constexpr int SPIN_MAX = 1 << 20;
#define HD_D 4
#define TL_D 4
#define KV0 2

#define STAMP(i)
#define EST(i)
#define FST(i)

DI void lds_post(unsigned a, unsigned v) {
  asm volatile("s_waitcnt lgkmcnt(0)\n\tds_write_b32 %0, %1" ::"v"(a), "v"(v) : "memory");
}
DI void lds_post_nw(unsigned a, unsigned v) {
  asm volatile("ds_write_b32 %0, %1" ::"v"(a), "v"(v) : "memory");
}
DI void lds_wait_ge(unsigned a, unsigned n) {
#pragma nounroll
  for (int it = 0; it < SPIN_MAX; ++it) {
    unsigned v;
    asm volatile("ds_read_b32 %0, %1\n\ts_waitcnt lgkmcnt(0)" : "=v"(v) : "v"(a) : "memory");
    if (__builtin_amdgcn_readfirstlane(v) >= n) break;
    __builtin_amdgcn_s_sleep(1);
  }
}
DI void lds_wait_min4(unsigned a, unsigned n) {
#pragma nounroll
  for (int it = 0; it < SPIN_MAX; ++it) {
    u32x4 d;
    asm volatile("ds_read_b128 %0, %1\n\ts_waitcnt lgkmcnt(0)" : "=v"(d) : "v"(a) : "memory");
    const unsigned mn = min(min(d[0], d[1]), min(d[2], d[3]));
    if (__builtin_amdgcn_readfirstlane(mn) >= n) break;
    __builtin_amdgcn_s_sleep(1);
  }
}
DI unsigned lds_rd1(unsigned a) {
  unsigned v;
  asm volatile("ds_read_b32 %0, %1\n\ts_waitcnt lgkmcnt(0)" : "=v"(v) : "v"(a) : "memory");
  return __builtin_amdgcn_readfirstlane(v);
}
DI unsigned lds_min3(unsigned a) {
  u32x4 d;
  asm volatile("ds_read_b128 %0, %1\n\ts_waitcnt lgkmcnt(0)" : "=v"(d) : "v"(a) : "memory");
  return __builtin_amdgcn_readfirstlane(min(min(d[0], d[1]), d[2]));
}
DI unsigned getvm() {
  unsigned sts;
  asm volatile("s_getreg_b32 %0, hwreg(HW_REG_IB_STS)" : "=s"(sts)::"memory");
  return (sts & 15u) | ((sts >> 18) & 0x30u);
}
DI unsigned lds_peek(unsigned a) { return *(volatile LAS unsigned*)(size_t)a; }

typedef __attribute__((address_space(1))) unsigned long long gu64;
extern "C" __device__ unsigned long long amd_dispatch_id() __asm("llvm.amdgcn.dispatch.id");
constexpr size_t WS_O = 0, WS_P = 32u << 20, WS_F = 96u << 20;

DI bool poll_flags(gu64* f, int nl, int skip, unsigned long long T, int lane) {
#pragma nounroll
  for (int it = 0; it < SPIN_MAX; ++it) {
    unsigned long long v = T;
    if (lane < nl && lane != skip) v = __hip_atomic_load(f + lane, __ATOMIC_RELAXED, __HIP_MEMORY_SCOPE_AGENT);
    if (__all(v == T)) {
      if (lane < nl && lane != skip) __hip_atomic_store(f + lane, 0ull, __ATOMIC_RELAXED, __HIP_MEMORY_SCOPE_AGENT);
      return true;
    }
    __builtin_amdgcn_s_sleep(2);
  }
  return false;
}

constexpr int hNL(int j) { return j < 20 ? 3 : 0; }
constexpr int hKV(int p) { return (p >= KV0 && p < KV0 + 12) ? 1 : 0; }
constexpr int hPend(int j) {
  int cnt = 0, first = 0;
  if (j < HD_D) { cnt = 3 * (HD_D - 1 - j); first = 0; }
  else { cnt = hKV(j - HD_D); first = j - HD_D + 1; }
  for (int p = first; p < j; ++p) cnt += hNL(p + HD_D) + hKV(p);
  return cnt;
}
constexpr int tNL(int j) { return j < 24 ? 3 : 0; }
constexpr int tPend(int j) {
  int cnt = 0, first = 0;
  if (j < TL_D) { cnt = 3 * (TL_D - 1 - j); first = 0; }
  else { cnt = 0; first = j - TL_D + 1; }
  for (int p = first; p < j; ++p) cnt += tNL(p + TL_D);
  return cnt;
}

__global__ __launch_bounds__(512, 2) void attn_kernel(const u16* x, const u16* kc, const u16* vc, const u16* wq, const u16* wo,
                                                      u16* out, char* ws, unsigned long long* stamps) {
  extern __shared__ __attribute__((aligned(16))) char lds[];
  const unsigned L0 = (unsigned)(size_t)(LAS char*)lds;
  const int tid = threadIdx.x, lane = tid & 63;
  const int wave = __builtin_amdgcn_readfirstlane(tid >> 6);
  const int xj = blockIdx.x & 7, xi = blockIdx.x >> 3;
  const int hq = xi & 3, h = 4 * xj + hq, b0 = xi >> 2;
  const size_t UB0 = (size_t)(b0 * NH + h) * SK * 384, UB1 = (size_t)((b0 + 8) * NH + h) * SK * 384;
  unsigned long long TAG;
  {
    const unsigned long long did = amd_dispatch_id(), dp = (unsigned long long)__builtin_amdgcn_dispatch_ptr();
    const unsigned t32 = (unsigned)(did * 0x9E3779B1ull) ^ (unsigned)(dp >> 6) * 0x85EBCA77u ^ (unsigned)(did >> 32);
    TAG = ((unsigned long long)(t32 ^ 0x5A17C3E9u) << 32) | t32;
  }
  const int qbase = xj + 32 * b0;
  gu64* FL = (gu64*)(ws + WS_F);
  const int kh = (0x1A >> wave) & 1, r = (0x102210 >> (4 * wave)) & 3;
  STAMP(0);
  bf16x8 qf[12];

  unsigned offK[6], offV[3];
  {
    if (wave == 6 && lane < 64) *(LAS unsigned*)(size_t)(L0 + CTL + lane * 4) = (lane == 46 || lane == 47) ? 1u : 0u;
    const int rot = (b0 * 5 / 4 + hq * 5 / 2) % 10;
    const int dh = wave & 1, rq = wave >> 1;
    const unsigned g = lane >> 4, q4 = (lane & 15) >> 2, p4 = lane & 3, l15 = lane & 15;
    const __amdgpu_buffer_rsrc_t rsW = __builtin_amdgcn_make_buffer_rsrc((void*)((const char*)wq + h * DH * 2), (short)0, DM * HD * 2 - h * DH * 2, 0x00020000);
    const __amdgpu_buffer_rsrc_t rsX = __builtin_amdgcn_make_buffer_rsrc((void*)((const char*)x + b0 * SQ * DM * 2), (short)0, (NB - b0) * SQ * DM * 2, 0x00020000);
    unsigned gA, gB, gC;
    {
      auto wpiece = [&](int p) {
        const int n = 64 * p + lane, row = n / 24, pc = n % 24;
        const int sw = ((row >> 1) & 1) | (((row >> 3) & 1) << 1);
        return (unsigned)(row * (HD * 2) + ((pc ^ (sw << 1)) * 16));
      };
      auto xpiece = [&](int p) {
        const int row = 8 * p + (lane >> 3);
        return (unsigned)(row * (DM * 2) + (((lane & 7) ^ ((row >> 1) & 7)) * 16));
      };
      gA = wpiece(wave);
      gB = wave < 4 ? wpiece(8 + wave) : xpiece(wave - 4);
      gC = xpiece(4 + wave);
    }
    unsigned hK[3], hV[3];
    constexpr unsigned WB = 3 * ITB, G1X = GRB + 2 * ITB, U1 = 8 * SQ * DM * 2;
    constexpr unsigned WSTEP = 32 * HD * 2;
    const unsigned mW = L0 + WB + wave * 1024;
    const unsigned mX = L0 + (4 + wave) * 1024;
    const unsigned s2 = ((q4 >> 1) & 1) | ((g & 1) << 1);
    unsigned E[4];
    const unsigned XA0 = L0 + (rq >> 1) * G1X + (48 * (rq & 1) + l15) * 128 + ((g ^ ((l15 >> 1) & 7)) * 16), XA1 = XA0 ^ 64;
    f32x4 acc[6][3] = {};
    bf16x8 af[2][6], bfr[2][3];
    unsigned xa;
    auto rdA = [&](int par, int c1, int db) {
      const unsigned a = E[db & 3] + (db >> 2) * 128 + (c1 & 3) * ITB;
      af[par][db] = cat(ldstr(a), ldstr(a + 4 * 384));
    };
    auto rdB = [&](int par, int rb) { bfr[par][rb] = lds128(xa + rb * 2048); };
    unsigned woff = (unsigned)(2 * rot) * WSTEP, xoff = (unsigned)rot * 128;
    auto advW = [&]() { woff += WSTEP; woff = woff == 20 * WSTEP ? 0u : woff; };
    auto advX = [&]() { xoff += 128; xoff = xoff == 1280 ? 0u : xoff; };
    auto request = [&](auto VV, int k, int wsl, unsigned xs, int un) {
      constexpr int V = decltype(VV)::value;
      if (k == 0) bdma(mW + wsl * ITB, rsW, gA, woff);
      if (k == 1) {
        if (V == 0) bdma(mW + 8192 + wsl * ITB, rsW, gB, woff);
        else bdma(mX - 8192 + un * G1X + xs, rsX, gB, xoff + un * U1);
      }
      if (k == 2) bdma(mX + un * G1X + xs, rsX, gC, xoff + un * U1);
    };
    unsigned xr = 2 * ITB;
    unsigned xw = 1 * ITB;
    auto body = [&](auto VV) {
      constexpr int V = decltype(VV)::value;
      if (V == 1) bdma(mX - 8192 + 2 * ITB, rsX, gB, xoff);
      bdma(mX + 2 * ITB, rsX, gC, xoff);
#pragma unroll
      for (int c = -4; c < 0; ++c) {
        if ((c & 1) != 0) advX();
        const unsigned xs = (unsigned)((((c + 1) >> 1) + 2 + 2) % 3) * ITB;
#pragma unroll
        for (int k = 0; k < 3; ++k) request(VV, k, c & 3, xs, (c + 1) & 1);
        advW();
      }
      SBAR();
      asm volatile("" ::: "memory");
#pragma unroll
    for (int j = 0; j < 6; ++j) {
      int n = 64 * j + lane, row = n / 24, pc = n % 24;
      offK[j] = row * 384 + ((pc ^ ((row >> 1) & 7)) * 16);
    }
#pragma unroll
    for (int j = 0; j < 3; ++j) {
      int n = 64 * j + lane, row = n / 24, pc = n % 24;
      offV[j] = row * 384 + ((pc ^ (((row >> 1) & 1) << 2)) * 16);
    }
#pragma unroll
    for (int j = 0; j < 3; ++j) {
      const int n = 64 * (3 * (wave & 3) + j) + lane, row = n / 24, pc = n % 24;
      hK[j] = row * 384 + ((pc ^ ((row >> 1) & 7)) * 16);
      hV[j] = row * 384 + ((pc ^ (((row >> 1) & 1) << 2)) * 16);
    }
#pragma unroll
      for (int j = 0; j < 4; ++j) E[j] = L0 + WB + (8 * g + q4) * 384 + 32 * ((unsigned)(6 * dh + j) ^ s2) + 8 * p4;
      WAITV(9);
      BAR();
      xa = XA0 + 2 * ITB;
#pragma unroll
      for (int rb = 0; rb < 3; ++rb) rdB(0, rb);
#pragma unroll
      for (int db = 0; db < 6; ++db) rdA(0, 0, db);
      WAITV(6);
      BAR();
      STAMP(4);
      auto phase = [&](int c, auto QQ, auto MODE) {
        constexpr int q = decltype(QQ)::value, mode = decltype(MODE)::value, cur = q & 1;
        if (cur == 1) {
          xr = xr == 2 * ITB ? 0 : xr + ITB;
          if (mode == 0) { xw = xw == 2 * ITB ? 0 : xw + ITB; advX(); }
        }
        xa = (cur == 1 ? XA0 : XA1) + xr;
        constexpr int R0 = V == 0 ? 0 : 5, D0 = V == 0 ? 10 : 0;
#pragma unroll
        for (int i = 0; i < 18; ++i) {
          acc[i / 3][i % 3] = MFMA16(af[cur][i / 3], bfr[cur][i % 3], acc[i / 3][i % 3]);
          SBAR();
          if (i >= R0 && i < R0 + 3) rdB(cur ^ 1, i - R0);
          if (i >= R0 + 3 && i < R0 + 9) rdA(cur ^ 1, q + 1, i - R0 - 3);
          if (mode == 0 && i >= D0 && i < D0 + 6 && !((i - D0) & 1)) request(VV, (i - D0) >> 1, q, xw, (q + 1) & 1);
          if (mode == 0 && i == D0 + 5) advW();
          if (mode == 2 && i >= D0 && i < D0 + 6 && !((i - D0) & 1)) {
            const int k = (i - D0) >> 1, li = c - 17;
            const unsigned dst = L0 + V * G1X + li * ITB + (3 * (wave & 3) + k) * 1024;
            if (li == 2) dma16nt(dst, (const char*)vc + (V ? UB1 : UB0), hV[k]);
            else dma16nt(dst, (const char*)kc + (V ? UB1 : UB0) + (size_t)li * ITB, hK[k]);
          }
          SBAR();
        }
        if (mode == 0) WAITV(6);
        if (mode == 1) WAITV(0);
        BAR();
      };
      using I0 = std::integral_constant<int, 0>; using I1 = std::integral_constant<int, 1>;
      using I2 = std::integral_constant<int, 2>; using I3 = std::integral_constant<int, 3>;
#pragma unroll 1
      for (int c = 0; c < 16; c += 4) {
        phase(c, I0{}, I0{});
        phase(c + 1, I1{}, I0{});
        phase(c + 2, I2{}, I0{});
        phase(c + 3, I3{}, I0{});
      }
      STAMP(5);
      phase(16, I0{}, I1{});
      phase(17, I1{}, I2{});
      phase(18, I2{}, I2{});
      phase(19, I3{}, I2{});
    };
    if (wave < 4) body(std::integral_constant<int, 0>{}); else body(std::integral_constant<int, 1>{});
    STAMP(11);
    {
      unsigned lz; LANEID(lz);
      const unsigned zg = lz >> 4, zl = lz & 15;
      const unsigned qimg = L0 + (wave < 4 ? SIDE : WB);
#pragma unroll
      for (int rb = 0; rb < 3; ++rb) {
        const int R16 = 3 * (rq & 1) + rb;
        const unsigned qb = qimg + ((R16 >> 1) * 12 + 6 * dh) * 1024 + (16 * (R16 & 1) + zl + 32 * (zg >> 1)) * 16 + 8 * (zg & 1);
#pragma unroll
        for (int db = 0; db < 6; ++db) {
          u32x2 w = {cvtpk(acc[db][rb][0], acc[db][rb][1]), cvtpk(acc[db][rb][2], acc[db][rb][3])};
          *(LAS u32x2*)(size_t)(qb + db * 1024) = w;
        }
      }
      BAR();
      if (wave < 6) {
#pragma unroll
        for (int ks = 0; ks < 12; ++ks) qf[ks] = lds128(L0 + (kh ? WB : SIDE) + ((r * 12 + ks) * 64 + lz) * 16);
      }
      BAR();
    }
  }
  STAMP(1);

  if (wave >= 6) {
    const int hw = wave - 6;
    __builtin_amdgcn_s_setprio(3);
    unsigned ll; LANEID(ll);
    const unsigned aL = L0 + C_L + 4 * hw, aR = L0 + C_R + 16 * hw;
    auto issue_item = [&](unsigned li_, unsigned slot_) {
      const bool isK = (li_ == 0) || ((li_ & 1) && li_ < 63);
      const unsigned tile = isK ? (li_ + 1) >> 1 : (li_ - 1) >> 1;
      const size_t ub = (hw ? UB1 : UB0) + (size_t)tile * ITB;
      const unsigned dst = L0 + hw * GRB + slot_;
      if (isK) {
        const char* src = (const char*)kc + ub;
#pragma unroll
        for (int q = 0; q < 12; ++q) dma16nt(dst + q * 1024, src + (q / 6) * 6144, offK[q % 6]);
      } else {
        const char* src = (const char*)vc + ub;
#pragma unroll
        for (int q = 0; q < 12; ++q) dma16nt(dst + q * 1024, src + (q / 3) * 3072, offV[q % 3]);
      }
    };
    issue_item(3, hw ? 0 : 3 * ITB);
    issue_item(4, hw ? ITB : 4 * ITB);
    WAITV(30); lds_post_nw(L0 + C_H + 4 * wave, 1); lds_wait_min4(L0 + C_H + 16 * hw, 1); lds_post_nw(aL, 1);
    WAITV(27); lds_post_nw(L0 + C_H + 4 * wave, 2); lds_wait_min4(L0 + C_H + 16 * hw, 2); lds_post_nw(aL, 2);
    WAITV(24); lds_post_nw(L0 + C_H + 4 * wave, 3); lds_wait_min4(L0 + C_H + 16 * hw, 3); lds_post_nw(aL, 3);
    unsigned li = 5, lpub = 3, slot = hw ? 2 * ITB : 0;
    {
      const char* wsrc = (const char*)wo + (size_t)(4 * xj * DH + 96 * b0 + 48 * hw) * (DM * 2) + hq * 320;
      const unsigned o0 = (ll >> 2) * (DM * 2) + min((ll & 3) * 128u, 316u);
#pragma unroll
      for (int z = 0; z < 3; ++z)
        asm volatile("s_mov_b32 m0, %0\n\ts_nop 0\n\tglobal_load_lds_dword %1, %2" ::"s"(L0 + MISC + 1536), "v"(o0 + z * 16 * DM * 2), "s"(wsrc) : "memory");
      const char* t0 = ll == 0 ? ws + WS_O + (size_t)blockIdx.x * 36864 : ll == 1 ? ws + WS_O + (size_t)(256 + blockIdx.x) * 36864
                     : ll == 2 ? ws + WS_P + (size_t)(((b0 * 4 + hq) * 8 + xj) * 30720) : ll == 3 ? ws + WS_P + (size_t)((((8 + b0) * 4 + hq) * 8 + xj) * 30720)
                     : ll == 4 ? ws + WS_F + (size_t)blockIdx.x * 256 : (const char*)out + (size_t)((b0 + 8 * (ll & 1)) * SQ + 12 * xj) * DM * 2;
      if (ll < 7)
        asm volatile("s_mov_b32 m0, %0\n\ts_nop 0\n\tglobal_load_lds_dword %1, off" ::"s"(L0 + MISC + 1536), "v"(t0) : "memory");
    }
    unsigned tix = 0, xt = 0;
    const __amdgpu_buffer_rsrc_t rsWo = __builtin_amdgcn_make_buffer_rsrc((void*)((const char*)wo + 4 * xj * DH * DM * 2 + hq * 320), (short)0, HD * DM * 2 - (4 * xj * DH * DM * 2 + hq * 320), 0x00020000);
    unsigned go[5];
#pragma unroll
    for (int p = 0; p < 5; ++p) {
      const int n = 64 * p + ll, row = n / 20, pc = n % 20;
      go[p] = row * (DM * 2) + ((pc ^ (((row >> 3) & 1) << 1)) * 16);
    }
#pragma nounroll
    for (int it = 0; it < (SPIN_MAX << 2); ++it) {
      bool idle = true;
      if (li < 64) {
        if (lds_min3(aR) + NSL > li && getvm() <= 48) {
          issue_item(li, slot);
          ++li;
          slot = slot + ITB == GRB ? 0 : slot + ITB;
          idle = false;
        }
      } else if (tix < 5) {
        if (lds_min3(L0 + C_R) >= 60 + tix && getvm() <= 50) {
          const unsigned c0 = tix == 0 ? 5 : tix - 1, nc = tix == 4 ? 2 : 1;
#pragma unroll 1
          for (unsigned c6 = c0; c6 < c0 + nc; ++c6) {
#pragma unroll
            for (int p = 0; p < 5; ++p) bdma(L0 + c6 * 10240 + (5 * hw + p) * 1024, rsWo, go[p], (hq * DH + 32 * c6 + 16 * hw) * (DM * 2));
            xt += 5;
          }
          ++tix;
          idle = false;
        }
      }
      if (lpub < li) {
        const unsigned vm = getvm();
        unsigned done = lpub;
#pragma nounroll
        while (done < li) {
          const unsigned after = 12 * (li - 1 - done) + xt + (done < 5 ? 4 : 0);
          if (vm > after) break;
          ++done;
        }
        if (done > lpub) {
          lpub = done;
          lds_post_nw(aL, lpub);
          idle = false;
        }
      } else if (li == 64 && tix == 5) {
        break;
      }
      if (idle) __builtin_amdgcn_s_sleep(1);
    }
    __builtin_amdgcn_s_setprio(0);
  } else {
    unsigned ln; LANEID(ln);
    unsigned kof[4], vE, vO;
    {
      const unsigned a32 = ln & 31, ahi = ln >> 5, ablk = (ln >> 4) & 1, aq4 = (ln & 15) >> 2, ap4 = ln & 3, axq = (aq4 >> 1) & 1;
#pragma unroll
      for (int i = 0; i < 4; ++i) kof[i] = L0 + kh * GRB + a32 * 384 + (((2 * i + ahi) ^ ((a32 >> 1) & 7)) * 16);
      vE = L0 + kh * GRB + (4 * ahi + aq4) * 384 + 64 * axq + 32 * ablk + 8 * ap4;
      vO = L0 + kh * GRB + (4 * ahi + aq4) * 384 - 64 * axq + 32 * ablk + 8 * ap4;
    }
    const unsigned aF = L0 + C_L + 4 * kh, aR = L0 + C_R + 16 * kh + 4 * r;
    const int pw = (0x102354 >> (4 * wave)) & 7;
    const unsigned fme = L0 + C_XF + 8 * wave, fpa = L0 + C_XF + 8 * pw;
    unsigned cur = kh ? 2 * ITB : 0;
    unsigned cnt = 0;
#define ADV() do { cur = (cur + ITB == GRB) ? 0u : cur + ITB; ++cnt; } while (0)
#define KADDR(ks) (((ks) & 3) == 0 ? k0 : ((ks) & 3) == 1 ? k1 : ((ks) & 3) == 2 ? k2 : k3) + 128 * ((ks) >> 2)
#define VADDR(j) ((((j) % 6) & 1) ? vbO : vbE) + (16 * ((j) / 6)) * 384 + 64 * ((j) % 6)

    {
      float m, l = 0.f;
      f32x16 o[6] = {};
      f32x16 sB;
      float pe[16];
      bf16x8 kf[12], vf[12];
      unsigned k0, k1, k2, k3, vbE, vbO;
      WAITV(6); lds_post_nw(L0 + C_H + 4 * wave, 1);
      lds_wait_ge(aF, cnt + 1);
      k0 = kof[0] + cur; k1 = kof[1] + cur; k2 = kof[2] + cur; k3 = kof[3] + cur;
#pragma unroll
      for (int ks = 0; ks < 12; ++ks) kf[ks] = lds128(KADDR(ks));
      ADV();
      lds_post_nw(aR, cnt);
      SBAR();
      {
        f32x16 z = {};
        sB = MFMA32(kf[0], qf[0], z);
#pragma unroll
        for (int ks = 1; ks < 12; ++ks) sB = MFMA32(kf[ks], qf[ks], sB);
      }
      SBAR();
      WAITV(3); lds_post_nw(L0 + C_H + 4 * wave, 2);
      lds_wait_ge(aF, cnt + 1);
      WAITV(0); lds_post_nw(L0 + C_H + 4 * wave, 3);
      k0 = kof[0] + cur; k1 = kof[1] + cur; k2 = kof[2] + cur; k3 = kof[3] + cur;
#pragma unroll
      for (int ks = 0; ks < 4; ++ks) kf[ks] = lds128(KADDR(ks));
      {
        float pmax = sB[0];
#pragma unroll
        for (int i = 1; i < 16; ++i) pmax = fmaxf(pmax, sB[i]);
        auto rr = __builtin_amdgcn_permlane32_swap(__float_as_uint(pmax), __float_as_uint(pmax), false, false);
        m = fmaxf(__uint_as_float(rr[0]), __uint_as_float(rr[1]));
        const float mc = -m * CSC;
#pragma unroll
        for (int i = 0; i < 16; ++i) pe[i] = fmaf(sB[i], CSC, mc);
      }
      SBAR();
      bf16x8 pa[2];
      auto expstep = [&](int i) {
        pe[2 * i] = __builtin_amdgcn_exp2f(pe[2 * i]);
        pe[2 * i + 1] = __builtin_amdgcn_exp2f(pe[2 * i + 1]);
        l += pe[2 * i];
        l += pe[2 * i + 1];
        asm volatile("" : "+v"(pe[2 * i]), "+v"(pe[2 * i + 1]), "+v"(l));
      };
      auto cvtstep = [&](int s) {
        u32x4 w = {cvtpk(pe[8 * s], pe[8 * s + 1]), cvtpk(pe[8 * s + 2], pe[8 * s + 3]), cvtpk(pe[8 * s + 4], pe[8 * s + 5]),
                   cvtpk(pe[8 * s + 6], pe[8 * s + 7])};
        asm volatile("" : "+v"(w));
        pa[s] = __builtin_bit_cast(bf16x8, w);
      };
#pragma unroll 1
      for (int t = 0; t < 31; ++t) {
        unsigned fl = 0;
#pragma unroll
        for (int i = 0; i < 12; ++i) {
          if (i == 0) { f32x16 z = {}; sB = MFMA32(kf[0], qf[0], z); }
          else sB = MFMA32(kf[i], qf[i], sB);
          if (i < 8) kf[i + 4] = lds128(KADDR(i + 4));
          if (i == 2) fl = lds_peek(aF);
          if (i == 7) { ADV(); lds_post_nw(aR, cnt); }
          if (i < 8) expstep(i);
          if (i == 8) {
            if (__builtin_amdgcn_readfirstlane(fl) < cnt + 1) lds_wait_ge(aF, cnt + 1);
            vbE = vE + cur; vbO = vO + cur;
            cvtstep(0);
          }
          if (i == 9) cvtstep(1);
          if (i >= 8) vf[i - 8] = cat(ldstr(VADDR(i - 8)), ldstr(VADDR(i - 8) + 8 * 384));
          SBAR();
        }
        float pmax = 0.f, alpha = 1.f, mc = 0.f;
        bool resc = false;
#pragma unroll
        for (int j = 0; j < 12; ++j) {
          o[j % 6] = MFMA32(vf[j], pa[j / 6], o[j % 6]);
          if (j < 8) vf[j + 4] = cat(ldstr(VADDR(j + 4)), ldstr(VADDR(j + 4) + 8 * 384));
          if (j == 2) fl = lds_peek(aF);
          if (j == 7) { ADV(); lds_post_nw(aR, cnt); }
          if (j == 1) {
            asm volatile("" : "+v"(sB));
            pmax = MAX3(MAX3(sB[0], sB[1], sB[2]), MAX3(sB[3], sB[4], sB[5]), MAX3(sB[6], sB[7], sB[8]));
            asm volatile("" : "+v"(pmax));
          }
          if (j == 2) {
            pmax = MAX3(MAX3(sB[9], sB[10], sB[11]), MAX3(sB[12], sB[13], sB[14]), MAX3(sB[15], sB[15], pmax));
            asm volatile("" : "+v"(pmax));
          }
          if (j == 3) {
            auto rr = __builtin_amdgcn_permlane32_swap(__float_as_uint(pmax), __float_as_uint(pmax), false, false);
            pmax = fmaxf(__uint_as_float(rr[0]), __uint_as_float(rr[1]));
            resc = pmax - m > THR_RAW;
            const float mn = resc ? pmax : m;
            alpha = __builtin_amdgcn_exp2f((m - mn) * CSC);
            m = mn;
            mc = -mn * CSC;
            l *= alpha;
            asm volatile("" : "+v"(m), "+v"(mc), "+v"(l), "+v"(alpha));
          }
          if (j >= 4 && j < 8) {
#pragma unroll
            for (int i = 4 * (j - 4); i < 4 * (j - 4) + 4; ++i) pe[i] = fmaf(sB[i], CSC, mc);
            asm volatile("" : "+v"(pe[4 * (j - 4)]), "+v"(pe[4 * (j - 4) + 1]), "+v"(pe[4 * (j - 4) + 2]), "+v"(pe[4 * (j - 4) + 3]));
          }
          if (j == 8) {
            if (t < 30 && __builtin_amdgcn_readfirstlane(fl) < cnt + 1) lds_wait_ge(aF, cnt + 1);
            k0 = kof[0] + cur; k1 = kof[1] + cur; k2 = kof[2] + cur; k3 = kof[3] + cur;
          }
          if (j >= 8) kf[j - 8] = lds128(KADDR(j - 8));
          SBAR();
        }
        if (__any(resc)) {
#pragma unroll
          for (int d = 0; d < 6; ++d)
#pragma unroll
            for (int i = 0; i < 16; ++i) o[d][i] *= alpha;
        }
      }
      {
        lds_wait_ge(aF, cnt + 1);
        vbE = vE + cur; vbO = vO + cur;
#pragma unroll
        for (int j = 0; j < 6; ++j) vf[j] = cat(ldstr(VADDR(j)), ldstr(VADDR(j) + 8 * 384));
        SBAR();
#pragma unroll
        for (int i = 0; i < 8; ++i) expstep(i);
        cvtstep(0);
        cvtstep(1);
        SBAR();
#pragma unroll
        for (int j = 0; j < 12; ++j) {
          o[j % 6] = MFMA32(vf[j], pa[j / 6], o[j % 6]);
          if (j < 6) vf[j + 6] = cat(ldstr(VADDR(j + 6)), ldstr(VADDR(j + 6) + 8 * 384));
          if (j == 5) { ADV(); lds_post_nw(aR, cnt); }
          SBAR();
        }
      }
      STAMP(6);
      {
        auto rr = __builtin_amdgcn_permlane32_swap(__float_as_uint(l), __float_as_uint(l), false, false);
        l = __uint_as_float(rr[0]) + __uint_as_float(rr[1]);
      }
      const float inv = 1.0f / l;
      unsigned lz; LANEID(lz);
      const int zc = lz & 31, zh = lz >> 5;
      const __amdgpu_buffer_rsrc_t rsO = __builtin_amdgcn_make_buffer_rsrc(ws + WS_O, (short)0, 2 * 256 * 36864, 0x00020000);
      const int osw = ((zc >> 2) & 1) << 1;
      const int obase = (kh * 256 + blockIdx.x) * 36864 + (32 * r + zc) * 64;
      u32x4 wq_[6][2];
#pragma unroll
      for (int d = 0; d < 6; ++d)
#pragma unroll
        for (int i = 0; i < 4; i += 2) {
          unsigned pk[4];
#pragma unroll
          for (int j = 0; j < 2; ++j) {
            pk[2 * j] = cvtpk(o[d][4 * (i + j)] * inv, o[d][4 * (i + j) + 1] * inv);
            pk[2 * j + 1] = cvtpk(o[d][4 * (i + j) + 2] * inv, o[d][4 * (i + j) + 3] * inv);
          }
          auto r0 = __builtin_amdgcn_permlane32_swap(pk[0], pk[2], false, false);
          auto r1 = __builtin_amdgcn_permlane32_swap(pk[1], pk[3], false, false);
          wq_[d][i >> 1] = u32x4{r0[0], r1[0], r0[1], r1[1]};
          __builtin_amdgcn_raw_buffer_store_b128(wq_[d][i >> 1], rsO, obase + d * 6144 + (((i + zh) ^ osw) * 16), 0, 16);
        }
#pragma nounroll
      for (int it = 0; it < SPIN_MAX; ++it) {
        if (lds_min3(L0 + C_R + 16) >= 64) break;
        __builtin_amdgcn_s_sleep(1);
      }
#pragma unroll
      for (int d = 0; d < 6; ++d)
#pragma unroll
        for (int i = 0; i < 2; ++i)
          *(LAS u32x4*)(size_t)(L0 + 61440 + (d < 5 ? (d + 1) % 5 : 5) * ITB + kh * 6144 + r * 2048 + zc * 64 + (((2 * i + zh) ^ osw) * 16)) = wq_[d][i];
      STAMP(7);
    }
  }
  {
    unsigned ln; LANEID(ln);
    constexpr unsigned TWO = 10240, TOB = 61440;
    const int ch = wave & 1, rq = wave >> 1;
    const unsigned g = ln >> 4, q4 = (ln & 15) >> 2, p4 = ln & 3, l15 = ln & 15;
    unsigned gA, gB;
    {
      auto wpiece = [&](int p) {
        const int n = 64 * p + ln, row = n / 20, pc = n % 20;
        return (unsigned)(row * (DM * 2) + ((pc ^ (((row >> 3) & 1) << 1)) * 16));
      };
      gA = wpiece(wave);
      gB = wave < 2 ? wpiece(8 + wave) : ln * 16 + (wave - 2) * 1024;
    }
    const int pc3 = wave < 6 ? wave : wave - 2;
    const unsigned gC = ln * 16 + pc3 * 1024 + 256 * 36864;
    const __amdgpu_buffer_rsrc_t rsWo = __builtin_amdgcn_make_buffer_rsrc((void*)((const char*)wo + 4 * xj * DH * DM * 2 + hq * 320), (short)0, HD * DM * 2 - (4 * xj * DH * DM * 2 + hq * 320), 0x00020000);
    const __amdgpu_buffer_rsrc_t rsOt = __builtin_amdgcn_make_buffer_rsrc(ws + WS_O + (size_t)qbase * 36864, (short)0, (512 - qbase) * 36864, 0x00020000);
    const __amdgpu_buffer_rsrc_t rsB = wave < 2 ? rsWo : rsOt;
    const unsigned mA = L0 + wave * 1024, mC = L0 + TOB + 6144 + pc3 * 1024;
    const unsigned mB = wave < 2 ? L0 + (8 + wave) * 1024 : L0 + TOB + (wave - 2) * 1024;
    const unsigned stepB = wave < 2 ? 32 * DM * 2 : 6144;
    unsigned wsrc, osrc, bsrc;
    auto sethead = [&](int hs) { wsrc = hs * (DH * DM * 2); osrc = hs * (8 * 36864); bsrc = wave < 2 ? wsrc : osrc; };
    auto request = [&](int c6, int k) {
      if (k == 0) bdma(mA + c6 * TWO, rsWo, gA, wsrc + c6 * (32 * DM * 2));
      const unsigned os = (c6 < 5 ? (c6 + 1) % 5 : 5) * ITB;
      if (k == 1) bdma1(mB + (wave < 2 ? c6 * TWO : os), rsB, gB, bsrc + c6 * stepB);
      if (k == 2) bdma1(mC + os, rsOt, gC, osrc + c6 * 6144);
    };
    const unsigned WAb = L0 + (8 * g + q4) * 320 + 160 * ch + 8 * p4;
    const unsigned WA0 = WAb + ((ch & 1) ? -32 * (int)(g & 1) : 32 * (int)(g & 1));
    const unsigned WA1 = WAb + ((ch & 1) ? 32 * (int)(g & 1) : -32 * (int)(g & 1));
    const unsigned OA = L0 + TOB + (48 * rq + l15) * 64 + ((g ^ (((l15 >> 2) & 1) << 1)) * 16);
    f32x4 acc[3][5] = {};
    bf16x8 af[2][5], bfr[2][3];
    auto rdA = [&](int par, int sl, int cb) {
      const unsigned a = ((cb & 1) ? WA1 : WA0) + sl * TWO + cb * 32;
      af[par][cb] = cat(ldstr(a), ldstr(a + 4 * 320));
    };
    auto rdB = [&](int par, int sl, int rb) { bfr[par][rb] = lds128(OA + (sl < 5 ? (sl + 1) % 5 : 5) * ITB + rb * 1024); };
    if (wave >= 6) WAITV(10);
    BAR();
    auto tbody = [&](auto VV) {
      constexpr int V = decltype(VV)::value;
#pragma unroll
      for (int rb = 0; rb < 3; ++rb) rdB(0, 0, rb);
#pragma unroll
      for (int cb = 0; cb < 5; ++cb) rdA(0, 0, cb);
      BAR();
      STAMP(12);
      auto phase = [&](auto QQ, auto MODE) {
        constexpr int q = decltype(QQ)::value, mode = decltype(MODE)::value, cur = q & 1;
        constexpr int R0 = V == 0 ? 0 : 5, D0 = V == 0 ? 9 : 0;
#pragma unroll
        for (int i = 0; i < 15; ++i) {
          acc[i % 3][i / 3] = MFMA16(af[cur][i / 3], bfr[cur][i % 3], acc[i % 3][i / 3]);
          SBAR();
          if (i >= R0 && i < R0 + 3) rdB(cur ^ 1, (q + 1) % 6, i - R0);
          if (i >= R0 + 3 && i < R0 + 8) rdA(cur ^ 1, (q + 1) % 6, i - R0 - 3);
          if (mode == 0 && i >= D0 && i < D0 + 6 && !((i - D0) & 1)) request(q, (i - D0) >> 1);
          if (mode == 2 && q >= 3 && i >= D0 && i < D0 + 6) request(2 * (q - 3) + (i - D0) / 3, (i - D0) % 3);
          SBAR();
        }
        if (mode == 0) WAITV(12);
        if (mode == 1) { if (q == 0) WAITV(9); else if (q == 1) WAITV(6); else if (q == 2) WAITV(3); else WAITV(0); }
        if (mode == 2) {
          if (q == 0 && wave < 6) WAITV(0);
          if (q == 1 && wave >= 6) WAITV(0);
          if (q == 2 && wave == 7) poll_flags(FL + (size_t)blockIdx.x * 32, 4, hq, TAG, ln);
          if (q == 4) WAITV(9);
          if (q == 5) WAITV(12);
        }
        BAR();
        if (mode == 2 && q == 0 && wave == 0 && ln < 4 && (int)ln != hq)
          __hip_atomic_store(FL + (size_t)(qbase + 8 * ln) * 32 + hq, TAG, __ATOMIC_RELAXED, __HIP_MEMORY_SCOPE_AGENT);
      };
      auto head6 = [&](auto MODE) {
        phase(std::integral_constant<int, 0>{}, MODE);
        phase(std::integral_constant<int, 1>{}, MODE);
        phase(std::integral_constant<int, 2>{}, MODE);
        phase(std::integral_constant<int, 3>{}, MODE);
        phase(std::integral_constant<int, 4>{}, MODE);
        phase(std::integral_constant<int, 5>{}, MODE);
      };
      sethead((hq + 1) & 3);
      head6(std::integral_constant<int, 2>{});
#pragma unroll 1
      for (int i = 2; i < 4; ++i) {
        sethead((hq + i) & 3);
        head6(std::integral_constant<int, 0>{});
      }
      STAMP(13);
      head6(std::integral_constant<int, 1>{});
    };
    if (wave < 4) tbody(std::integral_constant<int, 0>{}); else tbody(std::integral_constant<int, 1>{});
    STAMP(8);
    {
      const unsigned yw = L0 + (48 * rq + l15) * 336 + (80 * ch + 4 * g) * 2;
#pragma unroll
      for (int rb = 0; rb < 3; ++rb)
#pragma unroll
        for (int cb = 0; cb < 5; ++cb) {
          u32x2 w = {cvtpk(acc[rb][cb][0], acc[rb][cb][1]), cvtpk(acc[rb][cb][2], acc[rb][cb][3])};
          *(LAS u32x2*)(size_t)(yw + rb * 16 * 336 + cb * 32) = w;
        }
    }
    BAR();
    {
      const __amdgpu_buffer_rsrc_t rsP = __builtin_amdgcn_make_buffer_rsrc(ws + WS_P, (short)0, 2 * 8 * 4 * 8 * 30720, 0x00020000);
#pragma unroll
      for (int i = 0; i < 8; ++i) {
        const int j = wave + 8 * i;
        if (j < 60) {
          const int n = 64 * j + ln, row = n / 20, pc = n % 20, un = j >= 30 ? 1 : 0;
          const u32x4 v = *(const LAS u32x4*)(size_t)(L0 + row * 336 + pc * 16);
          __builtin_amdgcn_raw_buffer_store_b128(v, rsP, (((un * 8 + b0) * 4 + hq) * 8 + xj) * 30720 + (n - 1920 * un) * 16, 0, 16);
        }
      }
      WAITV(0);
      BAR();
      if (wave == 0) {
        if (ln < 8) __hip_atomic_store(FL + (size_t)((int)ln + 8 * xi) * 32 + 8 + xj, TAG, __ATOMIC_RELAXED, __HIP_MEMORY_SCOPE_AGENT);
        poll_flags(FL + (size_t)blockIdx.x * 32 + 8, 8, -1, TAG, ln);
      }
      BAR();
      STAMP(9);
      const int tz = wave * 64 + (int)ln;
      if (tz < 480) {
        const int u = tz / 240, e = tz % 240;
        const int pp = (((u * 8 + b0) * 4 + hq) * 8) * 30720 + 12 * xj * 320 + e * 16;
        u32x4 v[8];
#pragma unroll
        for (int j = 0; j < 8; ++j) v[j] = __builtin_amdgcn_raw_buffer_load_b128(rsP, pp + j * 30720, 0, 16);
        float a[8];
#pragma unroll
        for (int k = 0; k < 4; ++k) {
          float lo = 0.f, hi2 = 0.f;
#pragma unroll
          for (int j = 0; j < 8; ++j) { lo += __uint_as_float(v[j][k] << 16); hi2 += __uint_as_float(v[j][k] & 0xffff0000u); }
          a[2 * k] = lo; a[2 * k + 1] = hi2;
        }
        u32x4 w = {cvtpk(a[0], a[1]), cvtpk(a[2], a[3]), cvtpk(a[4], a[5]), cvtpk(a[6], a[7])};
        const int row = e / 20, c8 = e % 20;
        *(u32x4*)(out + (size_t)((b0 + 8 * u) * SQ + 12 * xj + row) * DM + hq * 160 + c8 * 8) = w;
      }
    }
    STAMP(10);
  }
}

extern "C" void kernel_launch(void* const* d_in, const int* in_sizes, int n_in, void* d_out, int out_size, void* d_ws,
                              size_t ws_size, hipStream_t stream) {
  const u16* x = (const u16*)d_in[0];
  const u16* kc = (const u16*)d_in[1];
  const u16* vc = (const u16*)d_in[2];
  const u16* wq = (const u16*)d_in[3];
  const u16* wo = (const u16*)d_in[4];
  (void)hipFuncSetAttribute((const void*)attn_kernel, hipFuncAttributeMaxDynamicSharedMemorySize, LDS_A);
  attn_kernel<<<256, 512, LDS_A, stream>>>(x, kc, vc, wq, wo, (u16*)d_out, (char*)d_ws, (unsigned long long*)((char*)d_ws + (128u << 20)));
}
```

```cpp
#include <hip/hip_runtime.h>
#include <hip/hip_bf16.h>
#include <stdint.h>
#include <type_traits>

typedef unsigned short u16;
typedef short bf16x8 __attribute__((ext_vector_type(8)));
typedef short s16x4 __attribute__((ext_vector_type(4)));
typedef float f32x16 __attribute__((ext_vector_type(16)));
typedef float f32x4 __attribute__((ext_vector_type(4)));
typedef float f32x2 __attribute__((ext_vector_type(2)));
typedef __bf16 bf16x2 __attribute__((ext_vector_type(2)));
typedef unsigned u32x4 __attribute__((ext_vector_type(4)));
typedef unsigned u32x2 __attribute__((ext_vector_type(2)));
#define LAS __attribute__((address_space(3)))
#define DI __device__ __forceinline__

constexpr int NB = 16, SQ = 96, DM = 640, NH = 32, SK = 1024, DH = 192;
constexpr int HD = NH * DH;

#define MFMA32(a, b, c) __builtin_amdgcn_mfma_f32_32x32x16_bf16((a), (b), (c), 0, 0, 0)
#define MFMA16(a, b, c) __builtin_amdgcn_mfma_f32_16x16x32_bf16((a), (b), (c), 0, 0, 0)
#define BAR() do { asm volatile("" ::: "memory"); __builtin_amdgcn_s_waitcnt(0xC07F); __builtin_amdgcn_s_barrier(); asm volatile("" ::: "memory"); } while (0)
#define WAITV(n) asm volatile("s_waitcnt vmcnt(%0)" ::"i"(n) : "memory")
#define SBAR() __builtin_amdgcn_sched_barrier(0)
#define MAX3(a, b, c) fmaxf(fmaxf((a), (b)), (c))
#define LANEID(v) asm volatile("v_mbcnt_lo_u32_b32 %0, -1, 0\n\tv_mbcnt_hi_u32_b32 %0, -1, %0" : "=v"(v))

DI unsigned cvtpk(float lo, float hi) {
  f32x2 v = {lo, hi};
  bf16x2 b = __builtin_convertvector(v, bf16x2);
  return __builtin_bit_cast(unsigned, b);
}
DI void dma16nt(unsigned lds_addr, const void* sbase, unsigned voff) {
  asm volatile("s_mov_b32 m0, %0\n\ts_nop 0\n\tglobal_load_lds_dwordx4 %1, %2 nt" ::"s"(lds_addr), "v"(voff), "s"(sbase) : "memory");
}
DI void bdma(unsigned lds_addr, __amdgpu_buffer_rsrc_t rs, unsigned voff, unsigned soff) {
  asm volatile("s_mov_b32 m0, %0\n\ts_nop 0\n\tbuffer_load_dwordx4 %1, %2, %3 offen lds" ::"s"(lds_addr), "v"(voff), "s"(rs), "s"(soff) : "memory");
}
DI void bdma1(unsigned lds_addr, __amdgpu_buffer_rsrc_t rs, unsigned voff, unsigned soff) {
  asm volatile("s_mov_b32 m0, %0\n\ts_nop 0\n\tbuffer_load_dwordx4 %1, %2, %3 offen sc1 lds" ::"s"(lds_addr), "v"(voff), "s"(rs), "s"(soff) : "memory");
}
DI void dma16(unsigned lds_addr, const void* sbase, unsigned voff) {
  asm volatile("s_mov_b32 m0, %0\n\ts_nop 0\n\tglobal_load_lds_dwordx4 %1, %2" ::"s"(lds_addr), "v"(voff), "s"(sbase) : "memory");
}
template <int I, int N, class F>
DI void sfor(F&& f) {
  if constexpr (I < N) { f(std::integral_constant<int, I>{}); sfor<I + 1, N>(f); }
}
DI bf16x8 lds128(unsigned a) { return *(const LAS bf16x8*)(size_t)a; }
DI s16x4 ldstr(unsigned a) { return __builtin_amdgcn_ds_read_tr16_b64_v4i16((LAS s16x4*)(size_t)a); }
DI bf16x8 cat(s16x4 a, s16x4 b) { return __builtin_shufflevector(a, b, 0, 1, 2, 3, 4, 5, 6, 7); }

constexpr unsigned ITB = 12288;
constexpr unsigned NSL = 5;
constexpr unsigned GRB = NSL * ITB;
constexpr unsigned SIDE = 2 * GRB;
constexpr unsigned MISC = SIDE + 36864;
constexpr unsigned CTL = MISC + 2048;
constexpr unsigned HB = MISC - 49152;
constexpr unsigned C_L = CTL;
constexpr unsigned C_R = CTL + 16;
constexpr unsigned C_QRDY = CTL + 48;
constexpr unsigned C_XF = CTL + 64;
constexpr unsigned C_H = CTL + 128;
constexpr unsigned C_OR = CTL + 160;
constexpr unsigned C_OD = CTL + 192;
constexpr unsigned LDS_A = 163840;
constexpr float CSC = 0.07216878364870322f * 1.4426950408889634f;
constexpr float THR_RAW = 8.0f / CSC;
constexpr int SPIN_MAX = 1 << 20;
#define HD_D 4
#define TL_D 4
#define KV0 2

#define STAMP(i)
#define EST(i)
#define FST(i)

DI void lds_post(unsigned a, unsigned v) {
  asm volatile("s_waitcnt lgkmcnt(0)\n\tds_write_b32 %0, %1" ::"v"(a), "v"(v) : "memory");
}
DI void lds_post_nw(unsigned a, unsigned v) {
  asm volatile("ds_write_b32 %0, %1" ::"v"(a), "v"(v) : "memory");
}
DI void lds_wait_ge(unsigned a, unsigned n) {
#pragma nounroll
  for (int it = 0; it < SPIN_MAX; ++it) {
    unsigned v;
    asm volatile("ds_read_b32 %0, %1\n\ts_waitcnt lgkmcnt(0)" : "=v"(v) : "v"(a) : "memory");
    if (__builtin_amdgcn_readfirstlane(v) >= n) break;
    __builtin_amdgcn_s_sleep(1);
  }
}
DI void lds_wait_min4(unsigned a, unsigned n) {
#pragma nounroll
  for (int it = 0; it < SPIN_MAX; ++it) {
    u32x4 d;
    asm volatile("ds_read_b128 %0, %1\n\ts_waitcnt lgkmcnt(0)" : "=v"(d) : "v"(a) : "memory");
    const unsigned mn = min(min(d[0], d[1]), min(d[2], d[3]));
    if (__builtin_amdgcn_readfirstlane(mn) >= n) break;
    __builtin_amdgcn_s_sleep(1);
  }
}
DI unsigned lds_rd1(unsigned a) {
  unsigned v;
  asm volatile("ds_read_b32 %0, %1\n\ts_waitcnt lgkmcnt(0)" : "=v"(v) : "v"(a) : "memory");
  return __builtin_amdgcn_readfirstlane(v);
}
DI unsigned lds_min3(unsigned a) {
  u32x4 d;
  asm volatile("ds_read_b128 %0, %1\n\ts_waitcnt lgkmcnt(0)" : "=v"(d) : "v"(a) : "memory");
  return __builtin_amdgcn_readfirstlane(min(min(d[0], d[1]), d[2]));
}
DI unsigned getvm() {
  unsigned sts;
  asm volatile("s_getreg_b32 %0, hwreg(HW_REG_IB_STS)" : "=s"(sts)::"memory");
  return (sts & 15u) | ((sts >> 18) & 0x30u);
}
DI unsigned lds_peek(unsigned a) { return *(volatile LAS unsigned*)(size_t)a; }

typedef __attribute__((address_space(1))) unsigned long long gu64;
extern "C" __device__ unsigned long long amd_dispatch_id() __asm("llvm.amdgcn.dispatch.id");
constexpr size_t WS_O = 0, WS_P = 32u << 20, WS_F = 96u << 20;

DI bool poll_flags(gu64* f, int nl, int skip, unsigned long long T, int lane) {
#pragma nounroll
  for (int it = 0; it < SPIN_MAX; ++it) {
    unsigned long long v = T;
    if (lane < nl && lane != skip) v = __hip_atomic_load(f + lane, __ATOMIC_RELAXED, __HIP_MEMORY_SCOPE_AGENT);
    if (__all(v == T)) {
      if (lane < nl && lane != skip) __hip_atomic_store(f + lane, 0ull, __ATOMIC_RELAXED, __HIP_MEMORY_SCOPE_AGENT);
      return true;
    }
    __builtin_amdgcn_s_sleep(2);
  }
  return false;
}

constexpr int hNL(int j) { return j < 20 ? 3 : 0; }
constexpr int hKV(int p) { return (p >= KV0 && p < KV0 + 12) ? 1 : 0; }
constexpr int hPend(int j) {
  int cnt = 0, first = 0;
  if (j < HD_D) { cnt = 3 * (HD_D - 1 - j); first = 0; }
  else { cnt = hKV(j - HD_D); first = j - HD_D + 1; }
  for (int p = first; p < j; ++p) cnt += hNL(p + HD_D) + hKV(p);
  return cnt;
}
constexpr int tNL(int j) { return j < 24 ? 3 : 0; }
constexpr int tPend(int j) {
  int cnt = 0, first = 0;
  if (j < TL_D) { cnt = 3 * (TL_D - 1 - j); first = 0; }
  else { cnt = 0; first = j - TL_D + 1; }
  for (int p = first; p < j; ++p) cnt += tNL(p + TL_D);
  return cnt;
}

__global__ __launch_bounds__(512, 2) void attn_kernel(const u16* x, const u16* kc, const u16* vc, const u16* wq, const u16* wo,
                                                      u16* out, char* ws, unsigned long long* stamps) {
  extern __shared__ __attribute__((aligned(16))) char lds[];
  const unsigned L0 = (unsigned)(size_t)(LAS char*)lds;
  const int tid = threadIdx.x, lane = tid & 63;
  const int wave = __builtin_amdgcn_readfirstlane(tid >> 6);
  const int xj = blockIdx.x & 7, xi = blockIdx.x >> 3;
  const int hq = xi & 3, h = 4 * xj + hq, b0 = xi >> 2;
  const size_t UB0 = (size_t)(b0 * NH + h) * SK * 384, UB1 = (size_t)((b0 + 8) * NH + h) * SK * 384;
  unsigned long long TAG;
  {
    const unsigned long long did = amd_dispatch_id(), dp = (unsigned long long)__builtin_amdgcn_dispatch_ptr();
    const unsigned t32 = (unsigned)(did * 0x9E3779B1ull) ^ (unsigned)(dp >> 6) * 0x85EBCA77u ^ (unsigned)(did >> 32);
    TAG = ((unsigned long long)(t32 ^ 0x5A17C3E9u) << 32) | t32;
  }
  const int qbase = xj + 32 * b0;
  gu64* FL = (gu64*)(ws + WS_F);
  const int kh = (0x1A >> wave) & 1, r = (0x102210 >> (4 * wave)) & 3;
  STAMP(0);
  bf16x8 qf[12];

  unsigned offK[6], offV[3];
  {
    if (wave == 6 && lane < 64) *(LAS unsigned*)(size_t)(L0 + CTL + lane * 4) = (lane == 46 || lane == 47) ? 1u : 0u;
    const int rot = (b0 * 5 / 4 + hq * 5 / 2) % 10;
    const int dh = wave & 1, rq = wave >> 1;
    const unsigned g = lane >> 4, q4 = (lane & 15) >> 2, p4 = lane & 3, l15 = lane & 15;
    const __amdgpu_buffer_rsrc_t rsW = __builtin_amdgcn_make_buffer_rsrc((void*)((const char*)wq + h * DH * 2), (short)0, DM * HD * 2 - h * DH * 2, 0x00020000);
    const __amdgpu_buffer_rsrc_t rsX = __builtin_amdgcn_make_buffer_rsrc((void*)((const char*)x + b0 * SQ * DM * 2), (short)0, (NB - b0) * SQ * DM * 2, 0x00020000);
    unsigned gA, gB, gC;
    {
      auto wpiece = [&](int p) {
        const int n = 64 * p + lane, row = n / 24, pc = n % 24;
        const int sw = ((row >> 1) & 1) | (((row >> 3) & 1) << 1);
        return (unsigned)(row * (HD * 2) + ((pc ^ (sw << 1)) * 16));
      };
      auto xpiece = [&](int p) {
        const int row = 8 * p + (lane >> 3);
        return (unsigned)(row * (DM * 2) + (((lane & 7) ^ ((row >> 1) & 7)) * 16));
      };
      gA = wpiece(wave);
      gB = wave < 4 ? wpiece(8 + wave) : xpiece(wave - 4);
      gC = xpiece(4 + wave);
    }
    unsigned hK[3], hV[3];
    constexpr unsigned WB = 3 * ITB, G1X = GRB + 2 * ITB, U1 = 8 * SQ * DM * 2;
    constexpr unsigned WSTEP = 32 * HD * 2;
    const unsigned mW = L0 + WB + wave * 1024;
    const unsigned mX = L0 + (4 + wave) * 1024;
    const unsigned s2 = ((q4 >> 1) & 1) | ((g & 1) << 1);
    unsigned E[4];
    const unsigned XA0 = L0 + (rq >> 1) * G1X + (48 * (rq & 1) + l15) * 128 + ((g ^ ((l15 >> 1) & 7)) * 16), XA1 = XA0 ^ 64;
    f32x4 acc[6][3] = {};
    bf16x8 af[2][6], bfr[2][3];
    unsigned xa;
    auto rdA = [&](int par, int c1, int db) {
      const unsigned a = E[db & 3] + (db >> 2) * 128 + (c1 & 3) * ITB;
      af[par][db] = cat(ldstr(a), ldstr(a + 4 * 384));
    };
    auto rdB = [&](int par, int rb) { bfr[par][rb] = lds128(xa + rb * 2048); };
    unsigned woff = (unsigned)(2 * rot) * WSTEP, xoff = (unsigned)rot * 128;
    auto advW = [&]() { woff += WSTEP; woff = woff == 20 * WSTEP ? 0u : woff; };
    auto advX = [&]() { xoff += 128; xoff = xoff == 1280 ? 0u : xoff; };
    auto request = [&](auto VV, int k, int wsl, unsigned xs, int un) {
      constexpr int V = decltype(VV)::value;
      if (k == 0) bdma(mW + wsl * ITB, rsW, gA, woff);
      if (k == 1) {
        if (V == 0) bdma(mW + 8192 + wsl * ITB, rsW, gB, woff);
        else bdma(mX - 8192 + un * G1X + xs, rsX, gB, xoff + un * U1);
      }
      if (k == 2) bdma(mX + un * G1X + xs, rsX, gC, xoff + un * U1);
    };
    unsigned xr = 2 * ITB;
    unsigned xw = 1 * ITB;
    auto body = [&](auto VV) {
      constexpr int V = decltype(VV)::value;
      if (V == 1) bdma(mX - 8192 + 2 * ITB, rsX, gB, xoff);
      bdma(mX + 2 * ITB, rsX, gC, xoff);
#pragma unroll
      for (int c = -4; c < 0; ++c) {
        if ((c & 1) != 0) advX();
        const unsigned xs = (unsigned)((((c + 1) >> 1) + 2 + 2) % 3) * ITB;
#pragma unroll
        for (int k = 0; k < 3; ++k) request(VV, k, c & 3, xs, (c + 1) & 1);
        advW();
      }
      SBAR();
      asm volatile("" ::: "memory");
#pragma unroll
    for (int j = 0; j < 6; ++j) {
      int n = 64 * j + lane, row = n / 24, pc = n % 24;
      offK[j] = row * 384 + ((pc ^ ((row >> 1) & 7)) * 16);
    }
#pragma unroll
    for (int j = 0; j < 3; ++j) {
      int n = 64 * j + lane, row = n / 24, pc = n % 24;
      offV[j] = row * 384 + ((pc ^ (((row >> 1) & 1) << 2)) * 16);
    }
#pragma unroll
    for (int j = 0; j < 3; ++j) {
      const int n = 64 * (3 * (wave & 3) + j) + lane, row = n / 24, pc = n % 24;
      hK[j] = row * 384 + ((pc ^ ((row >> 1) & 7)) * 16);
      hV[j] = row * 384 + ((pc ^ (((row >> 1) & 1) << 2)) * 16);
    }
#pragma unroll
      for (int j = 0; j < 4; ++j) E[j] = L0 + WB + (8 * g + q4) * 384 + 32 * ((unsigned)(6 * dh + j) ^ s2) + 8 * p4;
      WAITV(9);
      BAR();
      xa = XA0 + 2 * ITB;
#pragma unroll
      for (int rb = 0; rb < 3; ++rb) rdB(0, rb);
#pragma unroll
      for (int db = 0; db < 6; ++db) rdA(0, 0, db);
      WAITV(6);
      BAR();
      STAMP(4);
      auto phase = [&](int c, auto QQ, auto MODE) {
        constexpr int q = decltype(QQ)::value, mode = decltype(MODE)::value, cur = q & 1;
        if (cur == 1) {
          xr = xr == 2 * ITB ? 0 : xr + ITB;
          if (mode == 0) { xw = xw == 2 * ITB ? 0 : xw + ITB; advX(); }
        }
        xa = (cur == 1 ? XA0 : XA1) + xr;
        constexpr int R0 = V == 0 ? 0 : 5, D0 = V == 0 ? 10 : 0;
#pragma unroll
        for (int i = 0; i < 18; ++i) {
          acc[i / 3][i % 3] = MFMA16(af[cur][i / 3], bfr[cur][i % 3], acc[i / 3][i % 3]);
          SBAR();
          if (i >= R0 && i < R0 + 3) rdB(cur ^ 1, i - R0);
          if (i >= R0 + 3 && i < R0 + 9) rdA(cur ^ 1, q + 1, i - R0 - 3);
          if (mode == 0 && i >= D0 && i < D0 + 6 && !((i - D0) & 1)) request(VV, (i - D0) >> 1, q, xw, (q + 1) & 1);
          if (mode == 0 && i == D0 + 5) advW();
          if (mode == 2 && i >= D0 && i < D0 + 6 && !((i - D0) & 1)) {
            const int k = (i - D0) >> 1, li = c - 17;
            const unsigned dst = L0 + V * G1X + li * ITB + (3 * (wave & 3) + k) * 1024;
            if (li == 2) dma16nt(dst, (const char*)vc + (V ? UB1 : UB0), hV[k]);
            else dma16nt(dst, (const char*)kc + (V ? UB1 : UB0) + (size_t)li * ITB, hK[k]);
          }
          SBAR();
        }
        if (mode == 0) WAITV(6);
        if (mode == 1) WAITV(0);
        BAR();
      };
      using I0 = std::integral_constant<int, 0>; using I1 = std::integral_constant<int, 1>;
      using I2 = std::integral_constant<int, 2>; using I3 = std::integral_constant<int, 3>;
#pragma unroll 1
      for (int c = 0; c < 16; c += 4) {
        phase(c, I0{}, I0{});
        phase(c + 1, I1{}, I0{});
        phase(c + 2, I2{}, I0{});
        phase(c + 3, I3{}, I0{});
      }
      STAMP(5);
      phase(16, I0{}, I1{});
      phase(17, I1{}, I2{});
      phase(18, I2{}, I2{});
      phase(19, I3{}, I2{});
    };
    if (wave < 4) body(std::integral_constant<int, 0>{}); else body(std::integral_constant<int, 1>{});
    STAMP(11);
    {
      unsigned lz; LANEID(lz);
      const unsigned zg = lz >> 4, zl = lz & 15;
      const unsigned qimg = L0 + (wave < 4 ? SIDE : WB);
#pragma unroll
      for (int rb = 0; rb < 3; ++rb) {
        const int R16 = 3 * (rq & 1) + rb;
        const unsigned qb = qimg + ((R16 >> 1) * 12 + 6 * dh) * 1024 + (16 * (R16 & 1) + zl + 32 * (zg >> 1)) * 16 + 8 * (zg & 1);
#pragma unroll
        for (int db = 0; db < 6; ++db) {
          u32x2 w = {cvtpk(acc[db][rb][0], acc[db][rb][1]), cvtpk(acc[db][rb][2], acc[db][rb][3])};
          *(LAS u32x2*)(size_t)(qb + db * 1024) = w;
        }
      }
      BAR();
      if (wave < 6) {
#pragma unroll
        for (int ks = 0; ks < 12; ++ks) qf[ks] = lds128(L0 + (kh ? WB : SIDE) + ((r * 12 + ks) * 64 + lz) * 16);
      }
      BAR();
    }
  }
  STAMP(1);

  if (wave >= 6) {
    const int hw = wave - 6;
    __builtin_amdgcn_s_setprio(3);
    unsigned ll; LANEID(ll);
    const unsigned aL = L0 + C_L + 8 * hw;
    const char* sb = hw ? (const char*)vc : (const char*)kc;
    unsigned off[6];
#pragma unroll
    for (int q = 0; q < 6; ++q) off[q] = hw ? offV[q % 3] + (q / 3) * 3072 : offK[q];
    auto issue_item = [&](unsigned g_, unsigned tile, unsigned slot_) {
      const char* src = sb + (g_ ? UB1 : UB0) + (size_t)tile * ITB;
      const unsigned dst = L0 + g_ * GRB + slot_;
#pragma unroll
      for (int q = 0; q < 12; ++q) dma16nt(dst + q * 1024, src + (q / 6) * 6144, off[q % 6]);
    };
    {
      const unsigned p0 = 3 + hw, t0 = 2 - hw;
      issue_item(0, t0, p0 * ITB);
      issue_item(1, t0, ((p0 + 2) % 5) * ITB);
    }
    WAITV(30); lds_post_nw(L0 + C_H + 4 * wave, 1);
    WAITV(27); lds_post_nw(L0 + C_H + 4 * wave, 2);
    WAITV(24); lds_post_nw(L0 + C_H + 4 * wave, 3);
    if (hw == 0) {
      lds_wait_min4(L0 + C_H, 1); lds_post_nw(aL, 1);
      lds_wait_min4(L0 + C_H + 16, 1); lds_post_nw(aL + 4, 1);
      lds_wait_min4(L0 + C_H, 2); lds_post_nw(aL, 2);
      lds_wait_min4(L0 + C_H + 16, 2); lds_post_nw(aL + 4, 2);
    } else {
      lds_wait_min4(L0 + C_H, 3); lds_post_nw(aL, 1);
      lds_wait_min4(L0 + C_H + 16, 3); lds_post_nw(aL + 4, 1);
    }
    unsigned n0 = 3 - hw, n1 = 3 - hw;
    unsigned pub0 = 2 - hw, pub1 = 2 - hw;
    unsigned s0 = ((5 + hw) % 5) * ITB, s1 = ((5 + hw + 2) % 5) * ITB;
    unsigned fifo = 2, qn = 2, xtra = 4;
    {
      const char* wsrc = (const char*)wo + (size_t)(4 * xj * DH + 96 * b0 + 48 * hw) * (DM * 2) + hq * 320;
      const unsigned o0 = (ll >> 2) * (DM * 2) + min((ll & 3) * 128u, 316u);
#pragma unroll
      for (int z = 0; z < 3; ++z)
        asm volatile("s_mov_b32 m0, %0\n\ts_nop 0\n\tglobal_load_lds_dword %1, %2" ::"s"(L0 + MISC + 1536), "v"(o0 + z * 16 * DM * 2), "s"(wsrc) : "memory");
      const char* t0 = ll == 0 ? ws + WS_O + (size_t)blockIdx.x * 36864 : ll == 1 ? ws + WS_O + (size_t)(256 + blockIdx.x) * 36864
                     : ll == 2 ? ws + WS_P + (size_t)(((b0 * 4 + hq) * 8 + xj) * 30720) : ll == 3 ? ws + WS_P + (size_t)((((8 + b0) * 4 + hq) * 8 + xj) * 30720)
                     : ll == 4 ? ws + WS_F + (size_t)blockIdx.x * 256 : (const char*)out + (size_t)((b0 + 8 * (ll & 1)) * SQ + 12 * xj) * DM * 2;
      if (ll < 7)
        asm volatile("s_mov_b32 m0, %0\n\ts_nop 0\n\tglobal_load_lds_dword %1, off" ::"s"(L0 + MISC + 1536), "v"(t0) : "memory");
    }
    unsigned tix = 0, xt = 0;
    unsigned nissued = 2;
    const __amdgpu_buffer_rsrc_t rsWo = __builtin_amdgcn_make_buffer_rsrc((void*)((const char*)wo + 4 * xj * DH * DM * 2 + hq * 320), (short)0, HD * DM * 2 - (4 * xj * DH * DM * 2 + hq * 320), 0x00020000);
    unsigned go[5];
#pragma unroll
    for (int p = 0; p < 5; ++p) {
      const int n = 64 * p + ll, row = n / 20, pc = n % 20;
      go[p] = row * (DM * 2) + ((pc ^ (((row >> 3) & 1) << 1)) * 16);
    }
    auto adv2 = [&](unsigned s, unsigned d) { s += d * ITB; return s >= GRB ? s - GRB : s; };
    unsigned rr = 0;
#pragma nounroll
    for (int it = 0; it < (SPIN_MAX << 2); ++it) {
      bool idle = true;
      if (n0 < 32 || n1 < 32) {
        if (getvm() <= 48) {
          const unsigned p0 = hw ? min(2 * n0 + 2, 63u) : 2 * n0 - 1, p1 = hw ? min(2 * n1 + 2, 63u) : 2 * n1 - 1;
          const bool ok0 = n0 < 32 && lds_min3(L0 + C_R) + NSL > p0;
          const bool ok1 = n1 < 32 && lds_min3(L0 + C_R + 16) + NSL > p1;
          const bool take1 = ok1 && (!ok0 || rr);
          if (take1) {
            issue_item(1, n1, s1);
            ++n1; s1 = adv2(s1, (hw && n1 == 31) ? 1 : 2);
            fifo |= 1u << qn; ++qn; ++nissued; rr = 0; idle = false;
          } else if (ok0) {
            issue_item(0, n0, s0);
            ++n0; s0 = adv2(s0, (hw && n0 == 31) ? 1 : 2);
            ++qn; ++nissued; rr = 1; idle = false;
          }
        }
      } else if (tix < 5) {
        if (lds_min3(L0 + C_R) >= 60 + tix && getvm() <= 50) {
          const unsigned c0 = tix == 0 ? 5 : tix - 1, nc = tix == 4 ? 2 : 1;
#pragma unroll 1
          for (unsigned c6 = c0; c6 < c0 + nc; ++c6) {
#pragma unroll
            for (int p = 0; p < 5; ++p) bdma(L0 + c6 * 10240 + (5 * hw + p) * 1024, rsWo, go[p], (hq * DH + 32 * c6 + 16 * hw) * (DM * 2));
            xt += 5;
          }
          ++tix;
          idle = false;
        }
      }
      if (qn > 0) {
        const unsigned vm = getvm();
        bool any = false;
#pragma nounroll
        while (qn > 0) {
          const unsigned after = 12 * (qn - 1) + xt + ((nissued - qn) < 2 ? xtra : 0);
          if (vm > after) break;
          if (fifo & 1) ++pub1; else ++pub0;
          fifo >>= 1; --qn; any = true;
        }
        if (any) {
          lds_post_nw(aL, pub0);
          lds_post_nw(aL + 4, pub1);
          idle = false;
        }
      } else if (n0 == 32 && n1 == 32 && tix == 5) {
        break;
      }
      if (idle) __builtin_amdgcn_s_sleep(1);
    }
    __builtin_amdgcn_s_setprio(0);
  } else {
    unsigned ln; LANEID(ln);
    unsigned kof[4], vE, vO;
    {
      const unsigned a32 = ln & 31, ahi = ln >> 5, ablk = (ln >> 4) & 1, aq4 = (ln & 15) >> 2, ap4 = ln & 3, axq = (aq4 >> 1) & 1;
#pragma unroll
      for (int i = 0; i < 4; ++i) kof[i] = L0 + kh * GRB + a32 * 384 + (((2 * i + ahi) ^ ((a32 >> 1) & 7)) * 16);
      vE = L0 + kh * GRB + (4 * ahi + aq4) * 384 + 64 * axq + 32 * ablk + 8 * ap4;
      vO = L0 + kh * GRB + (4 * ahi + aq4) * 384 - 64 * axq + 32 * ablk + 8 * ap4;
    }
    const unsigned aFK = L0 + C_L + 4 * kh, aFV = L0 + C_L + 8 + 4 * kh, aR = L0 + C_R + 16 * kh + 4 * r;
    const int pw = (0x102354 >> (4 * wave)) & 7;
    const unsigned fme = L0 + C_XF + 8 * wave, fpa = L0 + C_XF + 8 * pw;
    unsigned cur = kh ? 2 * ITB : 0;
    unsigned cnt = 0;
#define ADV() do { cur = (cur + ITB == GRB) ? 0u : cur + ITB; ++cnt; } while (0)
#define KADDR(ks) (((ks) & 3) == 0 ? k0 : ((ks) & 3) == 1 ? k1 : ((ks) & 3) == 2 ? k2 : k3) + 128 * ((ks) >> 2)
#define VADDR(j) ((((j) % 6) & 1) ? vbO : vbE) + (16 * ((j) / 6)) * 384 + 64 * ((j) % 6)

    {
      float m, l = 0.f;
      f32x16 o[6] = {};
      f32x16 sB;
      float pe[16];
      bf16x8 kf[12], vf[12];
      unsigned k0, k1, k2, k3, vbE, vbO;
      WAITV(6); lds_post_nw(L0 + C_H + 4 * wave, 1);
      lds_wait_ge(aFK, 1);
      k0 = kof[0] + cur; k1 = kof[1] + cur; k2 = kof[2] + cur; k3 = kof[3] + cur;
#pragma unroll
      for (int ks = 0; ks < 12; ++ks) kf[ks] = lds128(KADDR(ks));
      ADV();
      lds_post_nw(aR, cnt);
      SBAR();
      {
        f32x16 z = {};
        sB = MFMA32(kf[0], qf[0], z);
#pragma unroll
        for (int ks = 1; ks < 12; ++ks) sB = MFMA32(kf[ks], qf[ks], sB);
      }
      SBAR();
      WAITV(3); lds_post_nw(L0 + C_H + 4 * wave, 2);
      lds_wait_ge(aFK, 2);
      WAITV(0); lds_post_nw(L0 + C_H + 4 * wave, 3);
      k0 = kof[0] + cur; k1 = kof[1] + cur; k2 = kof[2] + cur; k3 = kof[3] + cur;
#pragma unroll
      for (int ks = 0; ks < 4; ++ks) kf[ks] = lds128(KADDR(ks));
      {
        float pmax = sB[0];
#pragma unroll
        for (int i = 1; i < 16; ++i) pmax = fmaxf(pmax, sB[i]);
        auto rr = __builtin_amdgcn_permlane32_swap(__float_as_uint(pmax), __float_as_uint(pmax), false, false);
        m = fmaxf(__uint_as_float(rr[0]), __uint_as_float(rr[1]));
        const float mc = -m * CSC;
#pragma unroll
        for (int i = 0; i < 16; ++i) pe[i] = fmaf(sB[i], CSC, mc);
      }
      SBAR();
      bf16x8 pa[2];
      auto expstep = [&](int i) {
        pe[2 * i] = __builtin_amdgcn_exp2f(pe[2 * i]);
        pe[2 * i + 1] = __builtin_amdgcn_exp2f(pe[2 * i + 1]);
        l += pe[2 * i];
        l += pe[2 * i + 1];
        asm volatile("" : "+v"(pe[2 * i]), "+v"(pe[2 * i + 1]), "+v"(l));
      };
      auto cvtstep = [&](int s) {
        u32x4 w = {cvtpk(pe[8 * s], pe[8 * s + 1]), cvtpk(pe[8 * s + 2], pe[8 * s + 3]), cvtpk(pe[8 * s + 4], pe[8 * s + 5]),
                   cvtpk(pe[8 * s + 6], pe[8 * s + 7])};
        asm volatile("" : "+v"(w));
        pa[s] = __builtin_bit_cast(bf16x8, w);
      };
#pragma unroll 1
      for (int t = 0; t < 31; ++t) {
        unsigned fl = 0;
#pragma unroll
        for (int i = 0; i < 12; ++i) {
          if (i == 0) { f32x16 z = {}; sB = MFMA32(kf[0], qf[0], z); }
          else sB = MFMA32(kf[i], qf[i], sB);
          if (i < 8) kf[i + 4] = lds128(KADDR(i + 4));
          if (i == 2) fl = lds_peek(aFV);
          if (i == 7) { ADV(); lds_post_nw(aR, cnt); }
          if (i < 8) expstep(i);
          if (i == 8) {
            if (__builtin_amdgcn_readfirstlane(fl) < (unsigned)t + 1) lds_wait_ge(aFV, t + 1);
            vbE = vE + cur; vbO = vO + cur;
            cvtstep(0);
          }
          if (i == 9) cvtstep(1);
          if (i >= 8) vf[i - 8] = cat(ldstr(VADDR(i - 8)), ldstr(VADDR(i - 8) + 8 * 384));
          SBAR();
        }
        float pmax = 0.f, alpha = 1.f, mc = 0.f;
        bool resc = false;
#pragma unroll
        for (int j = 0; j < 12; ++j) {
          o[j % 6] = MFMA32(vf[j], pa[j / 6], o[j % 6]);
          if (j < 8) vf[j + 4] = cat(ldstr(VADDR(j + 4)), ldstr(VADDR(j + 4) + 8 * 384));
          if (j == 2) fl = lds_peek(aFK);
          if (j == 7) { ADV(); lds_post_nw(aR, cnt); }
          if (j == 1) {
            asm volatile("" : "+v"(sB));
            pmax = MAX3(MAX3(sB[0], sB[1], sB[2]), MAX3(sB[3], sB[4], sB[5]), MAX3(sB[6], sB[7], sB[8]));
            asm volatile("" : "+v"(pmax));
          }
          if (j == 2) {
            pmax = MAX3(MAX3(sB[9], sB[10], sB[11]), MAX3(sB[12], sB[13], sB[14]), MAX3(sB[15], sB[15], pmax));
            asm volatile("" : "+v"(pmax));
          }
          if (j == 3) {
            auto rr = __builtin_amdgcn_permlane32_swap(__float_as_uint(pmax), __float_as_uint(pmax), false, false);
            pmax = fmaxf(__uint_as_float(rr[0]), __uint_as_float(rr[1]));
            resc = pmax - m > THR_RAW;
            const float mn = resc ? pmax : m;
            alpha = __builtin_amdgcn_exp2f((m - mn) * CSC);
            m = mn;
            mc = -mn * CSC;
            l *= alpha;
            asm volatile("" : "+v"(m), "+v"(mc), "+v"(l), "+v"(alpha));
          }
          if (j >= 4 && j < 8) {
#pragma unroll
            for (int i = 4 * (j - 4); i < 4 * (j - 4) + 4; ++i) pe[i] = fmaf(sB[i], CSC, mc);
            asm volatile("" : "+v"(pe[4 * (j - 4)]), "+v"(pe[4 * (j - 4) + 1]), "+v"(pe[4 * (j - 4) + 2]), "+v"(pe[4 * (j - 4) + 3]));
          }
          if (j == 8) {
            if (t < 30 && __builtin_amdgcn_readfirstlane(fl) < (unsigned)t + 3) lds_wait_ge(aFK, t + 3);
            k0 = kof[0] + cur; k1 = kof[1] + cur; k2 = kof[2] + cur; k3 = kof[3] + cur;
          }
          if (j >= 8) kf[j - 8] = lds128(KADDR(j - 8));
          SBAR();
        }
        if (__any(resc)) {
#pragma unroll
          for (int d = 0; d < 6; ++d)
#pragma unroll
            for (int i = 0; i < 16; ++i) o[d][i] *= alpha;
        }
      }
      {
        lds_wait_ge(aFV, 32);
        vbE = vE + cur; vbO = vO + cur;
#pragma unroll
        for (int j = 0; j < 6; ++j) vf[j] = cat(ldstr(VADDR(j)), ldstr(VADDR(j) + 8 * 384));
        SBAR();
#pragma unroll
        for (int i = 0; i < 8; ++i) expstep(i);
        cvtstep(0);
        cvtstep(1);
        SBAR();
#pragma unroll
        for (int j = 0; j < 12; ++j) {
          o[j % 6] = MFMA32(vf[j], pa[j / 6], o[j % 6]);
          if (j < 6) vf[j + 6] = cat(ldstr(VADDR(j + 6)), ldstr(VADDR(j + 6) + 8 * 384));
          if (j == 5) { ADV(); lds_post_nw(aR, cnt); }
          SBAR();
        }
      }
      STAMP(6);
      {
        auto rr = __builtin_amdgcn_permlane32_swap(__float_as_uint(l), __float_as_uint(l), false, false);
        l = __uint_as_float(rr[0]) + __uint_as_float(rr[1]);
      }
      const float inv = 1.0f / l;
      unsigned lz; LANEID(lz);
      const int zc = lz & 31, zh = lz >> 5;
      const __amdgpu_buffer_rsrc_t rsO = __builtin_amdgcn_make_buffer_rsrc(ws + WS_O, (short)0, 2 * 256 * 36864, 0x00020000);
      const int osw = ((zc >> 2) & 1) << 1;
      const int obase = (kh * 256 + blockIdx.x) * 36864 + (32 * r + zc) * 64;
      u32x4 wq_[6][2];
#pragma unroll
      for (int d = 0; d < 6; ++d)
#pragma unroll
        for (int i = 0; i < 4; i += 2) {
          unsigned pk[4];
#pragma unroll
          for (int j = 0; j < 2; ++j) {
            pk[2 * j] = cvtpk(o[d][4 * (i + j)] * inv, o[d][4 * (i + j) + 1] * inv);
            pk[2 * j + 1] = cvtpk(o[d][4 * (i + j) + 2] * inv, o[d][4 * (i + j) + 3] * inv);
          }
          auto r0 = __builtin_amdgcn_permlane32_swap(pk[0], pk[2], false, false);
          auto r1 = __builtin_amdgcn_permlane32_swap(pk[1], pk[3], false, false);
          wq_[d][i >> 1] = u32x4{r0[0], r1[0], r0[1], r1[1]};
          __builtin_amdgcn_raw_buffer_store_b128(wq_[d][i >> 1], rsO, obase + d * 6144 + (((i + zh) ^ osw) * 16), 0, 16);
        }
#pragma nounroll
      for (int it = 0; it < SPIN_MAX; ++it) {
        if (lds_min3(L0 + C_R + 16) >= 64) break;
        __builtin_amdgcn_s_sleep(1);
      }
#pragma unroll
      for (int d = 0; d < 6; ++d)
#pragma unroll
        for (int i = 0; i < 2; ++i)
          *(LAS u32x4*)(size_t)(L0 + 61440 + (d < 5 ? (d + 1) % 5 : 5) * ITB + kh * 6144 + r * 2048 + zc * 64 + (((2 * i + zh) ^ osw) * 16)) = wq_[d][i];
      STAMP(7);
    }
  }
  {
    unsigned ln; LANEID(ln);
    constexpr unsigned TWO = 10240, TOB = 61440;
    const int ch = wave & 1, rq = wave >> 1;
    const unsigned g = ln >> 4, q4 = (ln & 15) >> 2, p4 = ln & 3, l15 = ln & 15;
    unsigned gA, gB;
    {
      auto wpiece = [&](int p) {
        const int n = 64 * p + ln, row = n / 20, pc = n % 20;
        return (unsigned)(row * (DM * 2) + ((pc ^ (((row >> 3) & 1) << 1)) * 16));
      };
      gA = wpiece(wave);
      gB = wave < 2 ? wpiece(8 + wave) : ln * 16 + (wave - 2) * 1024;
    }
    const int pc3 = wave < 6 ? wave : wave - 2;
    const unsigned gC = ln * 16 + pc3 * 1024 + 256 * 36864;
    const __amdgpu_buffer_rsrc_t rsWo = __builtin_amdgcn_make_buffer_rsrc((void*)((const char*)wo + 4 * xj * DH * DM * 2 + hq * 320), (short)0, HD * DM * 2 - (4 * xj * DH * DM * 2 + hq * 320), 0x00020000);
    const __amdgpu_buffer_rsrc_t rsOt = __builtin_amdgcn_make_buffer_rsrc(ws + WS_O + (size_t)qbase * 36864, (short)0, (512 - qbase) * 36864, 0x00020000);
    const __amdgpu_buffer_rsrc_t rsB = wave < 2 ? rsWo : rsOt;
    const unsigned mA = L0 + wave * 1024, mC = L0 + TOB + 6144 + pc3 * 1024;
    const unsigned mB = wave < 2 ? L0 + (8 + wave) * 1024 : L0 + TOB + (wave - 2) * 1024;
    const unsigned stepB = wave < 2 ? 32 * DM * 2 : 6144;
    unsigned wsrc, osrc, bsrc;
    auto sethead = [&](int hs) { wsrc = hs * (DH * DM * 2); osrc = hs * (8 * 36864); bsrc = wave < 2 ? wsrc : osrc; };
    auto request = [&](int c6, int k) {
      if (k == 0) bdma(mA + c6 * TWO, rsWo, gA, wsrc + c6 * (32 * DM * 2));
      const unsigned os = (c6 < 5 ? (c6 + 1) % 5 : 5) * ITB;
      if (k == 1) bdma1(mB + (wave < 2 ? c6 * TWO : os), rsB, gB, bsrc + c6 * stepB);
      if (k == 2) bdma1(mC + os, rsOt, gC, osrc + c6 * 6144);
    };
    const unsigned WAb = L0 + (8 * g + q4) * 320 + 160 * ch + 8 * p4;
    const unsigned WA0 = WAb + ((ch & 1) ? -32 * (int)(g & 1) : 32 * (int)(g & 1));
    const unsigned WA1 = WAb + ((ch & 1) ? 32 * (int)(g & 1) : -32 * (int)(g & 1));
    const unsigned OA = L0 + TOB + (48 * rq + l15) * 64 + ((g ^ (((l15 >> 2) & 1) << 1)) * 16);
    f32x4 acc[3][5] = {};
    bf16x8 af[2][5], bfr[2][3];
    auto rdA = [&](int par, int sl, int cb) {
      const unsigned a = ((cb & 1) ? WA1 : WA0) + sl * TWO + cb * 32;
      af[par][cb] = cat(ldstr(a), ldstr(a + 4 * 320));
    };
    auto rdB = [&](int par, int sl, int rb) { bfr[par][rb] = lds128(OA + (sl < 5 ? (sl + 1) % 5 : 5) * ITB + rb * 1024); };
    if (wave >= 6) WAITV(10);
    BAR();
    auto tbody = [&](auto VV) {
      constexpr int V = decltype(VV)::value;
#pragma unroll
      for (int rb = 0; rb < 3; ++rb) rdB(0, 0, rb);
#pragma unroll
      for (int cb = 0; cb < 5; ++cb) rdA(0, 0, cb);
      BAR();
      STAMP(12);
      auto phase = [&](auto QQ, auto MODE) {
        constexpr int q = decltype(QQ)::value, mode = decltype(MODE)::value, cur = q & 1;
        constexpr int R0 = V == 0 ? 0 : 5, D0 = V == 0 ? 9 : 0;
#pragma unroll
        for (int i = 0; i < 15; ++i) {
          acc[i % 3][i / 3] = MFMA16(af[cur][i / 3], bfr[cur][i % 3], acc[i % 3][i / 3]);
          SBAR();
          if (i >= R0 && i < R0 + 3) rdB(cur ^ 1, (q + 1) % 6, i - R0);
          if (i >= R0 + 3 && i < R0 + 8) rdA(cur ^ 1, (q + 1) % 6, i - R0 - 3);
          if (mode == 0 && i >= D0 && i < D0 + 6 && !((i - D0) & 1)) request(q, (i - D0) >> 1);
          if (mode == 2 && q >= 3 && i >= D0 && i < D0 + 6) request(2 * (q - 3) + (i - D0) / 3, (i - D0) % 3);
          SBAR();
        }
        if (mode == 0) WAITV(12);
        if (mode == 1) { if (q == 0) WAITV(9); else if (q == 1) WAITV(6); else if (q == 2) WAITV(3); else WAITV(0); }
        if (mode == 2) {
          if (q == 0 && wave < 6) WAITV(0);
          if (q == 1 && wave >= 6) WAITV(0);
          if (q == 2 && wave == 7) poll_flags(FL + (size_t)blockIdx.x * 32, 4, hq, TAG, ln);
          if (q == 4) WAITV(9);
          if (q == 5) WAITV(12);
        }
        BAR();
        if (mode == 2 && q == 0 && wave == 0 && ln < 4 && (int)ln != hq)
          __hip_atomic_store(FL + (size_t)(qbase + 8 * ln) * 32 + hq, TAG, __ATOMIC_RELAXED, __HIP_MEMORY_SCOPE_AGENT);
      };
      auto head6 = [&](auto MODE) {
        phase(std::integral_constant<int, 0>{}, MODE);
        phase(std::integral_constant<int, 1>{}, MODE);
        phase(std::integral_constant<int, 2>{}, MODE);
        phase(std::integral_constant<int, 3>{}, MODE);
        phase(std::integral_constant<int, 4>{}, MODE);
        phase(std::integral_constant<int, 5>{}, MODE);
      };
      sethead((hq + 1) & 3);
      head6(std::integral_constant<int, 2>{});
#pragma unroll 1
      for (int i = 2; i < 4; ++i) {
        sethead((hq + i) & 3);
        head6(std::integral_constant<int, 0>{});
      }
      STAMP(13);
      head6(std::integral_constant<int, 1>{});
    };
    if (wave < 4) tbody(std::integral_constant<int, 0>{}); else tbody(std::integral_constant<int, 1>{});
    STAMP(8);
    {
      const unsigned yw = L0 + (48 * rq + l15) * 336 + (80 * ch + 4 * g) * 2;
#pragma unroll
      for (int rb = 0; rb < 3; ++rb)
#pragma unroll
        for (int cb = 0; cb < 5; ++cb) {
          u32x2 w = {cvtpk(acc[rb][cb][0], acc[rb][cb][1]), cvtpk(acc[rb][cb][2], acc[rb][cb][3])};
          *(LAS u32x2*)(size_t)(yw + rb * 16 * 336 + cb * 32) = w;
        }
    }
    BAR();
    {
      const __amdgpu_buffer_rsrc_t rsP = __builtin_amdgcn_make_buffer_rsrc(ws + WS_P, (short)0, 2 * 8 * 4 * 8 * 30720, 0x00020000);
#pragma unroll
      for (int i = 0; i < 8; ++i) {
        const int j = wave + 8 * i;
        if (j < 60) {
          const int n = 64 * j + ln, row = n / 20, pc = n % 20, un = j >= 30 ? 1 : 0;
          const u32x4 v = *(const LAS u32x4*)(size_t)(L0 + row * 336 + pc * 16);
          __builtin_amdgcn_raw_buffer_store_b128(v, rsP, (((un * 8 + b0) * 4 + hq) * 8 + xj) * 30720 + (n - 1920 * un) * 16, 0, 16);
        }
      }
      WAITV(0);
      BAR();
      if (wave == 0) {
        if (ln < 8) __hip_atomic_store(FL + (size_t)((int)ln + 8 * xi) * 32 + 8 + xj, TAG, __ATOMIC_RELAXED, __HIP_MEMORY_SCOPE_AGENT);
        poll_flags(FL + (size_t)blockIdx.x * 32 + 8, 8, -1, TAG, ln);
      }
      BAR();
      STAMP(9);
      const int tz = wave * 64 + (int)ln;
      if (tz < 480) {
        const int u = tz / 240, e = tz % 240;
        const int pp = (((u * 8 + b0) * 4 + hq) * 8) * 30720 + 12 * xj * 320 + e * 16;
        u32x4 v[8];
#pragma unroll
        for (int j = 0; j < 8; ++j) v[j] = __builtin_amdgcn_raw_buffer_load_b128(rsP, pp + j * 30720, 0, 16);
        float a[8];
#pragma unroll
        for (int k = 0; k < 4; ++k) {
          float lo = 0.f, hi2 = 0.f;
#pragma unroll
          for (int j = 0; j < 8; ++j) { lo += __uint_as_float(v[j][k] << 16); hi2 += __uint_as_float(v[j][k] & 0xffff0000u); }
          a[2 * k] = lo; a[2 * k + 1] = hi2;
        }
        u32x4 w = {cvtpk(a[0], a[1]), cvtpk(a[2], a[3]), cvtpk(a[4], a[5]), cvtpk(a[6], a[7])};
        const int row = e / 20, c8 = e % 20;
        *(u32x4*)(out + (size_t)((b0 + 8 * u) * SQ + 12 * xj + row) * DM + hq * 160 + c8 * 8) = w;
      }
    }
    STAMP(10);
  }
}

extern "C" void kernel_launch(void* const* d_in, const int* in_sizes, int n_in, void* d_out, int out_size, void* d_ws,
                              size_t ws_size, hipStream_t stream) {
  const u16* x = (const u16*)d_in[0];
  const u16* kc = (const u16*)d_in[1];
  const u16* vc = (const u16*)d_in[2];
  const u16* wq = (const u16*)d_in[3];
  const u16* wo = (const u16*)d_in[4];
  (void)hipFuncSetAttribute((const void*)attn_kernel, hipFuncAttributeMaxDynamicSharedMemorySize, LDS_A);
  attn_kernel<<<256, 512, LDS_A, stream>>>(x, kc, vc, wq, wo, (u16*)d_out, (char*)d_ws, (unsigned long long*)((char*)d_ws + (128u << 20)));
}
```

```cpp
#include <hip/hip_runtime.h>
#include <hip/hip_bf16.h>
#include <stdint.h>
#include <type_traits>

typedef unsigned short u16;
typedef short bf16x8 __attribute__((ext_vector_type(8)));
typedef short s16x4 __attribute__((ext_vector_type(4)));
typedef float f32x16 __attribute__((ext_vector_type(16)));
typedef float f32x4 __attribute__((ext_vector_type(4)));
typedef float f32x2 __attribute__((ext_vector_type(2)));
typedef __bf16 bf16x2 __attribute__((ext_vector_type(2)));
typedef unsigned u32x4 __attribute__((ext_vector_type(4)));
typedef unsigned u32x2 __attribute__((ext_vector_type(2)));
#define LAS __attribute__((address_space(3)))
#define DI __device__ __forceinline__

constexpr int NB = 16, SQ = 96, DM = 640, NH = 32, SK = 1024, DH = 192;
constexpr int HD = NH * DH;

#define MFMA32(a, b, c) __builtin_amdgcn_mfma_f32_32x32x16_bf16((a), (b), (c), 0, 0, 0)
#define MFMA16(a, b, c) __builtin_amdgcn_mfma_f32_16x16x32_bf16((a), (b), (c), 0, 0, 0)
#define BAR() do { asm volatile("" ::: "memory"); __builtin_amdgcn_s_waitcnt(0xC07F); __builtin_amdgcn_s_barrier(); asm volatile("" ::: "memory"); } while (0)
#define WAITV(n) asm volatile("s_waitcnt vmcnt(%0)" ::"i"(n) : "memory")
#define SBAR() __builtin_amdgcn_sched_barrier(0)
#define MAX3(a, b, c) fmaxf(fmaxf((a), (b)), (c))
#define LANEID(v) asm volatile("v_mbcnt_lo_u32_b32 %0, -1, 0\n\tv_mbcnt_hi_u32_b32 %0, -1, %0" : "=v"(v))

DI unsigned cvtpk(float lo, float hi) {
  f32x2 v = {lo, hi};
  bf16x2 b = __builtin_convertvector(v, bf16x2);
  return __builtin_bit_cast(unsigned, b);
}
DI void dma16nt(unsigned lds_addr, const void* sbase, unsigned voff) {
  asm volatile("s_mov_b32 m0, %0\n\ts_nop 0\n\tglobal_load_lds_dwordx4 %1, %2 nt" ::"s"(lds_addr), "v"(voff), "s"(sbase) : "memory");
}
DI void bdma(unsigned lds_addr, __amdgpu_buffer_rsrc_t rs, unsigned voff, unsigned soff) {
  asm volatile("s_mov_b32 m0, %0\n\ts_nop 0\n\tbuffer_load_dwordx4 %1, %2, %3 offen lds" ::"s"(lds_addr), "v"(voff), "s"(rs), "s"(soff) : "memory");
}
DI void bdma1(unsigned lds_addr, __amdgpu_buffer_rsrc_t rs, unsigned voff, unsigned soff) {
  asm volatile("s_mov_b32 m0, %0\n\ts_nop 0\n\tbuffer_load_dwordx4 %1, %2, %3 offen sc1 lds" ::"s"(lds_addr), "v"(voff), "s"(rs), "s"(soff) : "memory");
}
DI void dma16(unsigned lds_addr, const void* sbase, unsigned voff) {
  asm volatile("s_mov_b32 m0, %0\n\ts_nop 0\n\tglobal_load_lds_dwordx4 %1, %2" ::"s"(lds_addr), "v"(voff), "s"(sbase) : "memory");
}
template <int I, int N, class F>
DI void sfor(F&& f) {
  if constexpr (I < N) { f(std::integral_constant<int, I>{}); sfor<I + 1, N>(f); }
}
DI bf16x8 lds128(unsigned a) { return *(const LAS bf16x8*)(size_t)a; }
DI s16x4 ldstr(unsigned a) { return __builtin_amdgcn_ds_read_tr16_b64_v4i16((LAS s16x4*)(size_t)a); }
DI bf16x8 cat(s16x4 a, s16x4 b) { return __builtin_shufflevector(a, b, 0, 1, 2, 3, 4, 5, 6, 7); }

constexpr unsigned ITB = 12288;
constexpr unsigned NSL = 5;
constexpr unsigned GRB = NSL * ITB;
constexpr unsigned SIDE = 2 * GRB;
constexpr unsigned MISC = SIDE + 36864;
constexpr unsigned CTL = MISC + 2048;
constexpr unsigned HB = MISC - 49152;
constexpr unsigned C_L = CTL;
constexpr unsigned C_R = CTL + 16;
constexpr unsigned C_QRDY = CTL + 48;
constexpr unsigned C_XF = CTL + 64;
constexpr unsigned C_H = CTL + 128;
constexpr unsigned C_OR = CTL + 160;
constexpr unsigned C_OD = CTL + 192;
constexpr unsigned LDS_A = 163840;
constexpr float CSC = 0.07216878364870322f * 1.4426950408889634f;
constexpr float THR_RAW = 8.0f / CSC;
constexpr int SPIN_MAX = 1 << 20;
#define HD_D 4
#define TL_D 4
#define KV0 2

#define STAMP(i)
#define EST(i)
#define FST(i)

DI void lds_post(unsigned a, unsigned v) {
  asm volatile("s_waitcnt lgkmcnt(0)\n\tds_write_b32 %0, %1" ::"v"(a), "v"(v) : "memory");
}
DI void lds_post_nw(unsigned a, unsigned v) {
  asm volatile("ds_write_b32 %0, %1" ::"v"(a), "v"(v) : "memory");
}
DI void lds_wait_ge(unsigned a, unsigned n) {
#pragma nounroll
  for (int it = 0; it < SPIN_MAX; ++it) {
    unsigned v;
    asm volatile("ds_read_b32 %0, %1\n\ts_waitcnt lgkmcnt(0)" : "=v"(v) : "v"(a) : "memory");
    if (__builtin_amdgcn_readfirstlane(v) >= n) break;
    __builtin_amdgcn_s_sleep(1);
  }
}
DI void lds_wait_min4(unsigned a, unsigned n) {
#pragma nounroll
  for (int it = 0; it < SPIN_MAX; ++it) {
    u32x4 d;
    asm volatile("ds_read_b128 %0, %1\n\ts_waitcnt lgkmcnt(0)" : "=v"(d) : "v"(a) : "memory");
    const unsigned mn = min(min(d[0], d[1]), min(d[2], d[3]));
    if (__builtin_amdgcn_readfirstlane(mn) >= n) break;
    __builtin_amdgcn_s_sleep(1);
  }
}
DI unsigned lds_rd1(unsigned a) {
  unsigned v;
  asm volatile("ds_read_b32 %0, %1\n\ts_waitcnt lgkmcnt(0)" : "=v"(v) : "v"(a) : "memory");
  return __builtin_amdgcn_readfirstlane(v);
}
DI unsigned lds_min3(unsigned a) {
  u32x4 d;
  asm volatile("ds_read_b128 %0, %1\n\ts_waitcnt lgkmcnt(0)" : "=v"(d) : "v"(a) : "memory");
  return __builtin_amdgcn_readfirstlane(min(min(d[0], d[1]), d[2]));
}
DI unsigned getvm() {
  unsigned sts;
  asm volatile("s_getreg_b32 %0, hwreg(HW_REG_IB_STS)" : "=s"(sts)::"memory");
  return (sts & 15u) | ((sts >> 18) & 0x30u);
}
DI unsigned lds_peek(unsigned a) { return *(volatile LAS unsigned*)(size_t)a; }

typedef __attribute__((address_space(1))) unsigned long long gu64;
extern "C" __device__ unsigned long long amd_dispatch_id() __asm("llvm.amdgcn.dispatch.id");
constexpr size_t WS_O = 0, WS_P = 32u << 20, WS_F = 96u << 20;

DI bool poll_flags(gu64* f, int nl, int skip, unsigned long long T, int lane) {
#pragma nounroll
  for (int it = 0; it < SPIN_MAX; ++it) {
    unsigned long long v = T;
    if (lane < nl && lane != skip) v = __hip_atomic_load(f + lane, __ATOMIC_RELAXED, __HIP_MEMORY_SCOPE_AGENT);
    if (__all(v == T)) {
      if (lane < nl && lane != skip) __hip_atomic_store(f + lane, 0ull, __ATOMIC_RELAXED, __HIP_MEMORY_SCOPE_AGENT);
      return true;
    }
    __builtin_amdgcn_s_sleep(2);
  }
  return false;
}

constexpr int hNL(int j) { return j < 20 ? 3 : 0; }
constexpr int hKV(int p) { return (p >= KV0 && p < KV0 + 12) ? 1 : 0; }
constexpr int hPend(int j) {
  int cnt = 0, first = 0;
  if (j < HD_D) { cnt = 3 * (HD_D - 1 - j); first = 0; }
  else { cnt = hKV(j - HD_D); first = j - HD_D + 1; }
  for (int p = first; p < j; ++p) cnt += hNL(p + HD_D) + hKV(p);
  return cnt;
}
constexpr int tNL(int j) { return j < 24 ? 3 : 0; }
constexpr int tPend(int j) {
  int cnt = 0, first = 0;
  if (j < TL_D) { cnt = 3 * (TL_D - 1 - j); first = 0; }
  else { cnt = 0; first = j - TL_D + 1; }
  for (int p = first; p < j; ++p) cnt += tNL(p + TL_D);
  return cnt;
}

__global__ __launch_bounds__(512, 2) void attn_kernel(const u16* x, const u16* kc, const u16* vc, const u16* wq, const u16* wo,
                                                      u16* out, char* ws, unsigned long long* stamps) {
  extern __shared__ __attribute__((aligned(16))) char lds[];
  const unsigned L0 = (unsigned)(size_t)(LAS char*)lds;
  const int tid = threadIdx.x, lane = tid & 63;
  const int wave = __builtin_amdgcn_readfirstlane(tid >> 6);
  const int xj = blockIdx.x & 7, xi = blockIdx.x >> 3;
  const int hq = xi & 3, h = 4 * xj + hq, b0 = xi >> 2;
  const size_t UB0 = (size_t)(b0 * NH + h) * SK * 384, UB1 = (size_t)((b0 + 8) * NH + h) * SK * 384;
  unsigned long long TAG;
  {
    const unsigned long long did = amd_dispatch_id(), dp = (unsigned long long)__builtin_amdgcn_dispatch_ptr();
    const unsigned t32 = (unsigned)(did * 0x9E3779B1ull) ^ (unsigned)(dp >> 6) * 0x85EBCA77u ^ (unsigned)(did >> 32);
    TAG = ((unsigned long long)(t32 ^ 0x5A17C3E9u) << 32) | t32;
  }
  const int qbase = xj + 32 * b0;
  gu64* FL = (gu64*)(ws + WS_F);
  const int kh = (0x1A >> wave) & 1, r = (0x102210 >> (4 * wave)) & 3;
  STAMP(0);
  bf16x8 qf[12];

  unsigned offK[6], offV[3];
  {
    if (wave == 6 && lane < 64) *(LAS unsigned*)(size_t)(L0 + CTL + lane * 4) = (lane == 46 || lane == 47) ? 1u : 0u;
    const int rot = (b0 * 5 / 4 + hq * 5 / 2) % 10;
    const int dh = wave & 1, rq = wave >> 1;
    const unsigned g = lane >> 4, q4 = (lane & 15) >> 2, p4 = lane & 3, l15 = lane & 15;
    const __amdgpu_buffer_rsrc_t rsW = __builtin_amdgcn_make_buffer_rsrc((void*)((const char*)wq + h * DH * 2), (short)0, DM * HD * 2 - h * DH * 2, 0x00020000);
    const __amdgpu_buffer_rsrc_t rsX = __builtin_amdgcn_make_buffer_rsrc((void*)((const char*)x + b0 * SQ * DM * 2), (short)0, (NB - b0) * SQ * DM * 2, 0x00020000);
    unsigned gA, gB, gC;
    {
      auto wpiece = [&](int p) {
        const int n = 64 * p + lane, row = n / 24, pc = n % 24;
        const int sw = ((row >> 1) & 1) | (((row >> 3) & 1) << 1);
        return (unsigned)(row * (HD * 2) + ((pc ^ (sw << 1)) * 16));
      };
      auto xpiece = [&](int p) {
        const int row = 8 * p + (lane >> 3);
        return (unsigned)(row * (DM * 2) + (((lane & 7) ^ ((row >> 1) & 7)) * 16));
      };
      gA = wpiece(wave);
      gB = wave < 4 ? wpiece(8 + wave) : xpiece(wave - 4);
      gC = xpiece(4 + wave);
    }
    unsigned hK[3], hV[3];
    constexpr unsigned WB = 3 * ITB, G1X = GRB + 2 * ITB, U1 = 8 * SQ * DM * 2;
    constexpr unsigned WSTEP = 32 * HD * 2;
    const unsigned mW = L0 + WB + wave * 1024;
    const unsigned mX = L0 + (4 + wave) * 1024;
    const unsigned s2 = ((q4 >> 1) & 1) | ((g & 1) << 1);
    unsigned E[4];
    const unsigned XA0 = L0 + (rq >> 1) * G1X + (48 * (rq & 1) + l15) * 128 + ((g ^ ((l15 >> 1) & 7)) * 16), XA1 = XA0 ^ 64;
    f32x4 acc[6][3] = {};
    bf16x8 af[2][6], bfr[2][3];
    unsigned xa;
    auto rdA = [&](int par, int c1, int db) {
      const unsigned a = E[db & 3] + (db >> 2) * 128 + (c1 & 3) * ITB;
      af[par][db] = cat(ldstr(a), ldstr(a + 4 * 384));
    };
    auto rdB = [&](int par, int rb) { bfr[par][rb] = lds128(xa + rb * 2048); };
    unsigned woff = (unsigned)(2 * rot) * WSTEP, xoff = (unsigned)rot * 128;
    auto advW = [&]() { woff += WSTEP; woff = woff == 20 * WSTEP ? 0u : woff; };
    auto advX = [&]() { xoff += 128; xoff = xoff == 1280 ? 0u : xoff; };
    auto request = [&](auto VV, int k, int wsl, unsigned xs, int un) {
      constexpr int V = decltype(VV)::value;
      if (k == 0) bdma(mW + wsl * ITB, rsW, gA, woff);
      if (k == 1) {
        if (V == 0) bdma(mW + 8192 + wsl * ITB, rsW, gB, woff);
        else bdma(mX - 8192 + un * G1X + xs, rsX, gB, xoff + un * U1);
      }
      if (k == 2) bdma(mX + un * G1X + xs, rsX, gC, xoff + un * U1);
    };
    unsigned xr = 2 * ITB;
    unsigned xw = 1 * ITB;
    auto body = [&](auto VV) {
      constexpr int V = decltype(VV)::value;
      if (V == 1) bdma(mX - 8192 + 2 * ITB, rsX, gB, xoff);
      bdma(mX + 2 * ITB, rsX, gC, xoff);
#pragma unroll
      for (int c = -4; c < 0; ++c) {
        if ((c & 1) != 0) advX();
        const unsigned xs = (unsigned)((((c + 1) >> 1) + 2 + 2) % 3) * ITB;
#pragma unroll
        for (int k = 0; k < 3; ++k) request(VV, k, c & 3, xs, (c + 1) & 1);
        advW();
      }
      SBAR();
      asm volatile("" ::: "memory");
#pragma unroll
    for (int j = 0; j < 6; ++j) {
      int n = 64 * j + lane, row = n / 24, pc = n % 24;
      offK[j] = row * 384 + ((pc ^ ((row >> 1) & 7)) * 16);
    }
#pragma unroll
    for (int j = 0; j < 3; ++j) {
      int n = 64 * j + lane, row = n / 24, pc = n % 24;
      offV[j] = row * 384 + ((pc ^ (((row >> 1) & 1) << 2)) * 16);
    }
#pragma unroll
    for (int j = 0; j < 3; ++j) {
      const int n = 64 * (3 * (wave & 3) + j) + lane, row = n / 24, pc = n % 24;
      hK[j] = row * 384 + ((pc ^ ((row >> 1) & 7)) * 16);
      hV[j] = row * 384 + ((pc ^ (((row >> 1) & 1) << 2)) * 16);
    }
#pragma unroll
      for (int j = 0; j < 4; ++j) E[j] = L0 + WB + (8 * g + q4) * 384 + 32 * ((unsigned)(6 * dh + j) ^ s2) + 8 * p4;
      WAITV(9);
      BAR();
      xa = XA0 + 2 * ITB;
#pragma unroll
      for (int rb = 0; rb < 3; ++rb) rdB(0, rb);
#pragma unroll
      for (int db = 0; db < 6; ++db) rdA(0, 0, db);
      WAITV(6);
      BAR();
      STAMP(4);
      auto phase = [&](int c, auto QQ, auto MODE) {
        constexpr int q = decltype(QQ)::value, mode = decltype(MODE)::value, cur = q & 1;
        if (cur == 1) {
          xr = xr == 2 * ITB ? 0 : xr + ITB;
          if (mode == 0) { xw = xw == 2 * ITB ? 0 : xw + ITB; advX(); }
        }
        xa = (cur == 1 ? XA0 : XA1) + xr;
        constexpr int R0 = V == 0 ? 0 : 5, D0 = V == 0 ? 10 : 0;
#pragma unroll
        for (int i = 0; i < 18; ++i) {
          acc[i / 3][i % 3] = MFMA16(af[cur][i / 3], bfr[cur][i % 3], acc[i / 3][i % 3]);
          SBAR();
          if (i >= R0 && i < R0 + 3) rdB(cur ^ 1, i - R0);
          if (i >= R0 + 3 && i < R0 + 9) rdA(cur ^ 1, q + 1, i - R0 - 3);
          if (mode == 0 && i >= D0 && i < D0 + 6 && !((i - D0) & 1)) request(VV, (i - D0) >> 1, q, xw, (q + 1) & 1);
          if (mode == 0 && i == D0 + 5) advW();
          if (mode == 2 && i >= D0 && i < D0 + 6 && !((i - D0) & 1)) {
            const int k = (i - D0) >> 1, li = c - 17;
            const unsigned dst = L0 + V * G1X + li * ITB + (3 * (wave & 3) + k) * 1024;
            if (li == 2) dma16nt(dst, (const char*)vc + (V ? UB1 : UB0), hV[k]);
            else dma16nt(dst, (const char*)kc + (V ? UB1 : UB0) + (size_t)li * ITB, hK[k]);
          }
          SBAR();
        }
        if (mode == 0) WAITV(6);
        if (mode == 1) WAITV(0);
        BAR();
      };
      using I0 = std::integral_constant<int, 0>; using I1 = std::integral_constant<int, 1>;
      using I2 = std::integral_constant<int, 2>; using I3 = std::integral_constant<int, 3>;
#pragma unroll 1
      for (int c = 0; c < 16; c += 4) {
        phase(c, I0{}, I0{});
        phase(c + 1, I1{}, I0{});
        phase(c + 2, I2{}, I0{});
        phase(c + 3, I3{}, I0{});
      }
      STAMP(5);
      phase(16, I0{}, I1{});
      phase(17, I1{}, I2{});
      phase(18, I2{}, I2{});
      phase(19, I3{}, I2{});
    };
    if (wave < 4) body(std::integral_constant<int, 0>{}); else body(std::integral_constant<int, 1>{});
    STAMP(11);
    {
      unsigned lz; LANEID(lz);
      const unsigned zg = lz >> 4, zl = lz & 15;
      const unsigned qimg = L0 + (wave < 4 ? SIDE : WB);
#pragma unroll
      for (int rb = 0; rb < 3; ++rb) {
        const int R16 = 3 * (rq & 1) + rb;
        const unsigned qb = qimg + ((R16 >> 1) * 12 + 6 * dh) * 1024 + (16 * (R16 & 1) + zl + 32 * (zg >> 1)) * 16 + 8 * (zg & 1);
#pragma unroll
        for (int db = 0; db < 6; ++db) {
          u32x2 w = {cvtpk(acc[db][rb][0], acc[db][rb][1]), cvtpk(acc[db][rb][2], acc[db][rb][3])};
          *(LAS u32x2*)(size_t)(qb + db * 1024) = w;
        }
      }
      BAR();
      if (wave < 6) {
#pragma unroll
        for (int ks = 0; ks < 12; ++ks) qf[ks] = lds128(L0 + (kh ? WB : SIDE) + ((r * 12 + ks) * 64 + lz) * 16);
      }
      BAR();
    }
  }
  STAMP(1);

  if (wave >= 6) {
    const int hw = wave - 6;
    __builtin_amdgcn_s_setprio(3);
    unsigned ll; LANEID(ll);
    const unsigned aL = L0 + C_L + 8 * hw;
    const char* sb = hw ? (const char*)vc : (const char*)kc;
    unsigned off[6];
#pragma unroll
    for (int q = 0; q < 6; ++q) off[q] = hw ? offV[q % 3] + (q / 3) * 3072 : offK[q];
    auto issue_item = [&](unsigned g_, unsigned tile, unsigned slot_) {
      const char* src = sb + (g_ ? UB1 : UB0) + (size_t)tile * ITB;
      const unsigned dst = L0 + g_ * GRB + slot_;
#pragma unroll
      for (int q = 0; q < 12; ++q) dma16nt(dst + q * 1024, src + (q / 6) * 6144, off[q % 6]);
    };
    {
      const unsigned p0 = 3 + hw, t0 = 2 - hw;
      issue_item(0, t0, p0 * ITB);
      issue_item(1, t0, ((p0 + 2) % 5) * ITB);
    }
    WAITV(30); lds_post_nw(L0 + C_H + 4 * wave, 1);
    WAITV(27); lds_post_nw(L0 + C_H + 4 * wave, 2);
    WAITV(24); lds_post_nw(L0 + C_H + 4 * wave, 3);
    if (hw == 0) {
      lds_wait_min4(L0 + C_H, 1); lds_post_nw(aL, 1);
      lds_wait_min4(L0 + C_H + 16, 1); lds_post_nw(aL + 4, 1);
      lds_wait_min4(L0 + C_H, 2); lds_post_nw(aL, 2);
      lds_wait_min4(L0 + C_H + 16, 2); lds_post_nw(aL + 4, 2);
    } else {
      lds_wait_min4(L0 + C_H, 3); lds_post_nw(aL, 1);
      lds_wait_min4(L0 + C_H + 16, 3); lds_post_nw(aL + 4, 1);
    }
    unsigned n0 = 3 - hw, n1 = 3 - hw;
    unsigned pub0 = 2 - hw, pub1 = 2 - hw;
    unsigned s0 = ((5 + hw) % 5) * ITB, s1 = ((5 + hw + 2) % 5) * ITB;
    unsigned fifo = 2, qn = 2, xtra = 4;
    {
      const char* wsrc = (const char*)wo + (size_t)(4 * xj * DH + 96 * b0 + 48 * hw) * (DM * 2) + hq * 320;
      const unsigned o0 = (ll >> 2) * (DM * 2) + min((ll & 3) * 128u, 316u);
#pragma unroll
      for (int z = 0; z < 3; ++z)
        asm volatile("s_mov_b32 m0, %0\n\ts_nop 0\n\tglobal_load_lds_dword %1, %2" ::"s"(L0 + MISC + 1536), "v"(o0 + z * 16 * DM * 2), "s"(wsrc) : "memory");
      const char* t0 = ll == 0 ? ws + WS_O + (size_t)blockIdx.x * 36864 : ll == 1 ? ws + WS_O + (size_t)(256 + blockIdx.x) * 36864
                     : ll == 2 ? ws + WS_P + (size_t)(((b0 * 4 + hq) * 8 + xj) * 30720) : ll == 3 ? ws + WS_P + (size_t)((((8 + b0) * 4 + hq) * 8 + xj) * 30720)
                     : ll == 4 ? ws + WS_F + (size_t)blockIdx.x * 256 : (const char*)out + (size_t)((b0 + 8 * (ll & 1)) * SQ + 12 * xj) * DM * 2;
      if (ll < 7)
        asm volatile("s_mov_b32 m0, %0\n\ts_nop 0\n\tglobal_load_lds_dword %1, off" ::"s"(L0 + MISC + 1536), "v"(t0) : "memory");
    }
    unsigned tix = 0, xt = 0;
    unsigned nissued = 2;
    const __amdgpu_buffer_rsrc_t rsWo = __builtin_amdgcn_make_buffer_rsrc((void*)((const char*)wo + 4 * xj * DH * DM * 2 + hq * 320), (short)0, HD * DM * 2 - (4 * xj * DH * DM * 2 + hq * 320), 0x00020000);
    unsigned go[5];
#pragma unroll
    for (int p = 0; p < 5; ++p) {
      const int n = 64 * p + ll, row = n / 20, pc = n % 20;
      go[p] = row * (DM * 2) + ((pc ^ (((row >> 3) & 1) << 1)) * 16);
    }
    auto adv2 = [&](unsigned s, unsigned d) { s += d * ITB; return s >= GRB ? s - GRB : s; };
    unsigned rr = 0;
#pragma nounroll
    for (int it = 0; it < (SPIN_MAX << 2); ++it) {
      bool idle = true;
      if (n0 < 32 || n1 < 32) {
        if (getvm() <= 48) {
          const unsigned p0 = hw ? min(2 * n0 + 2, 63u) : 2 * n0 - 1, p1 = hw ? min(2 * n1 + 2, 63u) : 2 * n1 - 1;
          const bool ok0 = n0 < 32 && lds_min3(L0 + C_R) + NSL > p0;
          const bool ok1 = n1 < 32 && lds_min3(L0 + C_R + 16) + NSL > p1;
          const bool take1 = ok1 && (!ok0 || rr);
          if (take1) {
            issue_item(1, n1, s1);
            ++n1; s1 = adv2(s1, (hw && n1 == 31) ? 1 : 2);
            fifo |= 1u << qn; ++qn; ++nissued; rr = 0; idle = false;
          } else if (ok0) {
            issue_item(0, n0, s0);
            ++n0; s0 = adv2(s0, (hw && n0 == 31) ? 1 : 2);
            ++qn; ++nissued; rr = 1; idle = false;
          }
        }
      } else if (tix < 5) {
        if (lds_min3(L0 + C_R) >= 60 + tix && getvm() <= 50) {
          const unsigned c0 = tix == 0 ? 5 : tix - 1, nc = tix == 4 ? 2 : 1;
#pragma unroll 1
          for (unsigned c6 = c0; c6 < c0 + nc; ++c6) {
#pragma unroll
            for (int p = 0; p < 5; ++p) bdma(L0 + c6 * 10240 + (5 * hw + p) * 1024, rsWo, go[p], (hq * DH + 32 * c6 + 16 * hw) * (DM * 2));
            xt += 5;
          }
          ++tix;
          idle = false;
        }
      }
      if (qn > 0) {
        const unsigned vm = getvm();
        bool any = false;
#pragma nounroll
        while (qn > 0) {
          const unsigned after = 12 * (qn - 1) + xt + ((nissued - qn) < 2 ? xtra : 0);
          if (vm > after) break;
          if (fifo & 1) ++pub1; else ++pub0;
          fifo >>= 1; --qn; any = true;
        }
        if (any) {
          lds_post_nw(aL, pub0);
          lds_post_nw(aL + 4, pub1);
          idle = false;
        }
      } else if (n0 == 32 && n1 == 32 && tix == 5) {
        break;
      }
      if (idle) __builtin_amdgcn_s_sleep(1);
    }
    __builtin_amdgcn_s_setprio(0);
  } else {
    unsigned ln; LANEID(ln);
    unsigned kof[4], vE, vO;
    {
      const unsigned a32 = ln & 31, ahi = ln >> 5, ablk = (ln >> 4) & 1, aq4 = (ln & 15) >> 2, ap4 = ln & 3, axq = (aq4 >> 1) & 1;
#pragma unroll
      for (int i = 0; i < 4; ++i) kof[i] = L0 + kh * GRB + a32 * 384 + (((2 * i + ahi) ^ ((a32 >> 1) & 7)) * 16);
      vE = L0 + kh * GRB + (4 * ahi + aq4) * 384 + 64 * axq + 32 * ablk + 8 * ap4;
      vO = L0 + kh * GRB + (4 * ahi + aq4) * 384 - 64 * axq + 32 * ablk + 8 * ap4;
    }
    const unsigned aFK = L0 + C_L + 4 * kh, aFV = L0 + C_L + 8 + 4 * kh, aR = L0 + C_R + 16 * kh + 4 * r;
    const int pw = (0x102354 >> (4 * wave)) & 7;
    const unsigned fme = L0 + C_XF + 8 * wave, fpa = L0 + C_XF + 8 * pw;
    unsigned cur = kh ? 2 * ITB : 0;
    unsigned cnt = 0;
#define ADV() do { cur = (cur + ITB == GRB) ? 0u : cur + ITB; ++cnt; } while (0)
#define KADDR(ks) (((ks) & 3) == 0 ? k0 : ((ks) & 3) == 1 ? k1 : ((ks) & 3) == 2 ? k2 : k3) + 128 * ((ks) >> 2)
#define VADDR(j) ((((j) % 6) & 1) ? vbO : vbE) + (16 * ((j) / 6)) * 384 + 64 * ((j) % 6)

    {
      float m, l = 0.f;
      f32x16 o[6] = {};
      f32x16 sB;
      float pe[16];
      bf16x8 kf[12], vf[12];
      unsigned k0, k1, k2, k3, vbE, vbO;
      WAITV(6); lds_post_nw(L0 + C_H + 4 * wave, 1);
      lds_wait_ge(aFK, 1);
      k0 = kof[0] + cur; k1 = kof[1] + cur; k2 = kof[2] + cur; k3 = kof[3] + cur;
#pragma unroll
      for (int ks = 0; ks < 12; ++ks) kf[ks] = lds128(KADDR(ks));
      ADV();
      lds_post_nw(aR, cnt);
      SBAR();
      {
        f32x16 z = {};
        sB = MFMA32(kf[0], qf[0], z);
#pragma unroll
        for (int ks = 1; ks < 12; ++ks) sB = MFMA32(kf[ks], qf[ks], sB);
      }
      SBAR();
      WAITV(3); lds_post_nw(L0 + C_H + 4 * wave, 2);
      lds_wait_ge(aFK, 2);
      WAITV(0); lds_post_nw(L0 + C_H + 4 * wave, 3);
      k0 = kof[0] + cur; k1 = kof[1] + cur; k2 = kof[2] + cur; k3 = kof[3] + cur;
#pragma unroll
      for (int ks = 0; ks < 4; ++ks) kf[ks] = lds128(KADDR(ks));
      {
        float pmax = sB[0];
#pragma unroll
        for (int i = 1; i < 16; ++i) pmax = fmaxf(pmax, sB[i]);
        auto rr = __builtin_amdgcn_permlane32_swap(__float_as_uint(pmax), __float_as_uint(pmax), false, false);
        m = fmaxf(__uint_as_float(rr[0]), __uint_as_float(rr[1]));
        const float mc = -m * CSC;
#pragma unroll
        for (int i = 0; i < 16; ++i) pe[i] = fmaf(sB[i], CSC, mc);
      }
      SBAR();
      bf16x8 pa[2];
      auto expstep = [&](int i) {
        pe[2 * i] = __builtin_amdgcn_exp2f(pe[2 * i]);
        pe[2 * i + 1] = __builtin_amdgcn_exp2f(pe[2 * i + 1]);
        l += pe[2 * i];
        l += pe[2 * i + 1];
        asm volatile("" : "+v"(pe[2 * i]), "+v"(pe[2 * i + 1]), "+v"(l));
      };
      auto cvtstep = [&](int s) {
        u32x4 w = {cvtpk(pe[8 * s], pe[8 * s + 1]), cvtpk(pe[8 * s + 2], pe[8 * s + 3]), cvtpk(pe[8 * s + 4], pe[8 * s + 5]),
                   cvtpk(pe[8 * s + 6], pe[8 * s + 7])};
        asm volatile("" : "+v"(w));
        pa[s] = __builtin_bit_cast(bf16x8, w);
      };
#pragma unroll 1
      for (int t = 0; t < 31; ++t) {
        unsigned fl = 0;
#pragma unroll
        for (int i = 0; i < 12; ++i) {
          if (i == 0) { f32x16 z = {}; sB = MFMA32(kf[0], qf[0], z); }
          else sB = MFMA32(kf[i], qf[i], sB);
          if (i < 8) kf[i + 4] = lds128(KADDR(i + 4));
          if (i == 2) fl = lds_peek(aFV);
          if (i == 7) { ADV(); lds_post_nw(aR, cnt); }
          if (i < 8) expstep(i);
          if (i == 8) {
            if (__builtin_amdgcn_readfirstlane(fl) < (unsigned)t + 1) lds_wait_ge(aFV, t + 1);
            vbE = vE + cur; vbO = vO + cur;
            cvtstep(0);
          }
          if (i == 9) cvtstep(1);
          if (i >= 8) vf[i - 8] = cat(ldstr(VADDR(i - 8)), ldstr(VADDR(i - 8) + 8 * 384));
          SBAR();
        }
        float pmax = 0.f, alpha = 1.f, mc = 0.f;
        bool resc = false;
#pragma unroll
        for (int j = 0; j < 12; ++j) {
          o[j % 6] = MFMA32(vf[j], pa[j / 6], o[j % 6]);
          if (j < 8) vf[j + 4] = cat(ldstr(VADDR(j + 4)), ldstr(VADDR(j + 4) + 8 * 384));
          if (j == 2) fl = lds_peek(aFK);
          if (j == 7) { ADV(); lds_post_nw(aR, cnt); }
          if (j == 1) {
            asm volatile("" : "+v"(sB));
            pmax = MAX3(MAX3(sB[0], sB[1], sB[2]), MAX3(sB[3], sB[4], sB[5]), MAX3(sB[6], sB[7], sB[8]));
            asm volatile("" : "+v"(pmax));
          }
          if (j == 2) {
            pmax = MAX3(MAX3(sB[9], sB[10], sB[11]), MAX3(sB[12], sB[13], sB[14]), MAX3(sB[15], sB[15], pmax));
            asm volatile("" : "+v"(pmax));
          }
          if (j == 3) {
            auto rr = __builtin_amdgcn_permlane32_swap(__float_as_uint(pmax), __float_as_uint(pmax), false, false);
            pmax = fmaxf(__uint_as_float(rr[0]), __uint_as_float(rr[1]));
            resc = pmax - m > THR_RAW;
            const float mn = resc ? pmax : m;
            alpha = __builtin_amdgcn_exp2f((m - mn) * CSC);
            m = mn;
            mc = -mn * CSC;
            l *= alpha;
            asm volatile("" : "+v"(m), "+v"(mc), "+v"(l), "+v"(alpha));
          }
          if (j >= 4 && j < 8) {
#pragma unroll
            for (int i = 4 * (j - 4); i < 4 * (j - 4) + 4; ++i) pe[i] = fmaf(sB[i], CSC, mc);
            asm volatile("" : "+v"(pe[4 * (j - 4)]), "+v"(pe[4 * (j - 4) + 1]), "+v"(pe[4 * (j - 4) + 2]), "+v"(pe[4 * (j - 4) + 3]));
          }
          if (j == 8) {
            if (t < 30 && __builtin_amdgcn_readfirstlane(fl) < (unsigned)t + 3) lds_wait_ge(aFK, t + 3);
            k0 = kof[0] + cur; k1 = kof[1] + cur; k2 = kof[2] + cur; k3 = kof[3] + cur;
          }
          if (j >= 8) kf[j - 8] = lds128(KADDR(j - 8));
          SBAR();
        }
        if (__any(resc)) {
#pragma unroll
          for (int d = 0; d < 6; ++d)
#pragma unroll
            for (int i = 0; i < 16; ++i) o[d][i] *= alpha;
        }
      }
      {
        lds_wait_ge(aFV, 32);
        vbE = vE + cur; vbO = vO + cur;
#pragma unroll
        for (int j = 0; j < 6; ++j) vf[j] = cat(ldstr(VADDR(j)), ldstr(VADDR(j) + 8 * 384));
        SBAR();
#pragma unroll
        for (int i = 0; i < 8; ++i) expstep(i);
        cvtstep(0);
        cvtstep(1);
        SBAR();
#pragma unroll
        for (int j = 0; j < 12; ++j) {
          o[j % 6] = MFMA32(vf[j], pa[j / 6], o[j % 6]);
          if (j < 6) vf[j + 6] = cat(ldstr(VADDR(j + 6)), ldstr(VADDR(j + 6) + 8 * 384));
          if (j == 5) { ADV(); lds_post_nw(aR, cnt); }
          SBAR();
        }
      }
      STAMP(6);
      {
        auto rr = __builtin_amdgcn_permlane32_swap(__float_as_uint(l), __float_as_uint(l), false, false);
        l = __uint_as_float(rr[0]) + __uint_as_float(rr[1]);
      }
      const float inv = 1.0f / l;
      unsigned lz; LANEID(lz);
      const int zc = lz & 31, zh = lz >> 5;
      const __amdgpu_buffer_rsrc_t rsO = __builtin_amdgcn_make_buffer_rsrc(ws + WS_O, (short)0, 2 * 256 * 36864, 0x00020000);
      const int obase = (kh * 256 + blockIdx.x) * 36864 + r * 2048 + (int)lz * 16;
      u32x4 wq_[6][2];
#pragma unroll
      for (int d = 0; d < 6; ++d)
#pragma unroll
        for (int i = 0; i < 4; i += 2) {
          unsigned pk[4];
#pragma unroll
          for (int j = 0; j < 2; ++j) {
            pk[2 * j] = cvtpk(o[d][4 * (i + j)] * inv, o[d][4 * (i + j) + 1] * inv);
            pk[2 * j + 1] = cvtpk(o[d][4 * (i + j) + 2] * inv, o[d][4 * (i + j) + 3] * inv);
          }
          auto r0 = __builtin_amdgcn_permlane32_swap(pk[0], pk[2], false, false);
          auto r1 = __builtin_amdgcn_permlane32_swap(pk[1], pk[3], false, false);
          wq_[d][i >> 1] = u32x4{r0[0], r1[0], r0[1], r1[1]};
          __builtin_amdgcn_raw_buffer_store_b128(wq_[d][i >> 1], rsO, obase + d * 6144 + (i >> 1) * 1024, 0, 16);
        }
#pragma nounroll
      for (int it = 0; it < SPIN_MAX; ++it) {
        if (lds_min3(L0 + C_R + 16) >= 64) break;
        __builtin_amdgcn_s_sleep(1);
      }
#pragma unroll
      for (int d = 0; d < 6; ++d)
#pragma unroll
        for (int i = 0; i < 2; ++i)
          *(LAS u32x4*)(size_t)(L0 + 61440 + (d < 5 ? (d + 1) % 5 : 5) * ITB + kh * 6144 + r * 2048 + i * 1024 + lz * 16) = wq_[d][i];
      STAMP(7);
    }
  }
  {
    unsigned ln; LANEID(ln);
    constexpr unsigned TWO = 10240, TOB = 61440;
    const int ch = wave & 1, rq = wave >> 1;
    const unsigned g = ln >> 4, q4 = (ln & 15) >> 2, p4 = ln & 3, l15 = ln & 15;
    unsigned gA, gB;
    {
      auto wpiece = [&](int p) {
        const int n = 64 * p + ln, row = n / 20, pc = n % 20;
        return (unsigned)(row * (DM * 2) + ((pc ^ (((row >> 3) & 1) << 1)) * 16));
      };
      gA = wpiece(wave);
      gB = wave < 2 ? wpiece(8 + wave) : ln * 16 + (wave - 2) * 1024;
    }
    const int pc3 = wave < 6 ? wave : wave - 2;
    const unsigned gC = ln * 16 + pc3 * 1024 + 256 * 36864;
    const __amdgpu_buffer_rsrc_t rsWo = __builtin_amdgcn_make_buffer_rsrc((void*)((const char*)wo + 4 * xj * DH * DM * 2 + hq * 320), (short)0, HD * DM * 2 - (4 * xj * DH * DM * 2 + hq * 320), 0x00020000);
    const __amdgpu_buffer_rsrc_t rsOt = __builtin_amdgcn_make_buffer_rsrc(ws + WS_O + (size_t)qbase * 36864, (short)0, (512 - qbase) * 36864, 0x00020000);
    const __amdgpu_buffer_rsrc_t rsB = wave < 2 ? rsWo : rsOt;
    const unsigned mA = L0 + wave * 1024, mC = L0 + TOB + 6144 + pc3 * 1024;
    const unsigned mB = wave < 2 ? L0 + (8 + wave) * 1024 : L0 + TOB + (wave - 2) * 1024;
    const unsigned stepB = wave < 2 ? 32 * DM * 2 : 6144;
    unsigned wsrc, osrc, bsrc;
    auto sethead = [&](int hs) { wsrc = hs * (DH * DM * 2); osrc = hs * (8 * 36864); bsrc = wave < 2 ? wsrc : osrc; };
    auto request = [&](int c6, int k) {
      if (k == 0) bdma(mA + c6 * TWO, rsWo, gA, wsrc + c6 * (32 * DM * 2));
      const unsigned os = (c6 < 5 ? (c6 + 1) % 5 : 5) * ITB;
      if (k == 1) bdma1(mB + (wave < 2 ? c6 * TWO : os), rsB, gB, bsrc + c6 * stepB);
      if (k == 2) bdma1(mC + os, rsOt, gC, osrc + c6 * 6144);
    };
    const unsigned WAb = L0 + (8 * g + q4) * 320 + 160 * ch + 8 * p4;
    const unsigned WA0 = WAb + ((ch & 1) ? -32 * (int)(g & 1) : 32 * (int)(g & 1));
    const unsigned WA1 = WAb + ((ch & 1) ? 32 * (int)(g & 1) : -32 * (int)(g & 1));
    unsigned OA[3];
#pragma unroll
    for (int rb = 0; rb < 3; ++rb) { const int t = 3 * rq + rb; OA[rb] = L0 + TOB + (t >> 1) * 2048 + (t & 1) * 256 + (g >> 1) * 1024 + (g & 1) * 512 + l15 * 16; }
    f32x4 acc[3][5] = {};
    bf16x8 af[2][5], bfr[2][3];
    auto rdA = [&](int par, int sl, int cb) {
      const unsigned a = ((cb & 1) ? WA1 : WA0) + sl * TWO + cb * 32;
      af[par][cb] = cat(ldstr(a), ldstr(a + 4 * 320));
    };
    auto rdB = [&](int par, int sl, int rb) { bfr[par][rb] = lds128(OA[rb] + (sl < 5 ? (sl + 1) % 5 : 5) * ITB); };
    if (wave >= 6) WAITV(10);
    BAR();
    auto tbody = [&](auto VV) {
      constexpr int V = decltype(VV)::value;
#pragma unroll
      for (int rb = 0; rb < 3; ++rb) rdB(0, 0, rb);
#pragma unroll
      for (int cb = 0; cb < 5; ++cb) rdA(0, 0, cb);
      BAR();
      STAMP(12);
      auto phase = [&](auto QQ, auto MODE) {
        constexpr int q = decltype(QQ)::value, mode = decltype(MODE)::value, cur = q & 1;
        constexpr int R0 = V == 0 ? 0 : 5, D0 = V == 0 ? 9 : 0;
#pragma unroll
        for (int i = 0; i < 15; ++i) {
          acc[i % 3][i / 3] = MFMA16(af[cur][i / 3], bfr[cur][i % 3], acc[i % 3][i / 3]);
          SBAR();
          if (i >= R0 && i < R0 + 3) rdB(cur ^ 1, (q + 1) % 6, i - R0);
          if (i >= R0 + 3 && i < R0 + 8) rdA(cur ^ 1, (q + 1) % 6, i - R0 - 3);
          if (mode == 0 && i >= D0 && i < D0 + 6 && !((i - D0) & 1)) request(q, (i - D0) >> 1);
          if (mode == 2 && q >= 3 && i >= D0 && i < D0 + 6) request(2 * (q - 3) + (i - D0) / 3, (i - D0) % 3);
          SBAR();
        }
        if (mode == 0) WAITV(12);
        if (mode == 1) { if (q == 0) WAITV(9); else if (q == 1) WAITV(6); else if (q == 2) WAITV(3); else WAITV(0); }
        if (mode == 2) {
          if (q == 0 && wave < 6) WAITV(0);
          if (q == 1 && wave >= 6) WAITV(0);
          if (q == 2 && wave == 7) poll_flags(FL + (size_t)blockIdx.x * 32, 4, hq, TAG, ln);
          if (q == 4) WAITV(9);
          if (q == 5) WAITV(12);
        }
        BAR();
        if (mode == 2 && q == 0 && wave == 0 && ln < 4 && (int)ln != hq)
          __hip_atomic_store(FL + (size_t)(qbase + 8 * ln) * 32 + hq, TAG, __ATOMIC_RELAXED, __HIP_MEMORY_SCOPE_AGENT);
      };
      auto head6 = [&](auto MODE) {
        phase(std::integral_constant<int, 0>{}, MODE);
        phase(std::integral_constant<int, 1>{}, MODE);
        phase(std::integral_constant<int, 2>{}, MODE);
        phase(std::integral_constant<int, 3>{}, MODE);
        phase(std::integral_constant<int, 4>{}, MODE);
        phase(std::integral_constant<int, 5>{}, MODE);
      };
      sethead((hq + 1) & 3);
      head6(std::integral_constant<int, 2>{});
#pragma unroll 1
      for (int i = 2; i < 4; ++i) {
        sethead((hq + i) & 3);
        head6(std::integral_constant<int, 0>{});
      }
      STAMP(13);
      head6(std::integral_constant<int, 1>{});
    };
    if (wave < 4) tbody(std::integral_constant<int, 0>{}); else tbody(std::integral_constant<int, 1>{});
    STAMP(8);
    {
      const unsigned yw = L0 + (48 * rq + l15) * 336 + (80 * ch + 4 * g) * 2;
#pragma unroll
      for (int rb = 0; rb < 3; ++rb)
#pragma unroll
        for (int cb = 0; cb < 5; ++cb) {
          u32x2 w = {cvtpk(acc[rb][cb][0], acc[rb][cb][1]), cvtpk(acc[rb][cb][2], acc[rb][cb][3])};
          *(LAS u32x2*)(size_t)(yw + rb * 16 * 336 + cb * 32) = w;
        }
    }
    BAR();
    {
      const __amdgpu_buffer_rsrc_t rsP = __builtin_amdgcn_make_buffer_rsrc(ws + WS_P, (short)0, 2 * 8 * 4 * 8 * 30720, 0x00020000);
#pragma unroll
      for (int i = 0; i < 8; ++i) {
        const int j = wave + 8 * i;
        if (j < 60) {
          const int n = 64 * j + ln, row = n / 20, pc = n % 20, un = j >= 30 ? 1 : 0;
          const u32x4 v = *(const LAS u32x4*)(size_t)(L0 + row * 336 + pc * 16);
          __builtin_amdgcn_raw_buffer_store_b128(v, rsP, (((un * 8 + b0) * 4 + hq) * 8 + xj) * 30720 + (n - 1920 * un) * 16, 0, 16);
        }
      }
      WAITV(0);
      BAR();
      if (wave == 0) {
        if (ln < 8) __hip_atomic_store(FL + (size_t)((int)ln + 8 * xi) * 32 + 8 + xj, TAG, __ATOMIC_RELAXED, __HIP_MEMORY_SCOPE_AGENT);
        poll_flags(FL + (size_t)blockIdx.x * 32 + 8, 8, -1, TAG, ln);
      }
      BAR();
      STAMP(9);
      const int tz = wave * 64 + (int)ln;
      if (tz < 480) {
        const int u = tz / 240, e = tz % 240;
        const int pp = (((u * 8 + b0) * 4 + hq) * 8) * 30720 + 12 * xj * 320 + e * 16;
        u32x4 v[8];
#pragma unroll
        for (int j = 0; j < 8; ++j) v[j] = __builtin_amdgcn_raw_buffer_load_b128(rsP, pp + j * 30720, 0, 16);
        float a[8];
#pragma unroll
        for (int k = 0; k < 4; ++k) {
          float lo = 0.f, hi2 = 0.f;
#pragma unroll
          for (int j = 0; j < 8; ++j) { lo += __uint_as_float(v[j][k] << 16); hi2 += __uint_as_float(v[j][k] & 0xffff0000u); }
          a[2 * k] = lo; a[2 * k + 1] = hi2;
        }
        u32x4 w = {cvtpk(a[0], a[1]), cvtpk(a[2], a[3]), cvtpk(a[4], a[5]), cvtpk(a[6], a[7])};
        const int row = e / 20, c8 = e % 20;
        *(u32x4*)(out + (size_t)((b0 + 8 * u) * SQ + 12 * xj + row) * DM + hq * 160 + c8 * 8) = w;
      }
    }
    STAMP(10);
  }
}

extern "C" void kernel_launch(void* const* d_in, const int* in_sizes, int n_in, void* d_out, int out_size, void* d_ws,
                              size_t ws_size, hipStream_t stream) {
  const u16* x = (const u16*)d_in[0];
  const u16* kc = (const u16*)d_in[1];
  const u16* vc = (const u16*)d_in[2];
  const u16* wq = (const u16*)d_in[3];
  const u16* wo = (const u16*)d_in[4];
  (void)hipFuncSetAttribute((const void*)attn_kernel, hipFuncAttributeMaxDynamicSharedMemorySize, LDS_A);
  attn_kernel<<<256, 512, LDS_A, stream>>>(x, kc, vc, wq, wo, (u16*)d_out, (char*)d_ws, (unsigned long long*)((char*)d_ws + (128u << 20)));
}
```

```cpp
#include <hip/hip_runtime.h>
#include <hip/hip_bf16.h>
#include <stdint.h>
#include <type_traits>

typedef unsigned short u16;
typedef short bf16x8 __attribute__((ext_vector_type(8)));
typedef short s16x4 __attribute__((ext_vector_type(4)));
typedef float f32x16 __attribute__((ext_vector_type(16)));
typedef float f32x4 __attribute__((ext_vector_type(4)));
typedef float f32x2 __attribute__((ext_vector_type(2)));
typedef __bf16 bf16x2 __attribute__((ext_vector_type(2)));
typedef unsigned u32x4 __attribute__((ext_vector_type(4)));
typedef unsigned u32x2 __attribute__((ext_vector_type(2)));
#define LAS __attribute__((address_space(3)))
#define DI __device__ __forceinline__

constexpr int NB = 16, SQ = 96, DM = 640, NH = 32, SK = 1024, DH = 192;
constexpr int HD = NH * DH;

#define MFMA32(a, b, c) __builtin_amdgcn_mfma_f32_32x32x16_bf16((a), (b), (c), 0, 0, 0)
#define MFMA16(a, b, c) __builtin_amdgcn_mfma_f32_16x16x32_bf16((a), (b), (c), 0, 0, 0)
#define BAR() do { asm volatile("" ::: "memory"); __builtin_amdgcn_s_waitcnt(0xC07F); __builtin_amdgcn_s_barrier(); asm volatile("" ::: "memory"); } while (0)
#define WAITV(n) asm volatile("s_waitcnt vmcnt(%0)" ::"i"(n) : "memory")
#define SBAR() __builtin_amdgcn_sched_barrier(0)
#define MAX3(a, b, c) fmaxf(fmaxf((a), (b)), (c))
#define LANEID(v) asm volatile("v_mbcnt_lo_u32_b32 %0, -1, 0\n\tv_mbcnt_hi_u32_b32 %0, -1, %0" : "=v"(v))

DI unsigned cvtpk(float lo, float hi) {
  f32x2 v = {lo, hi};
  bf16x2 b = __builtin_convertvector(v, bf16x2);
  return __builtin_bit_cast(unsigned, b);
}
DI void dma16nt(unsigned lds_addr, const void* sbase, unsigned voff) {
  asm volatile("s_mov_b32 m0, %0\n\ts_nop 0\n\tglobal_load_lds_dwordx4 %1, %2 nt" ::"s"(lds_addr), "v"(voff), "s"(sbase) : "memory");
}
DI void bdma(unsigned lds_addr, __amdgpu_buffer_rsrc_t rs, unsigned voff, unsigned soff) {
  asm volatile("s_mov_b32 m0, %0\n\ts_nop 0\n\tbuffer_load_dwordx4 %1, %2, %3 offen lds" ::"s"(lds_addr), "v"(voff), "s"(rs), "s"(soff) : "memory");
}
DI void bdma1(unsigned lds_addr, __amdgpu_buffer_rsrc_t rs, unsigned voff, unsigned soff) {
  asm volatile("s_mov_b32 m0, %0\n\ts_nop 0\n\tbuffer_load_dwordx4 %1, %2, %3 offen sc1 lds" ::"s"(lds_addr), "v"(voff), "s"(rs), "s"(soff) : "memory");
}
DI bf16x8 lds128(unsigned a) { return *(const LAS bf16x8*)(size_t)a; }
DI s16x4 ldstr(unsigned a) { return __builtin_amdgcn_ds_read_tr16_b64_v4i16((LAS s16x4*)(size_t)a); }
DI bf16x8 cat(s16x4 a, s16x4 b) { return __builtin_shufflevector(a, b, 0, 1, 2, 3, 4, 5, 6, 7); }

constexpr unsigned ITB = 12288;
constexpr unsigned NSL = 5;
constexpr unsigned GRB = NSL * ITB;
constexpr unsigned SIDE = 2 * GRB;
constexpr unsigned MISC = SIDE + 36864;
constexpr unsigned CTL = MISC + 2048;
constexpr unsigned C_L = CTL;
constexpr unsigned C_R = CTL + 16;
constexpr unsigned C_H = CTL + 128;
constexpr unsigned LDS_A = 163840;
constexpr float CSC = 0.07216878364870322f * 1.4426950408889634f;
constexpr float THR_RAW = 8.0f / CSC;
constexpr int SPIN_MAX = 1 << 20;
DI void lds_post_nw(unsigned a, unsigned v) {
  asm volatile("ds_write_b32 %0, %1" ::"v"(a), "v"(v) : "memory");
}
DI void lds_wait_ge(unsigned a, unsigned n) {
#pragma nounroll
  for (int it = 0; it < SPIN_MAX; ++it) {
    unsigned v;
    asm volatile("ds_read_b32 %0, %1\n\ts_waitcnt lgkmcnt(0)" : "=v"(v) : "v"(a) : "memory");
    if (__builtin_amdgcn_readfirstlane(v) >= n) break;
    __builtin_amdgcn_s_sleep(1);
  }
}
DI void lds_wait_min4(unsigned a, unsigned n) {
#pragma nounroll
  for (int it = 0; it < SPIN_MAX; ++it) {
    u32x4 d;
    asm volatile("ds_read_b128 %0, %1\n\ts_waitcnt lgkmcnt(0)" : "=v"(d) : "v"(a) : "memory");
    const unsigned mn = min(min(d[0], d[1]), min(d[2], d[3]));
    if (__builtin_amdgcn_readfirstlane(mn) >= n) break;
    __builtin_amdgcn_s_sleep(1);
  }
}
DI unsigned lds_min3(unsigned a) {
  u32x4 d;
  asm volatile("ds_read_b128 %0, %1\n\ts_waitcnt lgkmcnt(0)" : "=v"(d) : "v"(a) : "memory");
  return __builtin_amdgcn_readfirstlane(min(min(d[0], d[1]), d[2]));
}
DI unsigned getvm() {
  unsigned sts;
  asm volatile("s_getreg_b32 %0, hwreg(HW_REG_IB_STS)" : "=s"(sts)::"memory");
  return (sts & 15u) | ((sts >> 18) & 0x30u);
}
DI unsigned lds_peek(unsigned a) { return *(volatile LAS unsigned*)(size_t)a; }

typedef __attribute__((address_space(1))) unsigned long long gu64;
extern "C" __device__ unsigned long long amd_dispatch_id() __asm("llvm.amdgcn.dispatch.id");
constexpr size_t WS_O = 0, WS_P = 32u << 20, WS_F = 96u << 20;

DI bool poll_flags(gu64* f, int nl, int skip, unsigned long long T, int lane) {
#pragma nounroll
  for (int it = 0; it < SPIN_MAX; ++it) {
    unsigned long long v = T;
    if (lane < nl && lane != skip) v = __hip_atomic_load(f + lane, __ATOMIC_RELAXED, __HIP_MEMORY_SCOPE_AGENT);
    if (__all(v == T)) {
      if (lane < nl && lane != skip) __hip_atomic_store(f + lane, 0ull, __ATOMIC_RELAXED, __HIP_MEMORY_SCOPE_AGENT);
      return true;
    }
    __builtin_amdgcn_s_sleep(2);
  }
  return false;
}

__global__ __launch_bounds__(512, 2) void attn_kernel(const u16* x, const u16* kc, const u16* vc, const u16* wq, const u16* wo,
                                                      u16* out, char* ws) {
  extern __shared__ __attribute__((aligned(16))) char lds[];
  const unsigned L0 = (unsigned)(size_t)(LAS char*)lds;
  const int tid = threadIdx.x, lane = tid & 63;
  const int wave = __builtin_amdgcn_readfirstlane(tid >> 6);
  const int xj = blockIdx.x & 7, xi = blockIdx.x >> 3;
  const int hq = xi & 3, h = 4 * xj + hq, b0 = xi >> 2;
  const size_t UB0 = (size_t)(b0 * NH + h) * SK * 384, UB1 = (size_t)((b0 + 8) * NH + h) * SK * 384;
  unsigned long long TAG;
  {
    const unsigned long long did = amd_dispatch_id(), dp = (unsigned long long)__builtin_amdgcn_dispatch_ptr();
    const unsigned t32 = (unsigned)(did * 0x9E3779B1ull) ^ (unsigned)(dp >> 6) * 0x85EBCA77u ^ (unsigned)(did >> 32);
    TAG = ((unsigned long long)(t32 ^ 0x5A17C3E9u) << 32) | t32;
  }
  const int qbase = xj + 32 * b0;
  gu64* FL = (gu64*)(ws + WS_F);
  const int kh = (0x1A >> wave) & 1, r = (0x102210 >> (4 * wave)) & 3;
  bf16x8 qf[12];

  unsigned offK[6], offV[3];
  {
    if (wave == 6 && lane < 64) *(LAS unsigned*)(size_t)(L0 + CTL + lane * 4) = 0u;
    const int rot = (b0 * 5 / 4 + hq * 5 / 2) % 10;
    const int dh = wave & 1, rq = wave >> 1;
    const unsigned g = lane >> 4, q4 = (lane & 15) >> 2, p4 = lane & 3, l15 = lane & 15;
    const __amdgpu_buffer_rsrc_t rsW = __builtin_amdgcn_make_buffer_rsrc((void*)((const char*)wq + h * DH * 2), (short)0, DM * HD * 2 - h * DH * 2, 0x00020000);
    const __amdgpu_buffer_rsrc_t rsX = __builtin_amdgcn_make_buffer_rsrc((void*)((const char*)x + b0 * SQ * DM * 2), (short)0, (NB - b0) * SQ * DM * 2, 0x00020000);
    unsigned gA, gB, gC;
    {
      auto wpiece = [&](int p) {
        const int n = 64 * p + lane, row = n / 24, pc = n % 24;
        const int sw = ((row >> 1) & 1) | (((row >> 3) & 1) << 1);
        return (unsigned)(row * (HD * 2) + ((pc ^ (sw << 1)) * 16));
      };
      auto xpiece = [&](int p) {
        const int row = 8 * p + (lane >> 3);
        return (unsigned)(row * (DM * 2) + (((lane & 7) ^ ((row >> 1) & 7)) * 16));
      };
      gA = wpiece(wave);
      gB = wave < 4 ? wpiece(8 + wave) : xpiece(wave - 4);
      gC = xpiece(4 + wave);
    }
    unsigned hK[3], hV[3];
    constexpr unsigned WB = 3 * ITB, G1X = GRB + 2 * ITB, U1 = 8 * SQ * DM * 2;
    constexpr unsigned WSTEP = 32 * HD * 2;
    const unsigned mW = L0 + WB + wave * 1024;
    const unsigned mX = L0 + (4 + wave) * 1024;
    const unsigned s2 = ((q4 >> 1) & 1) | ((g & 1) << 1);
    unsigned E[4];
    const unsigned XA0 = L0 + (rq >> 1) * G1X + (48 * (rq & 1) + l15) * 128 + ((g ^ ((l15 >> 1) & 7)) * 16), XA1 = XA0 ^ 64;
    f32x4 acc[6][3] = {};
    bf16x8 af[2][6], bfr[2][3];
    unsigned xa;
    auto rdA = [&](int par, int c1, int db) {
      const unsigned a = E[db & 3] + (db >> 2) * 128 + (c1 & 3) * ITB;
      af[par][db] = cat(ldstr(a), ldstr(a + 4 * 384));
    };
    auto rdB = [&](int par, int rb) { bfr[par][rb] = lds128(xa + rb * 2048); };
    unsigned woff = (unsigned)(2 * rot) * WSTEP, xoff = (unsigned)rot * 128;
    auto advW = [&]() { woff += WSTEP; woff = woff == 20 * WSTEP ? 0u : woff; };
    auto advX = [&]() { xoff += 128; xoff = xoff == 1280 ? 0u : xoff; };
    auto request = [&](auto VV, int k, int wsl, unsigned xs, int un) {
      constexpr int V = decltype(VV)::value;
      if (k == 0) bdma(mW + wsl * ITB, rsW, gA, woff);
      if (k == 1) {
        if (V == 0) bdma(mW + 8192 + wsl * ITB, rsW, gB, woff);
        else bdma(mX - 8192 + un * G1X + xs, rsX, gB, xoff + un * U1);
      }
      if (k == 2) bdma(mX + un * G1X + xs, rsX, gC, xoff + un * U1);
    };
    unsigned xr = 2 * ITB;
    unsigned xw = 1 * ITB;
    auto body = [&](auto VV) {
      constexpr int V = decltype(VV)::value;
      if (V == 1) bdma(mX - 8192 + 2 * ITB, rsX, gB, xoff);
      bdma(mX + 2 * ITB, rsX, gC, xoff);
#pragma unroll
      for (int c = -4; c < 0; ++c) {
        if ((c & 1) != 0) advX();
        const unsigned xs = (unsigned)((((c + 1) >> 1) + 2 + 2) % 3) * ITB;
#pragma unroll
        for (int k = 0; k < 3; ++k) request(VV, k, c & 3, xs, (c + 1) & 1);
        advW();
      }
      SBAR();
      asm volatile("" ::: "memory");
#pragma unroll
    for (int j = 0; j < 6; ++j) {
      int n = 64 * j + lane, row = n / 24, pc = n % 24;
      offK[j] = row * 384 + ((pc ^ ((row >> 1) & 7)) * 16);
    }
#pragma unroll
    for (int j = 0; j < 3; ++j) {
      int n = 64 * j + lane, row = n / 24, pc = n % 24;
      offV[j] = row * 384 + ((pc ^ (((row >> 1) & 1) << 2)) * 16);
    }
#pragma unroll
    for (int j = 0; j < 3; ++j) {
      const int n = 64 * (3 * (wave & 3) + j) + lane, row = n / 24, pc = n % 24;
      hK[j] = row * 384 + ((pc ^ ((row >> 1) & 7)) * 16);
      hV[j] = row * 384 + ((pc ^ (((row >> 1) & 1) << 2)) * 16);
    }
#pragma unroll
      for (int j = 0; j < 4; ++j) E[j] = L0 + WB + (8 * g + q4) * 384 + 32 * ((unsigned)(6 * dh + j) ^ s2) + 8 * p4;
      WAITV(9);
      BAR();
      xa = XA0 + 2 * ITB;
#pragma unroll
      for (int rb = 0; rb < 3; ++rb) rdB(0, rb);
#pragma unroll
      for (int db = 0; db < 6; ++db) rdA(0, 0, db);
      WAITV(6);
      BAR();
      auto phase = [&](int c, auto QQ, auto MODE) {
        constexpr int q = decltype(QQ)::value, mode = decltype(MODE)::value, cur = q & 1;
        if (cur == 1) {
          xr = xr == 2 * ITB ? 0 : xr + ITB;
          if (mode == 0) { xw = xw == 2 * ITB ? 0 : xw + ITB; advX(); }
        }
        xa = (cur == 1 ? XA0 : XA1) + xr;
        constexpr int R0 = V == 0 ? 0 : 5, D0 = V == 0 ? 10 : 0;
#pragma unroll
        for (int i = 0; i < 18; ++i) {
          acc[i / 3][i % 3] = MFMA16(af[cur][i / 3], bfr[cur][i % 3], acc[i / 3][i % 3]);
          SBAR();
          if (i >= R0 && i < R0 + 3) rdB(cur ^ 1, i - R0);
          if (i >= R0 + 3 && i < R0 + 9) rdA(cur ^ 1, q + 1, i - R0 - 3);
          if (mode == 0 && i >= D0 && i < D0 + 6 && !((i - D0) & 1)) request(VV, (i - D0) >> 1, q, xw, (q + 1) & 1);
          if (mode == 0 && i == D0 + 5) advW();
          if (mode == 2 && i >= D0 && i < D0 + 6 && !((i - D0) & 1)) {
            const int k = (i - D0) >> 1, li = c - 17;
            const unsigned dst = L0 + V * G1X + li * ITB + (3 * (wave & 3) + k) * 1024;
            if (li == 2) dma16nt(dst, (const char*)vc + (V ? UB1 : UB0), hV[k]);
            else dma16nt(dst, (const char*)kc + (V ? UB1 : UB0) + (size_t)li * ITB, hK[k]);
          }
          SBAR();
        }
        if (mode == 0) WAITV(6);
        if (mode == 1) WAITV(0);
        BAR();
      };
      using I0 = std::integral_constant<int, 0>; using I1 = std::integral_constant<int, 1>;
      using I2 = std::integral_constant<int, 2>; using I3 = std::integral_constant<int, 3>;
#pragma unroll 1
      for (int c = 0; c < 16; c += 4) {
        phase(c, I0{}, I0{});
        phase(c + 1, I1{}, I0{});
        phase(c + 2, I2{}, I0{});
        phase(c + 3, I3{}, I0{});
      }
      phase(16, I0{}, I1{});
      phase(17, I1{}, I2{});
      phase(18, I2{}, I2{});
      phase(19, I3{}, I2{});
    };
    if (wave < 4) body(std::integral_constant<int, 0>{}); else body(std::integral_constant<int, 1>{});
    {
      unsigned lz; LANEID(lz);
      const unsigned zg = lz >> 4, zl = lz & 15;
      const unsigned qimg = L0 + (wave < 4 ? SIDE : WB);
#pragma unroll
      for (int rb = 0; rb < 3; ++rb) {
        const int R16 = 3 * (rq & 1) + rb;
        const unsigned qb = qimg + ((R16 >> 1) * 12 + 6 * dh) * 1024 + (16 * (R16 & 1) + zl + 32 * (zg >> 1)) * 16 + 8 * (zg & 1);
#pragma unroll
        for (int db = 0; db < 6; ++db) {
          u32x2 w = {cvtpk(acc[db][rb][0], acc[db][rb][1]), cvtpk(acc[db][rb][2], acc[db][rb][3])};
          *(LAS u32x2*)(size_t)(qb + db * 1024) = w;
        }
      }
      BAR();
      if (wave < 6) {
#pragma unroll
        for (int ks = 0; ks < 12; ++ks) qf[ks] = lds128(L0 + (kh ? WB : SIDE) + ((r * 12 + ks) * 64 + lz) * 16);
      }
      BAR();
    }
  }
  if (wave >= 6) {
    const int hw = wave - 6;
    __builtin_amdgcn_s_setprio(3);
    unsigned ll; LANEID(ll);
    const unsigned aL = L0 + C_L + 8 * hw;
    const char* sb = hw ? (const char*)vc : (const char*)kc;
    unsigned off[6];
#pragma unroll
    for (int q = 0; q < 6; ++q) off[q] = hw ? offV[q % 3] + (q / 3) * 3072 : offK[q];
    auto issue_item = [&](unsigned g_, unsigned tile, unsigned slot_) {
      const char* src = sb + (g_ ? UB1 : UB0) + (size_t)tile * ITB;
      const unsigned dst = L0 + g_ * GRB + slot_;
#pragma unroll
      for (int q = 0; q < 12; ++q) dma16nt(dst + q * 1024, src + (q / 6) * 6144, off[q % 6]);
    };
    {
      const unsigned p0 = 3 + hw, t0 = 2 - hw;
      issue_item(0, t0, p0 * ITB);
      issue_item(1, t0, ((p0 + 2) % 5) * ITB);
    }
    WAITV(30); lds_post_nw(L0 + C_H + 4 * wave, 1);
    WAITV(27); lds_post_nw(L0 + C_H + 4 * wave, 2);
    WAITV(24); lds_post_nw(L0 + C_H + 4 * wave, 3);
    if (hw == 0) {
      lds_wait_min4(L0 + C_H, 1); lds_post_nw(aL, 1);
      lds_wait_min4(L0 + C_H + 16, 1); lds_post_nw(aL + 4, 1);
      lds_wait_min4(L0 + C_H, 2); lds_post_nw(aL, 2);
      lds_wait_min4(L0 + C_H + 16, 2); lds_post_nw(aL + 4, 2);
    } else {
      lds_wait_min4(L0 + C_H, 3); lds_post_nw(aL, 1);
      lds_wait_min4(L0 + C_H + 16, 3); lds_post_nw(aL + 4, 1);
    }
    unsigned n0 = 3 - hw, n1 = 3 - hw;
    unsigned pub0 = 2 - hw, pub1 = 2 - hw;
    unsigned s0 = ((5 + hw) % 5) * ITB, s1 = ((5 + hw + 2) % 5) * ITB;
    unsigned fifo = 2, qn = 2, xtra = 4;
    {
      const char* wsrc = (const char*)wo + (size_t)(4 * xj * DH + 96 * b0 + 48 * hw) * (DM * 2) + hq * 320;
      const unsigned o0 = (ll >> 2) * (DM * 2) + min((ll & 3) * 128u, 316u);
#pragma unroll
      for (int z = 0; z < 3; ++z)
        asm volatile("s_mov_b32 m0, %0\n\ts_nop 0\n\tglobal_load_lds_dword %1, %2" ::"s"(L0 + MISC + 1536), "v"(o0 + z * 16 * DM * 2), "s"(wsrc) : "memory");
      const char* t0 = ll == 0 ? ws + WS_O + (size_t)blockIdx.x * 36864 : ll == 1 ? ws + WS_O + (size_t)(256 + blockIdx.x) * 36864
                     : ll == 2 ? ws + WS_P + (size_t)(((b0 * 4 + hq) * 8 + xj) * 30720) : ll == 3 ? ws + WS_P + (size_t)((((8 + b0) * 4 + hq) * 8 + xj) * 30720)
                     : ll == 4 ? ws + WS_F + (size_t)blockIdx.x * 256 : (const char*)out + (size_t)((b0 + 8 * (ll & 1)) * SQ + 12 * xj) * DM * 2;
      if (ll < 7)
        asm volatile("s_mov_b32 m0, %0\n\ts_nop 0\n\tglobal_load_lds_dword %1, off" ::"s"(L0 + MISC + 1536), "v"(t0) : "memory");
    }
    unsigned tix = 0, xt = 0;
    unsigned nissued = 2;
    const __amdgpu_buffer_rsrc_t rsWo = __builtin_amdgcn_make_buffer_rsrc((void*)((const char*)wo + 4 * xj * DH * DM * 2 + hq * 320), (short)0, HD * DM * 2 - (4 * xj * DH * DM * 2 + hq * 320), 0x00020000);
    unsigned go[5];
#pragma unroll
    for (int p = 0; p < 5; ++p) {
      const int n = 64 * p + ll, row = n / 20, pc = n % 20;
      go[p] = row * (DM * 2) + ((pc ^ (((row >> 3) & 1) << 1)) * 16);
    }
    auto adv2 = [&](unsigned s, unsigned d) { s += d * ITB; return s >= GRB ? s - GRB : s; };
    unsigned rr = 0;
#pragma nounroll
    for (int it = 0; it < (SPIN_MAX << 2); ++it) {
      bool idle = true;
      if (n0 < 32 || n1 < 32) {
        if (getvm() <= 48) {
          const unsigned p0 = hw ? min(2 * n0 + 2, 63u) : 2 * n0 - 1, p1 = hw ? min(2 * n1 + 2, 63u) : 2 * n1 - 1;
          const bool ok0 = n0 < 32 && lds_min3(L0 + C_R) + NSL > p0;
          const bool ok1 = n1 < 32 && lds_min3(L0 + C_R + 16) + NSL > p1;
          const bool take1 = ok1 && (!ok0 || rr);
          if (take1) {
            issue_item(1, n1, s1);
            ++n1; s1 = adv2(s1, (hw && n1 == 31) ? 1 : 2);
            fifo |= 1u << qn; ++qn; ++nissued; rr = 0; idle = false;
          } else if (ok0) {
            issue_item(0, n0, s0);
            ++n0; s0 = adv2(s0, (hw && n0 == 31) ? 1 : 2);
            ++qn; ++nissued; rr = 1; idle = false;
          }
        }
      } else if (tix < 5) {
        if (lds_min3(L0 + C_R) >= 60 + tix && getvm() <= 50) {
          const unsigned c0 = tix == 0 ? 5 : tix - 1, nc = tix == 4 ? 2 : 1;
#pragma unroll 1
          for (unsigned c6 = c0; c6 < c0 + nc; ++c6) {
#pragma unroll
            for (int p = 0; p < 5; ++p) bdma(L0 + c6 * 10240 + (5 * hw + p) * 1024, rsWo, go[p], (hq * DH + 32 * c6 + 16 * hw) * (DM * 2));
            xt += 5;
          }
          ++tix;
          idle = false;
        }
      }
      if (qn > 0) {
        const unsigned vm = getvm();
        bool any = false;
#pragma nounroll
        while (qn > 0) {
          const unsigned after = 12 * (qn - 1) + xt + ((nissued - qn) < 2 ? xtra : 0);
          if (vm > after) break;
          if (fifo & 1) ++pub1; else ++pub0;
          fifo >>= 1; --qn; any = true;
        }
        if (any) {
          lds_post_nw(aL, pub0);
          lds_post_nw(aL + 4, pub1);
          idle = false;
        }
      } else if (n0 == 32 && n1 == 32 && tix == 5) {
        break;
      }
      if (idle) __builtin_amdgcn_s_sleep(1);
    }
    __builtin_amdgcn_s_setprio(0);
  } else {
    unsigned ln; LANEID(ln);
    unsigned kof[4], vE, vO;
    {
      const unsigned a32 = ln & 31, ahi = ln >> 5, ablk = (ln >> 4) & 1, aq4 = (ln & 15) >> 2, ap4 = ln & 3, axq = (aq4 >> 1) & 1;
#pragma unroll
      for (int i = 0; i < 4; ++i) kof[i] = L0 + kh * GRB + a32 * 384 + (((2 * i + ahi) ^ ((a32 >> 1) & 7)) * 16);
      vE = L0 + kh * GRB + (4 * ahi + aq4) * 384 + 64 * axq + 32 * ablk + 8 * ap4;
      vO = L0 + kh * GRB + (4 * ahi + aq4) * 384 - 64 * axq + 32 * ablk + 8 * ap4;
    }
    const unsigned aFK = L0 + C_L + 4 * kh, aFV = L0 + C_L + 8 + 4 * kh, aR = L0 + C_R + 16 * kh + 4 * r;
    unsigned cur = kh ? 2 * ITB : 0;
    unsigned cnt = 0;
#define ADV() do { cur = (cur + ITB == GRB) ? 0u : cur + ITB; ++cnt; } while (0)
#define KADDR(ks) (((ks) & 3) == 0 ? k0 : ((ks) & 3) == 1 ? k1 : ((ks) & 3) == 2 ? k2 : k3) + 128 * ((ks) >> 2)
#define VADDR(j) ((((j) % 6) & 1) ? vbO : vbE) + (16 * ((j) / 6)) * 384 + 64 * ((j) % 6)

    {
      float m, l = 0.f;
      f32x16 o[6] = {};
      f32x16 sB;
      float pe[16];
      bf16x8 kf[12], vf[12];
      unsigned k0, k1, k2, k3, vbE, vbO;
      WAITV(6); lds_post_nw(L0 + C_H + 4 * wave, 1);
      lds_wait_ge(aFK, 1);
      k0 = kof[0] + cur; k1 = kof[1] + cur; k2 = kof[2] + cur; k3 = kof[3] + cur;
#pragma unroll
      for (int ks = 0; ks < 12; ++ks) kf[ks] = lds128(KADDR(ks));
      ADV();
      lds_post_nw(aR, cnt);
      SBAR();
      {
        f32x16 z = {};
        sB = MFMA32(kf[0], qf[0], z);
#pragma unroll
        for (int ks = 1; ks < 12; ++ks) sB = MFMA32(kf[ks], qf[ks], sB);
      }
      SBAR();
      WAITV(3); lds_post_nw(L0 + C_H + 4 * wave, 2);
      lds_wait_ge(aFK, 2);
      WAITV(0); lds_post_nw(L0 + C_H + 4 * wave, 3);
      k0 = kof[0] + cur; k1 = kof[1] + cur; k2 = kof[2] + cur; k3 = kof[3] + cur;
#pragma unroll
      for (int ks = 0; ks < 4; ++ks) kf[ks] = lds128(KADDR(ks));
      {
        float pmax = sB[0];
#pragma unroll
        for (int i = 1; i < 16; ++i) pmax = fmaxf(pmax, sB[i]);
        auto rr = __builtin_amdgcn_permlane32_swap(__float_as_uint(pmax), __float_as_uint(pmax), false, false);
        m = fmaxf(__uint_as_float(rr[0]), __uint_as_float(rr[1]));
        const float mc = -m * CSC;
#pragma unroll
        for (int i = 0; i < 16; ++i) pe[i] = fmaf(sB[i], CSC, mc);
      }
      SBAR();
      bf16x8 pa[2];
      auto expstep = [&](int i) {
        pe[2 * i] = __builtin_amdgcn_exp2f(pe[2 * i]);
        pe[2 * i + 1] = __builtin_amdgcn_exp2f(pe[2 * i + 1]);
        l += pe[2 * i];
        l += pe[2 * i + 1];
        asm volatile("" : "+v"(pe[2 * i]), "+v"(pe[2 * i + 1]), "+v"(l));
      };
      auto cvtstep = [&](int s) {
        u32x4 w = {cvtpk(pe[8 * s], pe[8 * s + 1]), cvtpk(pe[8 * s + 2], pe[8 * s + 3]), cvtpk(pe[8 * s + 4], pe[8 * s + 5]),
                   cvtpk(pe[8 * s + 6], pe[8 * s + 7])};
        asm volatile("" : "+v"(w));
        pa[s] = __builtin_bit_cast(bf16x8, w);
      };
#pragma unroll 1
      for (int t = 0; t < 31; ++t) {
        unsigned fl = 0;
#pragma unroll
        for (int i = 0; i < 12; ++i) {
          if (i == 0) { f32x16 z = {}; sB = MFMA32(kf[0], qf[0], z); }
          else sB = MFMA32(kf[i], qf[i], sB);
          if (i < 8) kf[i + 4] = lds128(KADDR(i + 4));
          if (i == 2) fl = lds_peek(aFV);
          if (i == 7) { ADV(); lds_post_nw(aR, cnt); }
          if (i < 8) expstep(i);
          if (i == 8) {
            if (__builtin_amdgcn_readfirstlane(fl) < (unsigned)t + 1) lds_wait_ge(aFV, t + 1);
            vbE = vE + cur; vbO = vO + cur;
            cvtstep(0);
          }
          if (i == 9) cvtstep(1);
          if (i >= 8) vf[i - 8] = cat(ldstr(VADDR(i - 8)), ldstr(VADDR(i - 8) + 8 * 384));
          SBAR();
        }
        float pmax = 0.f, alpha = 1.f, mc = 0.f;
        bool resc = false;
#pragma unroll
        for (int j = 0; j < 12; ++j) {
          o[j % 6] = MFMA32(vf[j], pa[j / 6], o[j % 6]);
          if (j < 8) vf[j + 4] = cat(ldstr(VADDR(j + 4)), ldstr(VADDR(j + 4) + 8 * 384));
          if (j == 2) fl = lds_peek(aFK);
          if (j == 7) { ADV(); lds_post_nw(aR, cnt); }
          if (j == 1) {
            asm volatile("" : "+v"(sB));
            pmax = MAX3(MAX3(sB[0], sB[1], sB[2]), MAX3(sB[3], sB[4], sB[5]), MAX3(sB[6], sB[7], sB[8]));
            asm volatile("" : "+v"(pmax));
          }
          if (j == 2) {
            pmax = MAX3(MAX3(sB[9], sB[10], sB[11]), MAX3(sB[12], sB[13], sB[14]), MAX3(sB[15], sB[15], pmax));
            asm volatile("" : "+v"(pmax));
          }
          if (j == 3) {
            auto rr = __builtin_amdgcn_permlane32_swap(__float_as_uint(pmax), __float_as_uint(pmax), false, false);
            pmax = fmaxf(__uint_as_float(rr[0]), __uint_as_float(rr[1]));
            resc = pmax - m > THR_RAW;
            const float mn = resc ? pmax : m;
            alpha = __builtin_amdgcn_exp2f((m - mn) * CSC);
            m = mn;
            mc = -mn * CSC;
            l *= alpha;
            asm volatile("" : "+v"(m), "+v"(mc), "+v"(l), "+v"(alpha));
          }
          if (j >= 4 && j < 8) {
#pragma unroll
            for (int i = 4 * (j - 4); i < 4 * (j - 4) + 4; ++i) pe[i] = fmaf(sB[i], CSC, mc);
            asm volatile("" : "+v"(pe[4 * (j - 4)]), "+v"(pe[4 * (j - 4) + 1]), "+v"(pe[4 * (j - 4) + 2]), "+v"(pe[4 * (j - 4) + 3]));
          }
          if (j == 8) {
            if (t < 30 && __builtin_amdgcn_readfirstlane(fl) < (unsigned)t + 3) lds_wait_ge(aFK, t + 3);
            k0 = kof[0] + cur; k1 = kof[1] + cur; k2 = kof[2] + cur; k3 = kof[3] + cur;
          }
          if (j >= 8) kf[j - 8] = lds128(KADDR(j - 8));
          SBAR();
        }
        if (__any(resc)) {
#pragma unroll
          for (int d = 0; d < 6; ++d)
#pragma unroll
            for (int i = 0; i < 16; ++i) o[d][i] *= alpha;
        }
      }
      {
        lds_wait_ge(aFV, 32);
        vbE = vE + cur; vbO = vO + cur;
#pragma unroll
        for (int j = 0; j < 6; ++j) vf[j] = cat(ldstr(VADDR(j)), ldstr(VADDR(j) + 8 * 384));
        SBAR();
#pragma unroll
        for (int i = 0; i < 8; ++i) expstep(i);
        cvtstep(0);
        cvtstep(1);
        SBAR();
#pragma unroll
        for (int j = 0; j < 12; ++j) {
          o[j % 6] = MFMA32(vf[j], pa[j / 6], o[j % 6]);
          if (j < 6) vf[j + 6] = cat(ldstr(VADDR(j + 6)), ldstr(VADDR(j + 6) + 8 * 384));
          if (j == 5) { ADV(); lds_post_nw(aR, cnt); }
          SBAR();
        }
      }
      {
        auto rr = __builtin_amdgcn_permlane32_swap(__float_as_uint(l), __float_as_uint(l), false, false);
        l = __uint_as_float(rr[0]) + __uint_as_float(rr[1]);
      }
      const float inv = 1.0f / l;
      unsigned lz; LANEID(lz);
      const int zc = lz & 31, zh = lz >> 5;
      const __amdgpu_buffer_rsrc_t rsO = __builtin_amdgcn_make_buffer_rsrc(ws + WS_O, (short)0, 2 * 256 * 36864, 0x00020000);
      const int obase = (kh * 256 + blockIdx.x) * 36864 + r * 2048 + (int)lz * 16;
      u32x4 wq_[6][2];
#pragma unroll
      for (int d = 0; d < 6; ++d)
#pragma unroll
        for (int i = 0; i < 4; i += 2) {
          unsigned pk[4];
#pragma unroll
          for (int j = 0; j < 2; ++j) {
            pk[2 * j] = cvtpk(o[d][4 * (i + j)] * inv, o[d][4 * (i + j) + 1] * inv);
            pk[2 * j + 1] = cvtpk(o[d][4 * (i + j) + 2] * inv, o[d][4 * (i + j) + 3] * inv);
          }
          auto r0 = __builtin_amdgcn_permlane32_swap(pk[0], pk[2], false, false);
          auto r1 = __builtin_amdgcn_permlane32_swap(pk[1], pk[3], false, false);
          wq_[d][i >> 1] = u32x4{r0[0], r1[0], r0[1], r1[1]};
          __builtin_amdgcn_raw_buffer_store_b128(wq_[d][i >> 1], rsO, obase + d * 6144 + (i >> 1) * 1024, 0, 16);
        }
#pragma nounroll
      for (int it = 0; it < SPIN_MAX; ++it) {
        if (lds_min3(L0 + C_R + 16) >= 64) break;
        __builtin_amdgcn_s_sleep(1);
      }
#pragma unroll
      for (int d = 0; d < 6; ++d)
#pragma unroll
        for (int i = 0; i < 2; ++i)
          *(LAS u32x4*)(size_t)(L0 + 61440 + (d < 5 ? (d + 1) % 5 : 5) * ITB + kh * 6144 + r * 2048 + i * 1024 + lz * 16) = wq_[d][i];
    }
  }
  {
    unsigned ln; LANEID(ln);
    constexpr unsigned TWO = 10240, TOB = 61440;
    const int ch = wave & 1, rq = wave >> 1;
    const unsigned g = ln >> 4, q4 = (ln & 15) >> 2, p4 = ln & 3, l15 = ln & 15;
    unsigned gA, gB;
    {
      auto wpiece = [&](int p) {
        const int n = 64 * p + ln, row = n / 20, pc = n % 20;
        return (unsigned)(row * (DM * 2) + ((pc ^ (((row >> 3) & 1) << 1)) * 16));
      };
      gA = wpiece(wave);
      gB = wave < 2 ? wpiece(8 + wave) : ln * 16 + (wave - 2) * 1024;
    }
    const int pc3 = wave < 6 ? wave : wave - 2;
    const unsigned gC = ln * 16 + pc3 * 1024 + 256 * 36864;
    const __amdgpu_buffer_rsrc_t rsWo = __builtin_amdgcn_make_buffer_rsrc((void*)((const char*)wo + 4 * xj * DH * DM * 2 + hq * 320), (short)0, HD * DM * 2 - (4 * xj * DH * DM * 2 + hq * 320), 0x00020000);
    const __amdgpu_buffer_rsrc_t rsOt = __builtin_amdgcn_make_buffer_rsrc(ws + WS_O + (size_t)qbase * 36864, (short)0, (512 - qbase) * 36864, 0x00020000);
    const __amdgpu_buffer_rsrc_t rsB = wave < 2 ? rsWo : rsOt;
    const unsigned mA = L0 + wave * 1024, mC = L0 + TOB + 6144 + pc3 * 1024;
    const unsigned mB = wave < 2 ? L0 + (8 + wave) * 1024 : L0 + TOB + (wave - 2) * 1024;
    const unsigned stepB = wave < 2 ? 32 * DM * 2 : 6144;
    unsigned wsrc, osrc, bsrc;
    auto sethead = [&](int hs) { wsrc = hs * (DH * DM * 2); osrc = hs * (8 * 36864); bsrc = wave < 2 ? wsrc : osrc; };
    auto request = [&](int c6, int k) {
      if (k == 0) bdma(mA + c6 * TWO, rsWo, gA, wsrc + c6 * (32 * DM * 2));
      const unsigned os = (c6 < 5 ? (c6 + 1) % 5 : 5) * ITB;
      if (k == 1) bdma1(mB + (wave < 2 ? c6 * TWO : os), rsB, gB, bsrc + c6 * stepB);
      if (k == 2) bdma1(mC + os, rsOt, gC, osrc + c6 * 6144);
    };
    const unsigned WAb = L0 + (8 * g + q4) * 320 + 160 * ch + 8 * p4;
    const unsigned WA0 = WAb + ((ch & 1) ? -32 * (int)(g & 1) : 32 * (int)(g & 1));
    const unsigned WA1 = WAb + ((ch & 1) ? 32 * (int)(g & 1) : -32 * (int)(g & 1));
    unsigned OA[3];
#pragma unroll
    for (int rb = 0; rb < 3; ++rb) { const int t = 3 * rq + rb; OA[rb] = L0 + TOB + (t >> 1) * 2048 + (t & 1) * 256 + (g >> 1) * 1024 + (g & 1) * 512 + l15 * 16; }
    f32x4 acc[3][5] = {};
    bf16x8 af[2][5], bfr[2][3];
    auto rdA = [&](int par, int sl, int cb) {
      const unsigned a = ((cb & 1) ? WA1 : WA0) + sl * TWO + cb * 32;
      af[par][cb] = cat(ldstr(a), ldstr(a + 4 * 320));
    };
    auto rdB = [&](int par, int sl, int rb) { bfr[par][rb] = lds128(OA[rb] + (sl < 5 ? (sl + 1) % 5 : 5) * ITB); };
    if (wave >= 6) WAITV(10);
    BAR();
    auto tbody = [&](auto VV) {
      constexpr int V = decltype(VV)::value;
#pragma unroll
      for (int rb = 0; rb < 3; ++rb) rdB(0, 0, rb);
#pragma unroll
      for (int cb = 0; cb < 5; ++cb) rdA(0, 0, cb);
      BAR();
      auto phase = [&](auto QQ, auto MODE) {
        constexpr int q = decltype(QQ)::value, mode = decltype(MODE)::value, cur = q & 1;
        constexpr int R0 = V == 0 ? 0 : 5, D0 = V == 0 ? 9 : 0;
#pragma unroll
        for (int i = 0; i < 15; ++i) {
          acc[i % 3][i / 3] = MFMA16(af[cur][i / 3], bfr[cur][i % 3], acc[i % 3][i / 3]);
          SBAR();
          if (i >= R0 && i < R0 + 3) rdB(cur ^ 1, (q + 1) % 6, i - R0);
          if (i >= R0 + 3 && i < R0 + 8) rdA(cur ^ 1, (q + 1) % 6, i - R0 - 3);
          if (mode == 0 && i >= D0 && i < D0 + 6 && !((i - D0) & 1)) request(q, (i - D0) >> 1);
          if (mode == 2 && q >= 3 && i >= D0 && i < D0 + 6) request(2 * (q - 3) + (i - D0) / 3, (i - D0) % 3);
          SBAR();
        }
        if (mode == 0) WAITV(12);
        if (mode == 1) { if (q == 0) WAITV(9); else if (q == 1) WAITV(6); else if (q == 2) WAITV(3); else WAITV(0); }
        if (mode == 2) {
          if (q == 0 && wave < 6) WAITV(0);
          if (q == 1 && wave >= 6) WAITV(0);
          if (q == 2 && wave == 7) poll_flags(FL + (size_t)blockIdx.x * 32, 4, hq, TAG, ln);
          if (q == 4) WAITV(9);
          if (q == 5) WAITV(12);
        }
        BAR();
        if (mode == 2 && q == 0 && wave == 0 && ln < 4 && (int)ln != hq)
          __hip_atomic_store(FL + (size_t)(qbase + 8 * ln) * 32 + hq, TAG, __ATOMIC_RELAXED, __HIP_MEMORY_SCOPE_AGENT);
      };
      auto head6 = [&](auto MODE) {
        phase(std::integral_constant<int, 0>{}, MODE);
        phase(std::integral_constant<int, 1>{}, MODE);
        phase(std::integral_constant<int, 2>{}, MODE);
        phase(std::integral_constant<int, 3>{}, MODE);
        phase(std::integral_constant<int, 4>{}, MODE);
        phase(std::integral_constant<int, 5>{}, MODE);
      };
      sethead((hq + 1) & 3);
      head6(std::integral_constant<int, 2>{});
#pragma unroll 1
      for (int i = 2; i < 4; ++i) {
        sethead((hq + i) & 3);
        head6(std::integral_constant<int, 0>{});
      }
      head6(std::integral_constant<int, 1>{});
    };
    if (wave < 4) tbody(std::integral_constant<int, 0>{}); else tbody(std::integral_constant<int, 1>{});
    {
      const unsigned yw = L0 + (48 * rq + l15) * 336 + (80 * ch + 4 * g) * 2;
#pragma unroll
      for (int rb = 0; rb < 3; ++rb)
#pragma unroll
        for (int cb = 0; cb < 5; ++cb) {
          u32x2 w = {cvtpk(acc[rb][cb][0], acc[rb][cb][1]), cvtpk(acc[rb][cb][2], acc[rb][cb][3])};
          *(LAS u32x2*)(size_t)(yw + rb * 16 * 336 + cb * 32) = w;
        }
    }
    BAR();
    {
      const __amdgpu_buffer_rsrc_t rsP = __builtin_amdgcn_make_buffer_rsrc(ws + WS_P, (short)0, 2 * 8 * 4 * 8 * 30720, 0x00020000);
#pragma unroll
      for (int i = 0; i < 8; ++i) {
        const int j = wave + 8 * i;
        if (j < 60) {
          const int n = 64 * j + ln, row = n / 20, pc = n % 20, un = j >= 30 ? 1 : 0;
          const u32x4 v = *(const LAS u32x4*)(size_t)(L0 + row * 336 + pc * 16);
          __builtin_amdgcn_raw_buffer_store_b128(v, rsP, (((un * 8 + b0) * 4 + hq) * 8 + xj) * 30720 + (n - 1920 * un) * 16, 0, 16);
        }
      }
      WAITV(0);
      BAR();
      if (wave == 0) {
        if (ln < 8) __hip_atomic_store(FL + (size_t)((int)ln + 8 * xi) * 32 + 8 + xj, TAG, __ATOMIC_RELAXED, __HIP_MEMORY_SCOPE_AGENT);
        poll_flags(FL + (size_t)blockIdx.x * 32 + 8, 8, -1, TAG, ln);
      }
      BAR();
      const int tz = wave * 64 + (int)ln;
      if (tz < 480) {
        const int u = tz / 240, e = tz % 240;
        const int pp = (((u * 8 + b0) * 4 + hq) * 8) * 30720 + 12 * xj * 320 + e * 16;
        u32x4 v[8];
#pragma unroll
        for (int j = 0; j < 8; ++j) v[j] = __builtin_amdgcn_raw_buffer_load_b128(rsP, pp + j * 30720, 0, 16);
        float a[8];
#pragma unroll
        for (int k = 0; k < 4; ++k) {
          float lo = 0.f, hi2 = 0.f;
#pragma unroll
          for (int j = 0; j < 8; ++j) { lo += __uint_as_float(v[j][k] << 16); hi2 += __uint_as_float(v[j][k] & 0xffff0000u); }
          a[2 * k] = lo; a[2 * k + 1] = hi2;
        }
        u32x4 w = {cvtpk(a[0], a[1]), cvtpk(a[2], a[3]), cvtpk(a[4], a[5]), cvtpk(a[6], a[7])};
        const int row = e / 20, c8 = e % 20;
        *(u32x4*)(out + (size_t)((b0 + 8 * u) * SQ + 12 * xj + row) * DM + hq * 160 + c8 * 8) = w;
      }
    }
  }
}

extern "C" void kernel_launch(void* const* d_in, const int* in_sizes, int n_in, void* d_out, int out_size, void* d_ws,
                              size_t ws_size, hipStream_t stream) {
  const u16* x = (const u16*)d_in[0];
  const u16* kc = (const u16*)d_in[1];
  const u16* vc = (const u16*)d_in[2];
  const u16* wq = (const u16*)d_in[3];
  const u16* wo = (const u16*)d_in[4];
  (void)hipFuncSetAttribute((const void*)attn_kernel, hipFuncAttributeMaxDynamicSharedMemorySize, LDS_A);
  attn_kernel<<<256, 512, LDS_A, stream>>>(x, kc, vc, wq, wo, (u16*)d_out, (char*)d_ws);
}
```
